```python
import torch

def _softmax(x, axis=-1):
    return torch.softmax(x, dim=axis)
def _gelu(x):
    return torch.nn.functional.gelu(x, approximate="tanh")
def _silu(x):
    return torch.nn.functional.silu(x)
def _sigmoid(x):
    return torch.sigmoid(x)
def _layernorm(x, axis=-1, eps=1e-5):
    m = x.mean(dim=axis, keepdim=True)
    v = x.var(dim=axis, keepdim=True, unbiased=False)
    return (x - m) / torch.sqrt(v + eps)
def _scatter_add(tgt, idx, src, axis=0):
    return tgt.clone().index_add_(axis, idx, src)
def _segment_sum(src, seg, n):
    out = torch.zeros((n,) + src.shape[1:], dtype=src.dtype)
    return out.index_add_(0, seg, src)
def _pad(x, pw):
    flat = tuple(v for pair in reversed(pw) for v in pair)
    return torch.nn.functional.pad(x, flat)

kernel_name = 'concat_heads__gqa_block_cached__gqa_block_cached__near_ridge'

def setup_inputs():

    _w = (lambda a: (lambda t: t.to(torch.bfloat16) if t.is_floating_point() else t)(torch.as_tensor(a)))
    def _A_setup():
        B, Sq, D = 16, 128, 640
        Hq, Hkv, Sk, Dh = 32, 32, 512, 192
        assert Hq % Hkv == 0, (Hq, Hkv)
        return {
            "x": _w((lambda *s: torch.randn(*s, dtype=torch.bfloat16) * 0.8)(B, Sq, D)),
            "k_cache": _w((lambda *s: torch.randn(*s, dtype=torch.bfloat16) * 0.8)(B, Hkv, Sk, Dh)),
            "v_cache": _w((lambda *s: torch.randn(*s, dtype=torch.bfloat16) * 0.05)(B, Hkv, Sk, Dh)),
            "wq": _w((lambda *s: torch.randn(*s, dtype=torch.bfloat16) * 0.8)(D, Hq * Dh) * (D ** -0.5)),
            "wo": _w((lambda *s: torch.randn(*s, dtype=torch.bfloat16) * 0.05)(Hq * Dh, D) * ((Hq * Dh) ** -0.5)),
        }
    _A_in = _A_setup()

    def _B_setup():
        B, Sq, D = 16, 128, 640
        Hq, Hkv, Sk, Dh = 32, 32, 512, 192
        assert Hq % Hkv == 0, (Hq, Hkv)
        return {
            "x": _w((lambda *s: torch.randn(*s, dtype=torch.bfloat16) * 0.8)(B, Sq, D)),
            "k_cache": _w((lambda *s: torch.randn(*s, dtype=torch.bfloat16) * 0.8)(B, Hkv, Sk, Dh)),
            "v_cache": _w((lambda *s: torch.randn(*s, dtype=torch.bfloat16) * 0.05)(B, Hkv, Sk, Dh)),
            "wq": _w((lambda *s: torch.randn(*s, dtype=torch.bfloat16) * 0.8)(D, Hq * Dh) * (D ** -0.5)),
            "wo": _w((lambda *s: torch.randn(*s, dtype=torch.bfloat16) * 0.05)(Hq * Dh, D) * ((Hq * Dh) ** -0.5)),
        }
    _B_in = _B_setup()

    return {
        "A_x": _A_in["x"],
        "A_k_cache": _A_in["k_cache"],
        "A_v_cache": _A_in["v_cache"],
        "A_wq": _A_in["wq"],
        "A_wo": _A_in["wo"],
        "B_x": _B_in["x"],
        "B_k_cache": _B_in["k_cache"],
        "B_v_cache": _B_in["v_cache"],
        "B_wq": _B_in["wq"],
        "B_wo": _B_in["wo"],
    }

def reference(A_x, A_k_cache, A_v_cache, A_wq, A_wo, B_x, B_k_cache, B_v_cache, B_wq, B_wo):
    def _A_ref(x, k_cache, v_cache, wq, wo):
        B, Sq, D = x.shape
        Hq, Dh = 32, 192
        Hkv = k_cache.shape[1]
        rep = Hq // Hkv
        q = (lambda x, *d: x.permute(*d))((x @ wq).reshape(B, Sq, Hq, Dh), 0, 2, 1, 3)
        k = (lambda x, n, axis: torch.repeat_interleave(x, n, dim=axis))(k_cache, rep, axis=1)
        v = (lambda x, n, axis: torch.repeat_interleave(x, n, dim=axis))(v_cache, rep, axis=1)
        scores = torch.einsum("bhqd,bhkd->bhqk", q, k) * (Dh ** -0.5)
        p = _softmax(scores, axis=-1)
        o = torch.einsum("bhqk,bhkd->bhqd", p, v)
        return (lambda x, *d: x.permute(*d))(o, 0, 2, 1, 3).reshape(B, Sq, Hq * Dh) @ wo

    def _B_ref(x, k_cache, v_cache, wq, wo):
        B, Sq, D = x.shape
        Hq, Dh = 32, 192
        Hkv = k_cache.shape[1]
        rep = Hq // Hkv
        q = (lambda x, *d: x.permute(*d))((x @ wq).reshape(B, Sq, Hq, Dh), 0, 2, 1, 3)
        k = (lambda x, n, axis: torch.repeat_interleave(x, n, dim=axis))(k_cache, rep, axis=1)
        v = (lambda x, n, axis: torch.repeat_interleave(x, n, dim=axis))(v_cache, rep, axis=1)
        scores = torch.einsum("bhqd,bhkd->bhqk", q, k) * (Dh ** -0.5)
        p = _softmax(scores, axis=-1)
        o = torch.einsum("bhqk,bhkd->bhqd", p, v)
        return (lambda x, *d: x.permute(*d))(o, 0, 2, 1, 3).reshape(B, Sq, Hq * Dh) @ wo

    a = _A_ref(A_x, A_k_cache, A_v_cache, A_wq, A_wo)
    b = _B_ref(B_x, B_k_cache, B_v_cache, B_wq, B_wo)
    return torch.concatenate([a, b], axis=-1)
```

```cpp
#include <hip/hip_runtime.h>
#include <hip/hip_bf16.h>
#include <utility>

typedef float    f32x16 __attribute__((ext_vector_type(16)));
typedef float    f32x2  __attribute__((ext_vector_type(2)));
typedef __bf16   bf16x8 __attribute__((ext_vector_type(8)));
typedef __bf16   bf16x4 __attribute__((ext_vector_type(4)));
typedef __bf16   bf16x2 __attribute__((ext_vector_type(2)));
typedef unsigned u32x4  __attribute__((ext_vector_type(4)));
typedef unsigned u32x2  __attribute__((ext_vector_type(2)));
#define LAS __attribute__((address_space(3)))
#define DI __device__ __forceinline__
#define MFMA(a, b, c) __builtin_amdgcn_mfma_f32_32x32x16_bf16((a), (b), (c), 0, 0, 0)

constexpr int DM = 640, SQ = 128, NH = 32, SK = 512, DH = 192, NB = 16;
constexpr unsigned TILE = 24576, NSLOT = 6, XOFF = NSLOT * TILE, LDS_BYTES = 163840;
constexpr unsigned KV_HEAD = SK * DH * 2;
constexpr unsigned WQ_RS = NH * DH * 2;
constexpr unsigned WO_RS = DM * 2;
constexpr unsigned X_RS = DM * 2;
constexpr int AHEAD = 4;

DI unsigned cvtpk(float lo, float hi) { f32x2 v = {lo, hi}; bf16x2 b = __builtin_convertvector(v, bf16x2); return __builtin_bit_cast(unsigned, b); }
DI void dma16(unsigned lds_addr, unsigned voff, __amdgpu_buffer_rsrc_t srd, unsigned soff) {
  asm volatile("s_mov_b32 m0, %0\n\ts_nop 0\n\tbuffer_load_dwordx4 %1, %2, %3 offen lds" :: "s"(lds_addr), "v"(voff), "s"(srd), "s"(soff) : "memory");
}
DI bf16x8 lds_b128(unsigned addr) { return *(const LAS bf16x8*)(size_t)addr; }
DI bf16x8 lds_tr2(unsigned a0, unsigned a1) {
  bf16x4 lo = __builtin_amdgcn_ds_read_tr16_b64_v4bf16((LAS bf16x4*)(size_t)a0);
  bf16x4 hi = __builtin_amdgcn_ds_read_tr16_b64_v4bf16((LAS bf16x4*)(size_t)a1);
  return __builtin_shufflevector(lo, hi, 0, 1, 2, 3, 4, 5, 6, 7);
}
DI bf16x8 pack8(const f32x16& x, int s, float sc) {
  u32x4 w;
  #pragma unroll
  for (int e = 0; e < 4; ++e) w[e] = cvtpk(x[8 * s + 2 * e] * sc, x[8 * s + 2 * e + 1] * sc);
  return __builtin_bit_cast(bf16x8, w);
}
DI bf16x8 pack8n(const f32x16& x, int s) {
  u32x4 w;
  #pragma unroll
  for (int e = 0; e < 4; ++e) w[e] = cvtpk(x[8 * s + 2 * e], x[8 * s + 2 * e + 1]);
  return __builtin_bit_cast(bf16x8, w);
}

struct Ctx {
  __amdgpu_buffer_rsrc_t sX, sK, sV, sWq, sWo;
  unsigned vK[2], vV, vWq, vWo, vX[2];
  unsigned ldsb;
  unsigned wid;
  unsigned b;
};

template <int TAU> DI void issue_tile(const Ctx& c, unsigned h) {
  constexpr unsigned slot = (TAU % NSLOT) * TILE;
  const unsigned dst = c.ldsb + slot + c.wid * 6144u;
  if constexpr (TAU < 10) {
    const unsigned base = (64u * TAU + 16u * c.wid) * WQ_RS + h * 384u;
    #pragma unroll
    for (int j = 0; j < 6; ++j) dma16(dst + 1024u * j, c.vWq, c.sWq, base + (j / 3) * 8u * WQ_RS + (j % 3) * 128u);
  } else if constexpr (TAU < 26) {
    constexpr int t = (TAU - 10) >> 1;
    const unsigned base = (c.b * NH + h) * KV_HEAD + t * TILE + c.wid * 6144u;
    if constexpr (((TAU - 10) & 1) == 0) {
      #pragma unroll
      for (int j = 0; j < 6; ++j) dma16(dst + 1024u * j, c.vK[j / 3], c.sK, base + (j / 3) * 3072u + (j % 3) * 128u);
    } else {
      #pragma unroll
      for (int j = 0; j < 6; ++j) dma16(dst + 1024u * j, c.vV, c.sV, base + (j / 3) * 3072u + (j % 3) * 128u);
    }
  } else {
    constexpr int jn = TAU - 26;
    const unsigned base = (h * DH + 48u * c.wid) * WO_RS + jn * 128u;
    #pragma unroll
    for (int j = 0; j < 6; ++j) dma16(dst + 1024u * j, c.vWo, c.sWo, base + j * 8u * WO_RS);
  }
}
DI void issue_x(const Ctx& c, unsigned t) {
  const unsigned dst = c.ldsb + XOFF + c.wid * 4096u;
  const unsigned base = (c.b * SQ + 32u * c.wid) * X_RS + t * 128u;
  #pragma unroll
  for (int j = 0; j < 4; ++j) dma16(dst + 1024u * j, c.vX[j & 1], c.sX, base + j * 8u * X_RS);
}
template <int TAU> DI void issue_ahead(const Ctx& c, unsigned h, unsigned hn) {
  if constexpr (TAU + AHEAD < 36) issue_tile<TAU + AHEAD>(c, h); else issue_tile<TAU + AHEAD - 36>(c, hn);
}
#define WAITV(n) asm volatile("s_waitcnt vmcnt(" #n ")" ::: "memory")
#define BAR() do { asm volatile("s_waitcnt lgkmcnt(0)\n\ts_barrier" ::: "memory"); } while (0)

template <int... Is> struct iseq {};

__global__ __launch_bounds__(256, 1)
void fused_kernel(const void* xA, const void* kA, const void* vA, const void* wqA, const void* woA,
                  const void* xB, const void* kB, const void* vB, const void* wqB, const void* woB, u32x4* __restrict__ part) {
  extern __shared__ __attribute__((aligned(16))) char lds[];
  const unsigned tid = threadIdx.x, l = tid & 63u, r = l & 31u, hh = l >> 5;
  const unsigned blk = blockIdx.x, hg = blk & 31u, bb = blk >> 5, br = bb >> 4;
  Ctx c;
  c.b = bb & 15u;
  c.wid = __builtin_amdgcn_readfirstlane(tid >> 6);
  c.ldsb = (unsigned)(size_t)lds;
  c.sX  = __builtin_amdgcn_make_buffer_rsrc((void*)(br ? xB : xA), (short)0, NB * SQ * DM * 2, 0x00020000);
  c.sK  = __builtin_amdgcn_make_buffer_rsrc((void*)(br ? kB : kA), (short)0, NB * NH * KV_HEAD, 0x00020000);
  c.sV  = __builtin_amdgcn_make_buffer_rsrc((void*)(br ? vB : vA), (short)0, NB * NH * KV_HEAD, 0x00020000);
  c.sWq = __builtin_amdgcn_make_buffer_rsrc((void*)(br ? wqB : wqA), (short)0, DM * WQ_RS, 0x00020000);
  c.sWo = __builtin_amdgcn_make_buffer_rsrc((void*)(br ? woB : woA), (short)0, NH * DH * WO_RS, 0x00020000);
  {
    const unsigned lr = l >> 3, lp = l & 7u;
    const unsigned xv = 16u * (lp ^ (4u * ((l >> 4) & 1u)));
    c.vK[0] = lr * 384u + 16u * (lp ^ (l >> 4)); c.vK[1] = lr * 384u + 16u * (lp ^ (4u + (l >> 4)));
    c.vX[0] = lr * X_RS + 16u * (lp ^ (l >> 4)); c.vX[1] = lr * X_RS + 16u * (lp ^ (4u + (l >> 4)));
    c.vV = lr * 384u + xv; c.vWq = lr * WQ_RS + xv; c.vWo = lr * WO_RS + xv;
  }
  const unsigned q4 = (l & 15u) >> 2, p = l & 3u, g1 = (l >> 4) & 1u;
  const unsigned ps = ((p & 1u) << 1) | (p >> 1);
  unsigned kb_[4], xb_[4], vb_[2], wb_[2];
  #pragma unroll
  for (int j = 0; j < 4; ++j) {
    kb_[j] = c.ldsb + (r >> 3) * 3072u + (r & 7u) * 128u + 16u * ((2u * j + hh) ^ ((r >> 1) & 7u));
    xb_[j] = c.ldsb + XOFF + (32u * c.wid + r) * 128u + 16u * ((2u * j + hh) ^ ((r >> 1) & 7u));
  }
  #pragma unroll
  for (int j = 0; j < 2; ++j) {
    vb_[j] = c.ldsb + (4u * hh + q4) * 128u + 16u * (((4u * j) ^ (4u * (q4 >> 1))) + 2u * g1 + (p >> 1)) + 8u * (p & 1u);
    wb_[j] = c.ldsb + hh * 3072u + q4 * 128u + 16u * (((4u * j) ^ (4u * (q4 >> 1))) + 2u * g1 + (ps >> 1)) + 8u * (ps & 1u);
  }
  const float QS = 0.07216878364870322f * 1.4426950408889634f;

  u32x4* dst = part + ((size_t)(bb * 32u + hg) * 4u + c.wid) * (20u * 128u) + l;

  const unsigned h0 = hg;
  issue_x(c, 0);
  issue_tile<0>(c, h0); issue_tile<1>(c, h0); issue_tile<2>(c, h0); issue_tile<3>(c, h0);

  {
    const unsigned h = h0, hn = h0;
    f32x16 acc[6];
    #pragma unroll
    for (int d = 0; d < 6; ++d) acc[d] = f32x16{};
    auto qp_step = [&](auto TAU_) {
      constexpr int TAU = decltype(TAU_)::value;
      constexpr unsigned slot = (TAU % NSLOT) * TILE;
      if constexpr (TAU == 0) WAITV(18); else WAITV(6);
      BAR();
      bf16x8 xf[4];
      #pragma unroll
      for (int s = 0; s < 4; ++s) xf[s] = lds_b128(xb_[s]);
      asm volatile("s_waitcnt lgkmcnt(0)" : "+v"(xf[0]), "+v"(xf[1]), "+v"(xf[2]), "+v"(xf[3]) :: "memory");
      if constexpr (TAU < 9) issue_x(c, TAU + 1);
      issue_ahead<TAU>(c, h, hn);
      #pragma unroll
      for (int s = 0; s < 4; ++s)
        #pragma unroll
        for (int d = 0; d < 6; ++d) {
          const unsigned a = wb_[d & 1] + slot + 2u * s * 3072u + (d >> 1) * 1024u;
          acc[d] = MFMA(lds_tr2(a, a + 512u), xf[s], acc[d]);
        }
    };
    qp_step(std::integral_constant<int, 0>{}); qp_step(std::integral_constant<int, 1>{}); qp_step(std::integral_constant<int, 2>{});
    qp_step(std::integral_constant<int, 3>{}); qp_step(std::integral_constant<int, 4>{}); qp_step(std::integral_constant<int, 5>{});
    qp_step(std::integral_constant<int, 6>{}); qp_step(std::integral_constant<int, 7>{}); qp_step(std::integral_constant<int, 8>{});
    qp_step(std::integral_constant<int, 9>{});
    bf16x8 qf[12];
    #pragma unroll
    for (int d = 0; d < 6; ++d) { qf[2 * d] = pack8(acc[d], 0, QS); qf[2 * d + 1] = pack8(acc[d], 1, QS); }
    #pragma unroll
    for (int d = 0; d < 6; ++d) acc[d] = f32x16{};
    float m = -__builtin_inff(), lsum = 0.f;
    auto at_step = [&](auto T_) {
      constexpr int T = decltype(T_)::value;
      constexpr int TK = 10 + 2 * T, TV = TK + 1;
      constexpr unsigned slotK = (TK % NSLOT) * TILE, slotV = (TV % NSLOT) * TILE;
      WAITV(18); BAR();
      issue_ahead<TK>(c, h, hn);
      f32x16 s0 = {}, s1 = {};
      #pragma unroll
      for (int s = 0; s < 12; ++s) {
        s0 = MFMA(lds_b128(kb_[s & 3] + slotK + (s >> 2) * 1024u), qf[s], s0);
        s1 = MFMA(lds_b128(kb_[s & 3] + slotK + (s >> 2) * 1024u + 12288u), qf[s], s1);
      }
      float mx = s0[0];
      #pragma unroll
      for (int i = 1; i < 16; ++i) mx = fmaxf(mx, s0[i]);
      #pragma unroll
      for (int i = 0; i < 16; ++i) mx = fmaxf(mx, s1[i]);
      { auto rr = __builtin_amdgcn_permlane32_swap(__float_as_uint(mx), __float_as_uint(mx), false, false);
        mx = fmaxf(__uint_as_float(rr[0]), __uint_as_float(rr[1])); }
      const float mn = fmaxf(m, mx), alpha = __builtin_amdgcn_exp2f(m - mn);
      m = mn;
      float ps_ = 0.f;
      #pragma unroll
      for (int i = 0; i < 16; ++i) { s0[i] = __builtin_amdgcn_exp2f(s0[i] - mn); ps_ += s0[i]; }
      #pragma unroll
      for (int i = 0; i < 16; ++i) { s1[i] = __builtin_amdgcn_exp2f(s1[i] - mn); ps_ += s1[i]; }
      lsum = lsum * alpha + ps_;
      #pragma unroll
      for (int d = 0; d < 6; ++d) acc[d] *= alpha;
      bf16x8 pf[4] = {pack8n(s0, 0), pack8n(s0, 1), pack8n(s1, 0), pack8n(s1, 1)};
      WAITV(18); BAR();
      issue_ahead<TV>(c, h, hn);
      #pragma unroll
      for (int ks = 0; ks < 4; ++ks)
        #pragma unroll
        for (int d = 0; d < 6; ++d) {
          const unsigned a = vb_[d & 1] + slotV + (2u * ks) * 3072u + (d >> 1) * 1024u;
          acc[d] = MFMA(lds_tr2(a, a + 3072u), pf[ks], acc[d]);
        }
    };
    at_step(std::integral_constant<int, 0>{}); at_step(std::integral_constant<int, 1>{}); at_step(std::integral_constant<int, 2>{});
    at_step(std::integral_constant<int, 3>{}); at_step(std::integral_constant<int, 4>{}); at_step(std::integral_constant<int, 5>{});
    at_step(std::integral_constant<int, 6>{}); at_step(std::integral_constant<int, 7>{});
    { auto rr = __builtin_amdgcn_permlane32_swap(__float_as_uint(lsum), __float_as_uint(lsum), false, false);
      lsum = __uint_as_float(rr[0]) + __uint_as_float(rr[1]); }
    const float rl = 1.0f / lsum;
    bf16x8 of[12];
    #pragma unroll
    for (int d = 0; d < 6; ++d) { of[2 * d] = pack8(acc[d], 0, rl); of[2 * d + 1] = pack8(acc[d], 1, rl); }
    auto op_step = [&](auto J_) {
      constexpr int J = decltype(J_)::value;
      constexpr int TAU = 26 + J;
      constexpr unsigned slot = (TAU % NSLOT) * TILE;
      WAITV(0); BAR();
      issue_ahead<TAU>(c, h, hn);
      #pragma unroll
      for (int nb = 0; nb < 2; ++nb) {
        f32x16 w = {};
        #pragma unroll
        for (int kk = 0; kk < 12; ++kk) {
          const unsigned a = vb_[nb] + slot + (2u * kk) * 1024u;
          w = MFMA(lds_tr2(a, a + 1024u), of[kk], w);
        }
        dst[(2 * J + nb) * 128] = __builtin_bit_cast(u32x4, pack8n(w, 0));
        dst[(2 * J + nb) * 128 + 64] = __builtin_bit_cast(u32x4, pack8n(w, 1));
      }
    };
    op_step(std::integral_constant<int, 0>{}); op_step(std::integral_constant<int, 1>{}); op_step(std::integral_constant<int, 2>{});
    op_step(std::integral_constant<int, 3>{}); op_step(std::integral_constant<int, 4>{}); op_step(std::integral_constant<int, 5>{});
    op_step(std::integral_constant<int, 6>{}); op_step(std::integral_constant<int, 7>{}); op_step(std::integral_constant<int, 8>{});
    op_step(std::integral_constant<int, 9>{});
  }
  WAITV(0);
}

__global__ __launch_bounds__(256)
void reduce_kernel(const u32x4* __restrict__ part, unsigned short* __restrict__ out) {
  const unsigned idx = blockIdx.x * 256u + threadIdx.x;
  const unsigned bb = idx / 10240u, rem = idx % 10240u;
  const unsigned w = rem / 2560u, jn = (rem % 2560u) >> 7, half = (rem >> 6) & 1u, l = rem & 63u;
  float s[8] = {0, 0, 0, 0, 0, 0, 0, 0};
  #pragma unroll
  for (int g = 0; g < 32; ++g) {
    const u32x4 v = part[(size_t)(bb * 32u + g) * 10240u + rem];
    #pragma unroll
    for (int e = 0; e < 4; ++e) { s[2 * e] += __uint_as_float(v[e] << 16); s[2 * e + 1] += __uint_as_float(v[e] & 0xffff0000u); }
  }
  const unsigned br = bb >> 4, b = bb & 15u, q = 32u * w + (l & 31u), hh = l >> 5;
  unsigned short* o = out + ((size_t)(b * SQ + q) * (2 * DM) + br * DM + 32u * jn + 16u * half + 4u * hh);
  *(u32x2*)o = u32x2{cvtpk(s[0], s[1]), cvtpk(s[2], s[3])};
  *(u32x2*)(o + 8) = u32x2{cvtpk(s[4], s[5]), cvtpk(s[6], s[7])};
}

extern "C" void kernel_launch(void* const* d_in, const int* in_sizes, int n_in, void* d_out, int out_size, void* d_ws, size_t ws_size, hipStream_t stream) {
  hipFuncSetAttribute((const void*)fused_kernel, hipFuncAttributeMaxDynamicSharedMemorySize, LDS_BYTES);
  fused_kernel<<<1024, 256, LDS_BYTES, stream>>>(d_in[0], d_in[1], d_in[2], d_in[3], d_in[4], d_in[5], d_in[6], d_in[7], d_in[8], d_in[9], (u32x4*)d_ws);
  reduce_kernel<<<1280, 256, 0, stream>>>((const u32x4*)d_ws, (unsigned short*)d_out);
}
```

```cpp
#include <hip/hip_runtime.h>
#include <hip/hip_bf16.h>
#include <utility>
typedef float    f32x4 __attribute__((ext_vector_type(4)));
typedef float    f32x2  __attribute__((ext_vector_type(2)));
typedef __bf16   bf16x8 __attribute__((ext_vector_type(8)));
typedef __bf16   bf16x4 __attribute__((ext_vector_type(4)));
typedef __bf16   bf16x2 __attribute__((ext_vector_type(2)));
typedef unsigned u32x4  __attribute__((ext_vector_type(4)));
typedef unsigned u32x2  __attribute__((ext_vector_type(2)));
#define LAS __attribute__((address_space(3)))
#define DI __device__ __forceinline__
#define MFMA(a, b, c) __builtin_amdgcn_mfma_f32_16x16x32_bf16((a), (b), (c), 0, 0, 0)
constexpr int DM = 640, SQ = 128, NH = 32, SK = 512, DH = 192, NB = 16;
constexpr unsigned TILE = 24576, NSLOT = 6, XOFF = NSLOT * TILE, LDS_BYTES = 163840;
constexpr unsigned KV_HEAD = SK * DH * 2, WQ_RS = NH * DH * 2, WO_RS = DM * 2, X_RS = DM * 2;
constexpr int AHEAD = 4;
DI unsigned cvtpk(float lo, float hi) { f32x2 v = {lo, hi}; bf16x2 b = __builtin_convertvector(v, bf16x2); return __builtin_bit_cast(unsigned, b); }
DI void dma16(unsigned lds_addr, unsigned voff, __amdgpu_buffer_rsrc_t srd, unsigned soff) {
  asm volatile("s_mov_b32 m0, %0\n\ts_nop 0\n\tbuffer_load_dwordx4 %1, %2, %3 offen lds" :: "s"(lds_addr), "v"(voff), "s"(srd), "s"(soff) : "memory");
}
DI void dma16nt(unsigned lds_addr, unsigned voff, __amdgpu_buffer_rsrc_t srd, unsigned soff) {
  asm volatile("s_mov_b32 m0, %0\n\ts_nop 0\n\tbuffer_load_dwordx4 %1, %2, %3 offen nt lds" :: "s"(lds_addr), "v"(voff), "s"(srd), "s"(soff) : "memory");
}
DI bf16x8 lds_b128(unsigned addr) { return *(const LAS bf16x8*)(size_t)addr; }
DI bf16x8 lds_tr2(unsigned a0, unsigned a1) {
  bf16x4 lo = __builtin_amdgcn_ds_read_tr16_b64_v4bf16((LAS bf16x4*)(size_t)a0);
  bf16x4 hi = __builtin_amdgcn_ds_read_tr16_b64_v4bf16((LAS bf16x4*)(size_t)a1);
  return __builtin_shufflevector(lo, hi, 0, 1, 2, 3, 4, 5, 6, 7);
}
DI u32x4 pack2(const f32x4& a, const f32x4& b, float sc) {
  return u32x4{cvtpk(a[0] * sc, a[1] * sc), cvtpk(a[2] * sc, a[3] * sc), cvtpk(b[0] * sc, b[1] * sc), cvtpk(b[2] * sc, b[3] * sc)};
}
struct Ctx { __amdgpu_buffer_rsrc_t sX, sK, sV, sWq, sWo; unsigned vK[2], vV, vWq, vWo, vX[2]; unsigned ldsb, wid, b; };
template <int TAU> DI void issue_tile(const Ctx& c, unsigned h) {
  constexpr unsigned slot = (TAU % NSLOT) * TILE;
  const unsigned dst = c.ldsb + slot + c.wid * 6144u;
  if constexpr (TAU < 10) {
    const unsigned base = (64u * TAU + 32u * (c.wid >> 1) + 4u * (c.wid & 1u)) * WQ_RS + h * 384u;
    #pragma unroll
    for (int j = 0; j < 6; ++j) dma16(dst + 1024u * j, c.vWq, c.sWq, base + (j / 3) * 16u * WQ_RS + (j % 3) * 128u);
  } else if constexpr (TAU < 26) {
    constexpr int t = (TAU - 10) >> 1;
    const unsigned base = (c.b * NH + h) * KV_HEAD + t * TILE + c.wid * 6144u;
    if constexpr (((TAU - 10) & 1) == 0) {
      #pragma unroll
      for (int j = 0; j < 6; ++j) dma16nt(dst + 1024u * j, c.vK[j / 3], c.sK, base + (j / 3) * 3072u + (j % 3) * 128u);
    } else {
      #pragma unroll
      for (int j = 0; j < 6; ++j) dma16nt(dst + 1024u * j, c.vV, c.sV, base + (j / 3) * 3072u + (j % 3) * 128u);
    }
  } else {
    constexpr int jn = TAU - 26;
    const unsigned base = (h * DH + 16u * c.wid) * WO_RS + jn * 128u;
    #pragma unroll
    for (int j = 0; j < 6; ++j) dma16(dst + 1024u * j, c.vWo, c.sWo, base + (j / 3) * 8u * WO_RS + (j % 3) * 64u * WO_RS);
  }
}
DI void issue_x(const Ctx& c, unsigned t) {
  const unsigned dst = c.ldsb + XOFF + c.wid * 4096u;
  const unsigned base = (c.b * SQ + 32u * c.wid) * X_RS + t * 128u;
  #pragma unroll
  for (int j = 0; j < 4; ++j) dma16(dst + 1024u * j, c.vX[j & 1], c.sX, base + j * 8u * X_RS);
}
template <int TAU> DI void issue_ahead(const Ctx& c, unsigned h, unsigned hn) {
  if constexpr (TAU + AHEAD < 36) issue_tile<TAU + AHEAD>(c, h); else issue_tile<TAU + AHEAD - 36>(c, hn);
}
#define WAITV(n) asm volatile("s_waitcnt vmcnt(" #n ")" ::: "memory")
#define BAR() do { asm volatile("s_waitcnt lgkmcnt(0)\n\ts_barrier" ::: "memory"); } while (0)
DI float xred_max(float v) { v = fmaxf(v, __shfl_xor(v, 16)); return fmaxf(v, __shfl_xor(v, 32)); }
DI float xred_sum(float v) { v += __shfl_xor(v, 16); return v + __shfl_xor(v, 32); }

__global__ __launch_bounds__(256, 1)
void fused_kernel(const void* xA, const void* kA, const void* vA, const void* wqA, const void* woA,
                  const void* xB, const void* kB, const void* vB, const void* wqB, const void* woB, u32x4* __restrict__ part) {
  extern __shared__ __attribute__((aligned(16))) char lds[];
  const unsigned tid = threadIdx.x, l = tid & 63u, i = l & 15u, g = l >> 4, q4 = i >> 2, p = i & 3u;
  const unsigned blk = blockIdx.x, hg = blk & 31u, bb = blk >> 5, br = bb >> 4;
  Ctx c;
  c.b = bb & 15u;
  c.wid = __builtin_amdgcn_readfirstlane(tid >> 6);
  c.ldsb = (unsigned)(size_t)lds;
  c.sX  = __builtin_amdgcn_make_buffer_rsrc((void*)(br ? xB : xA), (short)0, NB * SQ * DM * 2, 0x00020000);
  c.sK  = __builtin_amdgcn_make_buffer_rsrc((void*)(br ? kB : kA), (short)0, NB * NH * KV_HEAD, 0x00020000);
  c.sV  = __builtin_amdgcn_make_buffer_rsrc((void*)(br ? vB : vA), (short)0, NB * NH * KV_HEAD, 0x00020000);
  c.sWq = __builtin_amdgcn_make_buffer_rsrc((void*)(br ? wqB : wqA), (short)0, DM * WQ_RS, 0x00020000);
  c.sWo = __builtin_amdgcn_make_buffer_rsrc((void*)(br ? woB : woA), (short)0, NH * DH * WO_RS, 0x00020000);
  {
    const unsigned lr = l >> 3, lp = l & 7u;
    const unsigned xv = 16u * (lp ^ (2u * (lr >> 1)));
    c.vK[0] = lr * 384u + 16u * (lp ^ (lr >> 1)); c.vK[1] = lr * 384u + 16u * (lp ^ (4u + (lr >> 1)));
    c.vX[0] = lr * X_RS + 16u * (lp ^ (lr >> 1)); c.vX[1] = lr * X_RS + 16u * (lp ^ (4u + (lr >> 1)));
    c.vV = lr * 384u + xv; c.vWq = (8u * (lr >> 2) + (lr & 3u)) * WQ_RS + xv; c.vWo = lr * WO_RS + xv;
  }
  const unsigned F = 2u * (g & 1u) + (q4 >> 1);
  unsigned vb_[4], kb_[2], xb_[2];
  #pragma unroll
  for (int k = 0; k < 4; ++k) vb_[k] = c.ldsb + (g >> 1) * 3072u + (4u * (g & 1u) + q4) * 128u + 32u * (k ^ F) + 16u * (p >> 1) + 8u * (p & 1u);
  #pragma unroll
  for (int k = 0; k < 2; ++k) {
    kb_[k] = c.ldsb + (i >> 3) * 3072u + (i & 7u) * 128u + 16u * ((4u * k + g) ^ (i >> 1));
    xb_[k] = c.ldsb + XOFF + (32u * c.wid + i) * 128u + 16u * ((4u * k + g) ^ (i >> 1));
  }
  const float QS = 0.07216878364870322f * 1.4426950408889634f;
  u32x4* dst = part + ((size_t)(bb * 32u + hg) * 4u + c.wid) * (40u * 64u) + l;
  const unsigned h = hg, hn = hg;
  issue_x(c, 0);
  issue_tile<0>(c, h); issue_tile<1>(c, h); issue_tile<2>(c, h); issue_tile<3>(c, h);
  f32x4 acc[12][2];
  #pragma unroll
  for (int d = 0; d < 12; ++d) { acc[d][0] = f32x4{}; acc[d][1] = f32x4{}; }
  auto qp_step = [&](auto TAU_) {
    constexpr int TAU = decltype(TAU_)::value;
    constexpr unsigned slot = (TAU % NSLOT) * TILE;
    WAITV(6); BAR();
    bf16x8 xf[2][2];
    #pragma unroll
    for (int s = 0; s < 2; ++s) { xf[s][0] = lds_b128(xb_[s]); xf[s][1] = lds_b128(xb_[s] + 2048u); }
    asm volatile("s_waitcnt lgkmcnt(0)" : "+v"(xf[0][0]), "+v"(xf[0][1]), "+v"(xf[1][0]), "+v"(xf[1][1]) :: "memory");
    if constexpr (TAU < 9) issue_x(c, TAU + 1);
    issue_ahead<TAU>(c, h, hn);
    #pragma unroll
    for (int s = 0; s < 2; ++s)
      #pragma unroll
      for (int d = 0; d < 12; ++d) {
        const unsigned a = vb_[d & 3] + slot + 4u * s * 3072u + (d >> 2) * 1024u;
        const bf16x8 f = lds_tr2(a, a + 6144u);
        acc[d][0] = MFMA(f, xf[s][0], acc[d][0]); acc[d][1] = MFMA(f, xf[s][1], acc[d][1]);
      }
  };
  qp_step(std::integral_constant<int, 0>{}); qp_step(std::integral_constant<int, 1>{}); qp_step(std::integral_constant<int, 2>{});
  qp_step(std::integral_constant<int, 3>{}); qp_step(std::integral_constant<int, 4>{}); qp_step(std::integral_constant<int, 5>{});
  qp_step(std::integral_constant<int, 6>{}); qp_step(std::integral_constant<int, 7>{}); qp_step(std::integral_constant<int, 8>{});
  qp_step(std::integral_constant<int, 9>{});
  bf16x8 qf[6][2];
  #pragma unroll
  for (int s = 0; s < 6; ++s)
    #pragma unroll
    for (int qb = 0; qb < 2; ++qb) {
      u32x4 r = pack2(acc[2 * s][qb], acc[2 * s + 1][qb], QS);
      #pragma unroll
      for (int e = 0; e < 2; ++e) {
        auto a = __builtin_amdgcn_permlane32_swap(r[e], r[e + 2], false, false);
        auto b = __builtin_amdgcn_permlane16_swap(a[0], a[1], false, false);
        r[e] = b[0]; r[e + 2] = b[1];
      }
      qf[s][qb] = __builtin_bit_cast(bf16x8, r);
    }
  #pragma unroll
  for (int d = 0; d < 12; ++d) { acc[d][0] = f32x4{}; acc[d][1] = f32x4{}; }
  float m[2] = {-__builtin_inff(), -__builtin_inff()}, lsum[2] = {0.f, 0.f};
  auto at_step = [&](auto T_) {
    constexpr int T = decltype(T_)::value;
    constexpr int TK = 10 + 2 * T, TV = TK + 1;
    constexpr unsigned slotK = (TK % NSLOT) * TILE, slotV = (TV % NSLOT) * TILE;
    WAITV(18); BAR();
    issue_ahead<TK>(c, h, hn);
    f32x4 S[4][2];
    #pragma unroll
    for (int kb = 0; kb < 4; ++kb) { S[kb][0] = f32x4{}; S[kb][1] = f32x4{};
      #pragma unroll
      for (int s = 0; s < 6; ++s) {
        const bf16x8 f = lds_b128(kb_[s & 1] + slotK + 2u * kb * 3072u + (s >> 1) * 1024u);
        S[kb][0] = MFMA(f, qf[s][0], S[kb][0]); S[kb][1] = MFMA(f, qf[s][1], S[kb][1]);
      } }
    bf16x8 pf[2][2];
    #pragma unroll
    for (int qb = 0; qb < 2; ++qb) {
      float mx = -__builtin_inff();
      #pragma unroll
      for (int kb = 0; kb < 4; ++kb) for (int e = 0; e < 4; ++e) mx = fmaxf(mx, S[kb][qb][e]);
      mx = xred_max(mx);
      const float mn = fmaxf(m[qb], mx), alpha = __builtin_amdgcn_exp2f(m[qb] - mn);
      m[qb] = mn;
      float ps_ = 0.f;
      #pragma unroll
      for (int kb = 0; kb < 4; ++kb) for (int e = 0; e < 4; ++e) { S[kb][qb][e] = __builtin_amdgcn_exp2f(S[kb][qb][e] - mn); ps_ += S[kb][qb][e]; }
      lsum[qb] = lsum[qb] * alpha + ps_;
      #pragma unroll
      for (int d = 0; d < 12; ++d) acc[d][qb] *= alpha;
      pf[0][qb] = __builtin_bit_cast(bf16x8, pack2(S[0][qb], S[1][qb], 1.f));
      pf[1][qb] = __builtin_bit_cast(bf16x8, pack2(S[2][qb], S[3][qb], 1.f));
    }
    WAITV(18); BAR();
    issue_ahead<TV>(c, h, hn);
    #pragma unroll
    for (int ks = 0; ks < 2; ++ks)
      #pragma unroll
      for (int d = 0; d < 12; ++d) {
        const unsigned a = vb_[d & 3] + slotV + 4u * ks * 3072u + (d >> 2) * 1024u;
        const bf16x8 f = lds_tr2(a, a + 6144u);
        acc[d][0] = MFMA(f, pf[ks][0], acc[d][0]); acc[d][1] = MFMA(f, pf[ks][1], acc[d][1]);
      }
  };
  at_step(std::integral_constant<int, 0>{}); at_step(std::integral_constant<int, 1>{}); at_step(std::integral_constant<int, 2>{});
  at_step(std::integral_constant<int, 3>{}); at_step(std::integral_constant<int, 4>{}); at_step(std::integral_constant<int, 5>{});
  at_step(std::integral_constant<int, 6>{}); at_step(std::integral_constant<int, 7>{});
  bf16x8 of[6][2];
  #pragma unroll
  for (int qb = 0; qb < 2; ++qb) {
    const float rl = 1.0f / xred_sum(lsum[qb]);
    #pragma unroll
    for (int s = 0; s < 6; ++s) of[s][qb] = __builtin_bit_cast(bf16x8, pack2(acc[2 * s][qb], acc[2 * s + 1][qb], rl));
  }
  auto op_step = [&](auto J_) {
    constexpr int J = decltype(J_)::value;
    constexpr int TAU = 26 + J;
    constexpr unsigned slot = (TAU % NSLOT) * TILE;
    WAITV(0); BAR();
    issue_ahead<TAU>(c, h, hn);
    f32x4 w[4][2];
    #pragma unroll
    for (int nb = 0; nb < 4; ++nb) { w[nb][0] = f32x4{}; w[nb][1] = f32x4{};
      #pragma unroll
      for (int s = 0; s < 6; ++s) {
        const unsigned a = vb_[nb] + slot + 4u * (s & 1) * 3072u + (s >> 1) * 1024u;
        const bf16x8 f = lds_tr2(a, a + 6144u);
        w[nb][0] = MFMA(f, of[s][0], w[nb][0]); w[nb][1] = MFMA(f, of[s][1], w[nb][1]);
      } }
    #pragma unroll
    for (int np = 0; np < 2; ++np)
      #pragma unroll
      for (int qb = 0; qb < 2; ++qb) dst[(4 * J + 2 * np + qb) * 64] = pack2(w[2 * np][qb], w[2 * np + 1][qb], 1.f);
  };
  op_step(std::integral_constant<int, 0>{}); op_step(std::integral_constant<int, 1>{}); op_step(std::integral_constant<int, 2>{});
  op_step(std::integral_constant<int, 3>{}); op_step(std::integral_constant<int, 4>{}); op_step(std::integral_constant<int, 5>{});
  op_step(std::integral_constant<int, 6>{}); op_step(std::integral_constant<int, 7>{}); op_step(std::integral_constant<int, 8>{});
  op_step(std::integral_constant<int, 9>{});
  WAITV(0);
}

template <int NP> __global__ __launch_bounds__(256)
void reduce_kernel(const u32x4* __restrict__ part, unsigned short* __restrict__ out) {
  const unsigned idx = blockIdx.x * 256u + threadIdx.x;
  const unsigned bb = idx / 10240u, rem = idx % 10240u;
  const unsigned w = rem / 2560u, u = (rem % 2560u) >> 6, l = rem & 63u;
  const unsigned J = u >> 2, np = (u >> 1) & 1u, qb = u & 1u;
  float s[8] = {0, 0, 0, 0, 0, 0, 0, 0};
  #pragma unroll
  for (int g = 0; g < NP; ++g) {
    const u32x4 v = part[(size_t)(bb * NP + g) * 10240u + rem];
    #pragma unroll
    for (int e = 0; e < 4; ++e) { s[2 * e] += __uint_as_float(v[e] << 16); s[2 * e + 1] += __uint_as_float(v[e] & 0xffff0000u); }
  }
  const unsigned br = bb >> 4, b = bb & 15u, q = 32u * w + 16u * qb + (l & 15u), g4 = l >> 4;
  unsigned short* o = out + ((size_t)(b * SQ + q) * (2 * DM) + br * DM + 64u * J + 32u * np + 4u * g4);
  *(u32x2*)o = u32x2{cvtpk(s[0], s[1]), cvtpk(s[2], s[3])};
  *(u32x2*)(o + 16) = u32x2{cvtpk(s[4], s[5]), cvtpk(s[6], s[7])};
}

extern "C" void kernel_launch(void* const* d_in, const int* in_sizes, int n_in, void* d_out, int out_size, void* d_ws, size_t ws_size, hipStream_t stream) {
  (void)hipFuncSetAttribute((const void*)fused_kernel, hipFuncAttributeMaxDynamicSharedMemorySize, LDS_BYTES);
  fused_kernel<<<1024, 256, LDS_BYTES, stream>>>(d_in[0], d_in[1], d_in[2], d_in[3], d_in[4], d_in[5], d_in[6], d_in[7], d_in[8], d_in[9], (u32x4*)d_ws);
  reduce_kernel<32><<<1280, 256, 0, stream>>>((const u32x4*)d_ws, (unsigned short*)d_out);
}
```

```cpp
#include <hip/hip_runtime.h>
#include <hip/hip_bf16.h>

typedef float    f32x2  __attribute__((ext_vector_type(2)));
typedef __bf16   bf16x2 __attribute__((ext_vector_type(2)));
typedef unsigned u32x4  __attribute__((ext_vector_type(4)));
typedef unsigned u32x2  __attribute__((ext_vector_type(2)));
constexpr unsigned LDS_BYTES = 163840;

__global__ __launch_bounds__(256, 1)
void fused_kernel(const void* xA, const void* kA, const void* vA, const void* wqA, const void* woA,
                  const void* xB, const void* kB, const void* vB, const void* wqB, const void* woB, char* part) {
  extern __shared__ __attribute__((aligned(16))) char lds[];
  constexpr unsigned LHX = 2;
  const unsigned blk = blockIdx.x, xc = blk & 7u, idx = blk >> 3;
  const unsigned hg = ((xc & ((8u >> LHX) - 1u)) << LHX) | (idx & ((1u << LHX) - 1u));
  const unsigned bb = ((xc >> (3u - LHX)) << (5u - LHX)) | (idx >> LHX), br = bb >> 4, b = bb & 15u;
  const unsigned w = __builtin_amdgcn_readfirstlane(threadIdx.x >> 6);
  const unsigned chunk = (idx * 4u + w) & 31u;
  asm volatile(R"(
v_mbcnt_lo_u32_b32 v1, -1, 0
s_getpc_b64 s[36:37]
Lpc0:
v_mbcnt_hi_u32_b32 v1, -1, v1
s_mov_b64 s[40:41], s[36:37]
s_and_b32 s37, s37, 0xffff
s_mov_b32 s38, Lend-Lpc0
s_mov_b32 s39, 0x20000
v_lshlrev_b32 v24, 7, v1
s_lshl_b32 s42, %0, 13
buffer_load_dword v216, v24, s[36:39], s42 offen
)" :: "s"(chunk) : "v1", "v24", "v216", "s36", "s37", "s38", "s39", "s40", "s41", "s42", "scc", "memory");
  const void* x = br ? xB : xA; const void* k = br ? kB : kA; const void* v = br ? vB : vA;
  const void* wq = br ? wqB : wqA; const void* wo = br ? woB : woA;
  char* p = part + (size_t)((bb * 8u + hg) * 4u + w) * 40960u;
  const unsigned ldsb = (unsigned)(size_t)lds;
  asm volatile(
R"(
.macro mv d,a,b
v_mfma_f32_16x16x32_bf16 v[\d:\d+3], v[\a:\a+3], v[\b:\b+3], v[\d:\d+3]
.endm
.macro mvz d,a,b
v_mfma_f32_16x16x32_bf16 v[\d:\d+3], v[\a:\a+3], v[\b:\b+3], 0
.endm
.macro mvc d,a,b,c
v_mfma_f32_16x16x32_bf16 v[\d:\d+3], v[\a:\a+3], v[\b:\b+3], v[\c:\c+3]
.endm
.macro ma d,a,b
v_mfma_f32_16x16x32_bf16 a[\d:\d+3], v[\a:\a+3], v[\b:\b+3], a[\d:\d+3]
.endm
.macro maz d,a,b
v_mfma_f32_16x16x32_bf16 a[\d:\d+3], v[\a:\a+3], v[\b:\b+3], 0
.endm
.macro tr d,b,o
ds_read_b64_tr_b16 v[\d:\d+1], v\b offset:\o
.endm
.macro rd d,b,o
ds_read_b128 v[\d:\d+3], v\b offset:\o
.endm
.macro wr d,o
ds_write_b128 v19, v[\d:\d+3] offset:\o
.endm
.macro dl vo,r,so,o
buffer_load_dwordx4 v\vo, s[\r:\r+3], s\so offen offset:\o lds
.endm
.macro dn vo,r,so,o
buffer_load_dwordx4 v\vo, s[\r:\r+3], s\so offen offset:\o nt lds
.endm
.macro st d,so,o
buffer_store_dwordx4 v[\d:\d+3], v7, s[12:15], s\so offen offset:\o sc1
.endm
.macro sta d,so,o
buffer_store_dwordx4 a[\d:\d+3], v7, s[12:15], s\so offen offset:\o sc1
.endm
.macro cv d,x,y
v_cvt_pk_bf16_f32 v\d, v\x, v\y
.endm
.macro ar d,x
v_accvgpr_read_b32 v\d, a\x
.endm
.macro aw d,x
v_accvgpr_write_b32 a\d, v\x
.endm
v_mbcnt_lo_u32_b32 v0, -1, 0
s_getpc_b64 s[40:41]
Lpc1:
v_mbcnt_hi_u32_b32 v0, -1, v0
s_waitcnt lgkmcnt(0)
s_mov_b64 s[84:85], %0
s_mov_b64 s[86:87], %1
s_mov_b64 s[88:89], %2
s_mov_b64 s[90:91], %3
s_mov_b64 s[92:93], %4
s_mov_b64 s[94:95], %5
s_mov_b32 s96, %6
s_mov_b32 s97, %7
s_mov_b32 s98, %8
s_mov_b32 s99, %9
s_mov_b32 s35, %10
s_add_u32 s28, s40, Lsub-Lpc1
s_addc_u32 s29, s41, 0
s_add_u32 s4, s84, 0x0
s_addc_u32 s5, s85, 0
s_and_b32 s5, s5, 0xffff
s_mov_b32 s6, 2621440
s_mov_b32 s7, 0x20000
s_add_u32 s8, s84, 0x4800
s_addc_u32 s9, s85, 0
s_and_b32 s9, s9, 0xffff
s_mov_b32 s10, 2603008
s_mov_b32 s11, 0x20000
s_add_u32 s60, s90, 0x0
s_addc_u32 s61, s91, 0
s_and_b32 s61, s61, 0xffff
s_mov_b32 s62, 7864320
s_mov_b32 s63, 0x20000
s_add_u32 s64, s90, 0xfffffc80
s_addc_u32 s65, s91, -1
s_and_b32 s65, s65, 0xffff
s_mov_b32 s66, 7865216
s_mov_b32 s67, 0x20000
s_add_u32 s68, s90, 0xfffff900
s_addc_u32 s69, s91, -1
s_and_b32 s69, s69, 0xffff
s_mov_b32 s70, 7866112
s_mov_b32 s71, 0x20000
s_mul_i32 s16, s98, 6144
s_add_u32 s16, s16, s99
s_lshl_b32 s17, s98, 12
s_add_u32 s17, s17, s99
s_lshr_b32 s19, s98, 1
s_lshl_b32 s19, s19, 3
s_and_b32 s0, s98, 1
s_add_u32 s19, s19, s0
s_mul_i32 s19, s19, 49152
s_mul_i32 s0, s97, 1536
s_add_u32 s19, s19, s0
s_lshl_b32 s21, s96, 2
s_add_u32 s21, s21, s98
s_mul_i32 s21, s21, 40960
v_lshrrev_b32 v24, 3, v0
v_and_b32 v25, 7, v0
v_lshrrev_b32 v26, 4, v0
v_xor_b32 v27, v25, v26
v_lshlrev_b32 v27, 4, v27
v_mul_u32_u24 v17, 1280, v24
v_add_u32 v17, v17, v27
v_xor_b32 v18, 64, v17
v_add_u32 v18, 9216, v18
v_mul_u32_u24 v9, 384, v24
v_add_u32 v9, v9, v27
v_xor_b32 v10, 64, v9
v_add_u32 v10, 3072, v10
v_lshlrev_b32 v26, 1, v26
v_xor_b32 v27, v25, v26
v_lshlrev_b32 v27, 4, v27
v_mul_u32_u24 v11, 384, v24
v_add_u32 v11, v11, v27
v_add_u32 v12, 3072, v11
v_mul_u32_u24 v15, 1280, v24
v_add_u32 v15, v15, v27
v_add_u32 v16, 10240, v15
v_lshrrev_b32 v26, 2, v24
v_lshlrev_b32 v26, 3, v26
v_and_b32 v25, 3, v24
v_add_u32 v26, v26, v25
v_mul_u32_u24 v13, 12288, v26
v_add_u32 v13, v13, v27
v_add_u32 v14, 196608, v13
s_add_u32 m0, s17, 147456
s_nop 0
s_add_u32 s22, s21, 0
dl 17,4,22,0
dl 18,4,22,1024
dl 17,8,22,2048
dl 18,8,22,3072
s_add_u32 m0, s16, 0
s_nop 0
s_add_u32 s23, s19, 0
dn 13,60,23,0
dn 13,64,23,1024
dn 13,68,23,2048
s_add_u32 m0, s16, 3072
s_nop 0
s_add_u32 s24, s21, 128
dn 14,60,23,0
dn 14,64,23,1024
dn 14,68,23,2048
s_add_u32 m0, s17, 131072
s_nop 0
s_add_u32 s25, s19, 786432
dl 17,4,24,0
dl 18,4,24,1024
dl 17,8,24,2048
dl 18,8,24,3072
s_add_u32 m0, s16, 24576
s_nop 0
s_add_u32 s22, s21, 256
dn 13,60,25,0
dn 13,64,25,1024
dn 13,68,25,2048
s_add_u32 m0, s16, 27648
s_nop 0
s_add_u32 s23, s19, 1572864
dn 14,60,25,0
dn 14,64,25,1024
dn 14,68,25,2048
s_add_u32 m0, s17, 114688
s_nop 0
dl 17,4,22,0
dl 18,4,22,1024
dl 17,8,22,2048
dl 18,8,22,3072
s_add_u32 m0, s16, 49152
s_nop 0
dn 13,60,23,0
dn 13,64,23,1024
dn 13,68,23,2048
s_add_u32 m0, s16, 52224
s_nop 0
dn 14,60,23,0
dn 14,64,23,1024
dn 14,68,23,2048
s_add_u32 s36, s86, 0x0
s_addc_u32 s37, s87, 0
s_and_b32 s37, s37, 0xffff
s_mov_b32 s38, 100663296
s_mov_b32 s39, 0x20000
s_add_u32 s48, s88, 0x0
s_addc_u32 s49, s89, 0
s_and_b32 s49, s49, 0xffff
s_mov_b32 s50, 100663296
s_mov_b32 s51, 0x20000
s_add_u32 s72, s92, 0x0
s_addc_u32 s73, s93, 0
s_and_b32 s73, s73, 0xffff
s_mov_b32 s74, 7864320
s_mov_b32 s75, 0x20000
s_add_u32 s40, s86, 0xfffffc80
s_addc_u32 s41, s87, -1
s_and_b32 s41, s41, 0xffff
s_mov_b32 s42, 100664192
s_mov_b32 s43, 0x20000
s_add_u32 s52, s88, 0xfffffc80
s_addc_u32 s53, s89, -1
s_and_b32 s53, s53, 0xffff
s_mov_b32 s54, 100664192
s_mov_b32 s55, 0x20000
s_add_u32 s76, s92, 0x13c00
s_addc_u32 s77, s93, 0
s_and_b32 s77, s77, 0xffff
s_mov_b32 s78, 7783424
s_mov_b32 s79, 0x20000
s_add_u32 s44, s86, 0xfffff900
s_addc_u32 s45, s87, -1
s_and_b32 s45, s45, 0xffff
s_mov_b32 s46, 100665088
s_mov_b32 s47, 0x20000
s_add_u32 s56, s88, 0xfffff900
s_addc_u32 s57, s89, -1
s_and_b32 s57, s57, 0xffff
s_mov_b32 s58, 100665088
s_mov_b32 s59, 0x20000
s_add_u32 s80, s92, 0x27800
s_addc_u32 s81, s93, 0
s_and_b32 s81, s81, 0xffff
s_mov_b32 s82, 7702528
s_mov_b32 s83, 0x20000
s_add_u32 s12, s94, 0x0
s_addc_u32 s13, s95, 0
s_and_b32 s13, s13, 0xffff
s_mov_b32 s14, 40960
s_mov_b32 s15, 0x20000
s_lshl_b32 s18, s96, 3
s_add_u32 s18, s18, s97
s_mul_i32 s18, s18, 786432
s_mul_i32 s0, s98, 6144
s_add_u32 s18, s18, s0
s_mul_i32 s20, s97, 48
s_add_u32 s20, s20, s98
s_mul_i32 s20, s20, 20480
s_mov_b32 s26, 0x3dd53b95
s_mov_b32 s27, 0x41000000
s_mov_b32 s1, 0xffff0000
v_lshlrev_b32 v19, 4, v0
s_lshl_b32 s0, s98, 10
s_add_u32 s0, s0, s99
s_add_u32 s0, s0, 98304
v_add_u32 v19, s0, v19
v_and_b32 v24, 15, v0
v_lshrrev_b32 v25, 4, v0
v_lshlrev_b32 v25, 2, v25
v_sub_u32 v24, v24, v25
v_mov_b32 v25, 0x3f80
v_mov_b32 v26, 0x3f800000
v_cmp_eq_u32 vcc, 0, v24
s_nop 1
v_cndmask_b32 v22, 0, v25, vcc
v_cmp_eq_u32 vcc, 1, v24
s_nop 1
v_cndmask_b32 v22, v22, v26, vcc
v_cmp_eq_u32 vcc, 2, v24
s_nop 1
v_cndmask_b32 v23, 0, v25, vcc
v_cmp_eq_u32 vcc, 3, v24
s_nop 1
v_cndmask_b32 v23, v23, v26, vcc
aw 252,22
aw 253,23
v_mov_b32 v25, 0x3f803f80
aw 240,25
aw 241,25
aw 242,25
aw 243,25
v_and_b32 v24, 15, v0
v_lshrrev_b32 v25, 4, v0
v_lshrrev_b32 v26, 1, v24
v_xor_b32 v26, v26, v25
v_lshlrev_b32 v26, 4, v26
v_lshrrev_b32 v27, 3, v24
v_mul_u32_u24 v5, 3072, v27
v_and_b32 v27, 7, v24
v_lshlrev_b32 v27, 7, v27
v_add3_u32 v5, v5, v27, v26
v_xor_b32 v6, 64, v5
v_add_u32 v5, s99, v5
v_add_u32 v6, s99, v6
v_lshlrev_b32 v27, 7, v24
v_add_u32 v7, v27, v26
v_xor_b32 v8, 64, v7
v_add_u32 v7, s17, v7
v_add_u32 v8, s17, v8
v_lshrrev_b32 v26, 5, v0
v_mul_u32_u24 v1, 3072, v26
v_bfe_u32 v26, v0, 2, 3
v_lshlrev_b32 v26, 7, v26
v_bfe_u32 v27, v0, 3, 2
v_lshlrev_b32 v27, 5, v27
v_add3_u32 v1, v1, v26, v27
v_and_b32 v26, 3, v0
v_lshlrev_b32 v26, 3, v26
v_add_u32 v1, v1, v26
v_xor_b32 v2, 32, v1
v_xor_b32 v3, 64, v1
v_xor_b32 v4, 96, v1
v_add_u32 v1, s99, v1
v_add_u32 v2, s99, v2
v_add_u32 v3, s99, v3
v_add_u32 v4, s99, v4
s_and_b32 s0, s98, 1
s_mul_i32 s89, s0, 7168
s_lshl_b32 s90, s0, 12
s_lshl_b32 s0, s0, 6
v_xor_b32 v22, s0, v1
v_xor_b32 v23, s0, v2
v_subrev_u32 v7, s90, v7
v_subrev_u32 v8, s90, v8
s_nop 4
v_add_u32 v7, 0x16000, v7
v_add_u32 v8, 0x16000, v8
v_add_u32 v1, 0x8000, v1
s_waitcnt vmcnt(20) lgkmcnt(0)
s_barrier
rd 216,7,57344
tr 232,22,0
tr 234,22,6144
s_add_u32 m0, s17, 98304
s_add_u32 s24, s21, 384
rd 220,7,59392
dl 17,4,24,0
dl 18,4,24,1024
dl 17,8,24,2048
rd 224,7,61440
dl 18,8,24,3072
rd 228,7,63488
tr 236,23,0
tr 238,23,6144
tr 240,22,1024
tr 242,22,7168
tr 244,23,1024
tr 246,23,7168
tr 248,22,2048
tr 250,22,8192
tr 252,23,2048
tr 254,23,8192
v_add_u32 v2, 0x8000, v2
v_add_u32 v3, 0x8000, v3
v_add_u32 v4, 0x8000, v4
s_waitcnt lgkmcnt(12)
mvz 120,232,216
mvz 128,232,220
s_waitcnt lgkmcnt(8)
mvz 136,232,224
mvz 144,232,228
rd 232,8,57344
mvz 148,236,228
mvz 140,236,224
mvz 132,236,220
mvz 124,236,216
rd 236,8,59392
s_waitcnt lgkmcnt(2)
mvz 152,240,216
mvz 160,240,220
mvz 168,240,224
mvz 176,240,228
s_add_u32 m0, s16, 73728
s_add_u32 s25, s19, 2359296
dn 13,60,25,0
dn 13,64,25,1024
mvz 180,244,228
dn 13,68,25,2048
mvz 172,244,224
rd 240,8,61440
mvz 164,244,220
mvz 156,244,216
rd 244,8,63488
mvz 184,248,216
mvz 192,248,220
mvz 200,248,224
mvz 208,248,228
tr 248,22,12288
tr 250,22,18432
mvz 212,252,228
tr 228,23,12288
tr 230,23,18432
mvz 204,252,224
tr 224,22,13312
tr 226,22,19456
mvz 196,252,220
tr 220,23,13312
tr 222,23,19456
mvz 188,252,216
tr 252,22,14336
tr 254,22,20480
tr 216,23,14336
tr 218,23,20480
s_waitcnt lgkmcnt(8)
mv 120,248,232
mv 128,248,236
s_waitcnt vmcnt(17) lgkmcnt(0)
s_barrier
mv 136,248,240
mv 144,248,244
s_add_u32 m0, s16, 76800
rd 248,7,40960
dn 14,60,25,0
dn 14,64,25,1024
dn 14,68,25,2048
mv 148,228,244
mv 140,228,240
mv 132,228,236
mv 124,228,232
rd 228,7,43008
mv 152,224,232
mv 160,224,236
mv 168,224,240
mv 176,224,244
rd 224,7,45056
mv 180,220,244
mv 172,220,240
mv 164,220,236
mv 156,220,232
rd 220,7,47104
mv 184,252,232
mv 192,252,236
mv 200,252,240
mv 208,252,244
s_add_u32 m0, s17, 147456
s_add_u32 s22, s21, 512
dl 17,4,22,0
tr 252,22,24576
tr 254,22,30720
mv 212,216,244
tr 244,23,24576
tr 246,23,30720
mv 204,216,240
tr 240,22,25600
tr 242,22,31744
mv 196,216,236
tr 236,23,25600
tr 238,23,31744
dl 18,4,22,1024
mv 188,216,232
tr 216,22,26624
tr 218,22,32768
tr 232,23,26624
tr 234,23,32768
dl 17,8,22,2048
s_waitcnt lgkmcnt(8)
mv 120,252,248
mv 128,252,228
mv 136,252,224
mv 144,252,220
dl 18,8,22,3072
mv 148,244,220
rd 252,8,40960
mv 140,244,224
mv 132,244,228
mv 124,244,248
rd 244,8,43008
s_waitcnt lgkmcnt(2)
mv 152,240,248
mv 160,240,228
mv 168,240,224
mv 176,240,220
s_add_u32 s23, s19, 3145728
rd 240,8,45056
s_add_u32 m0, s16, 0
mv 180,236,220
dn 13,60,23,0
mv 172,236,224
mv 164,236,228
mv 156,236,248
rd 236,8,47104
mv 184,216,248
dn 13,64,23,1024
mv 192,216,228
mv 200,216,224
mv 208,216,220
tr 216,22,36864
tr 218,22,43008
mv 212,232,220
dn 13,68,23,2048
mv 204,232,224
tr 220,23,36864
tr 222,23,43008
mv 196,232,228
tr 224,22,37888
tr 226,22,44032
mv 188,232,248
tr 228,23,37888
tr 230,23,44032
tr 232,22,38912
tr 234,22,45056
tr 248,23,38912
tr 250,23,45056
v_add_u32 v22, 0x8000, v22
v_add_u32 v23, 0x8000, v23
s_waitcnt lgkmcnt(10)
mv 120,216,252
mv 128,216,244
s_waitcnt vmcnt(17) lgkmcnt(0)
s_barrier
mv 136,216,240
mv 144,216,236
s_add_u32 m0, s16, 3072
rd 216,7,24576
dn 14,60,23,0
mv 148,220,236
mv 140,220,240
mv 132,220,244
mv 124,220,252
dn 14,64,23,1024
mv 152,224,252
rd 220,7,26624
mv 160,224,244
mv 168,224,240
mv 176,224,236
dn 14,68,23,2048
mv 180,228,236
rd 224,7,28672
mv 172,228,240
mv 164,228,244
mv 156,228,252
rd 228,7,30720
mv 184,232,252
mv 192,232,244
mv 200,232,240
mv 208,232,236
s_add_u32 s24, s21, 640
tr 232,22,16384
tr 234,22,22528
s_add_u32 m0, s17, 131072
mv 212,248,236
dl 17,4,24,0
mv 204,248,240
tr 236,23,16384
tr 238,23,22528
mv 196,248,244
tr 240,22,17408
tr 242,22,23552
mv 188,248,252
tr 244,23,17408
tr 246,23,23552
tr 248,22,18432
tr 250,22,24576
dl 18,4,24,1024
tr 252,23,18432
tr 254,23,24576
s_waitcnt lgkmcnt(10)
mv 120,232,216
mv 128,232,220
dl 17,8,24,2048
mv 136,232,224
mv 144,232,228
rd 232,8,24576
s_waitcnt lgkmcnt(2)
mv 148,236,228
mv 140,236,224
dl 18,8,24,3072
mv 132,236,220
mv 124,236,216
rd 236,8,26624
mv 152,240,216
mv 160,240,220
mv 168,240,224
mv 176,240,228
s_add_u32 s25, s19, 3932160
rd 240,8,28672
mv 180,244,228
mv 172,244,224
mv 164,244,220
s_add_u32 m0, s16, 24576
s_nop 0
dn 13,60,25,0
mv 156,244,216
rd 244,8,30720
mv 184,248,216
mv 192,248,220
mv 200,248,224
dn 13,64,25,1024
mv 208,248,228
tr 248,22,28672
tr 250,22,34816
s_waitcnt lgkmcnt(3)
mv 212,252,228
tr 228,23,28672
tr 230,23,34816
mv 204,252,224
tr 224,22,29696
tr 226,22,35840
mv 196,252,220
dn 13,68,25,2048
mv 188,252,216
tr 220,23,29696
tr 222,23,35840
tr 252,22,30720
tr 254,22,36864
tr 216,23,30720
tr 218,23,36864
s_waitcnt lgkmcnt(8)
mv 120,248,232
mv 128,248,236
mv 136,248,240
s_waitcnt vmcnt(17) lgkmcnt(0)
s_barrier
mv 144,248,244
rd 248,7,8192
s_add_u32 m0, s16, 27648
mv 148,228,244
dn 14,60,25,0
mv 140,228,240
mv 132,228,236
mv 124,228,232
rd 228,7,10240
mv 152,224,232
dn 14,64,25,1024
mv 160,224,236
mv 168,224,240
mv 176,224,244
rd 224,7,12288
mv 180,220,244
dn 14,68,25,2048
mv 172,220,240
mv 164,220,236
mv 156,220,232
rd 220,7,14336
mv 184,252,232
mv 192,252,236
mv 200,252,240
mv 208,252,244
s_add_u32 s22, s21, 768
tr 252,22,40960
tr 254,22,47104
mv 212,216,244
tr 244,23,40960
tr 246,23,47104
s_add_u32 m0, s17, 114688
mv 204,216,240
dl 17,4,22,0
mv 196,216,236
tr 240,22,41984
tr 242,22,48128
mv 188,216,232
tr 236,23,41984
tr 238,23,48128
tr 216,22,43008
tr 218,22,49152
tr 232,23,43008
tr 234,23,49152
dl 18,4,22,1024
s_waitcnt lgkmcnt(8)
mv 120,252,248
mv 128,252,228
mv 136,252,224
dl 17,8,22,2048
mv 144,252,220
rd 252,8,8192
mv 148,244,220
mv 140,244,224
mv 132,244,228
dl 18,8,22,3072
mv 124,244,248
rd 244,8,10240
s_waitcnt lgkmcnt(2)
mv 152,240,248
mv 160,240,228
mv 168,240,224
mv 176,240,220
s_add_u32 s23, s19, 4718592
rd 240,8,12288
mv 180,236,220
mv 172,236,224
mv 164,236,228
s_add_u32 m0, s16, 49152
mv 156,236,248
dn 13,60,23,0
mv 184,216,248
rd 236,8,14336
mv 192,216,228
mv 200,216,224
mv 208,216,220
dn 13,64,23,1024
tr 216,22,53248
tr 218,22,59392
mv 212,232,220
tr 220,23,53248
tr 222,23,59392
mv 204,232,224
tr 224,22,54272
tr 226,22,60416
mv 196,232,228
tr 228,23,54272
tr 230,23,60416
dn 13,68,23,2048
mv 188,232,248
tr 232,22,55296
tr 234,22,61440
tr 248,23,55296
tr 250,23,61440
v_add_u32 v22, 0xffff8000, v22
v_add_u32 v23, 0xffff8000, v23
s_waitcnt lgkmcnt(8)
mv 120,216,252
mv 128,216,244
s_waitcnt vmcnt(17) lgkmcnt(0)
s_barrier
mv 136,216,240
mv 144,216,236
rd 216,7,57344
s_add_u32 m0, s16, 52224
mv 148,220,236
dn 14,60,23,0
mv 140,220,240
mv 132,220,244
mv 124,220,252
rd 220,7,59392
mv 152,224,252
dn 14,64,23,1024
mv 160,224,244
mv 168,224,240
mv 176,224,236
rd 224,7,61440
mv 180,228,236
dn 14,68,23,2048
mv 172,228,240
mv 164,228,244
mv 156,228,252
rd 228,7,63488
mv 184,232,252
mv 192,232,244
mv 200,232,240
mv 208,232,236
s_add_u32 s24, s21, 896
tr 232,22,0
tr 234,22,6144
mv 212,248,236
tr 236,23,0
tr 238,23,6144
s_add_u32 m0, s17, 98304
mv 204,248,240
dl 17,4,24,0
mv 196,248,244
tr 240,22,1024
tr 242,22,7168
mv 188,248,252
tr 244,23,1024
tr 246,23,7168
tr 248,22,2048
tr 250,22,8192
tr 252,23,2048
tr 254,23,8192
dl 18,4,24,1024
s_waitcnt lgkmcnt(8)
mv 120,232,216
mv 128,232,220
mv 136,232,224
dl 17,8,24,2048
mv 144,232,228
rd 232,8,57344
mv 148,236,228
mv 140,236,224
mv 132,236,220
dl 18,8,24,3072
mv 124,236,216
rd 236,8,59392
s_waitcnt lgkmcnt(2)
mv 152,240,216
mv 160,240,220
mv 168,240,224
mv 176,240,228
s_add_u32 s25, s19, 5505024
rd 240,8,61440
mv 180,244,228
mv 172,244,224
mv 164,244,220
s_add_u32 m0, s16, 73728
mv 156,244,216
dn 13,60,25,0
mv 184,248,216
rd 244,8,63488
mv 192,248,220
mv 200,248,224
mv 208,248,228
dn 13,64,25,1024
tr 248,22,12288
tr 250,22,18432
mv 212,252,228
tr 228,23,12288
tr 230,23,18432
mv 204,252,224
tr 224,22,13312
tr 226,22,19456
mv 196,252,220
tr 220,23,13312
tr 222,23,19456
dn 13,68,25,2048
mv 188,252,216
tr 252,22,14336
tr 254,22,20480
tr 216,23,14336
tr 218,23,20480
s_waitcnt lgkmcnt(8)
mv 120,248,232
mv 128,248,236
s_waitcnt vmcnt(17) lgkmcnt(0)
s_barrier
mv 136,248,240
mv 144,248,244
rd 248,7,40960
s_add_u32 m0, s16, 76800
mv 148,228,244
dn 14,60,25,0
mv 140,228,240
mv 132,228,236
mv 124,228,232
rd 228,7,43008
mv 152,224,232
dn 14,64,25,1024
mv 160,224,236
mv 168,224,240
mv 176,224,244
rd 224,7,45056
mv 180,220,244
dn 14,68,25,2048
mv 172,220,240
mv 164,220,236
mv 156,220,232
rd 220,7,47104
mv 184,252,232
mv 192,252,236
mv 200,252,240
mv 208,252,244
s_add_u32 s22, s21, 1024
tr 252,22,24576
tr 254,22,30720
mv 212,216,244
tr 244,23,24576
tr 246,23,30720
s_add_u32 m0, s17, 147456
mv 204,216,240
dl 17,4,22,0
mv 196,216,236
tr 240,22,25600
tr 242,22,31744
mv 188,216,232
tr 236,23,25600
tr 238,23,31744
tr 216,22,26624
tr 218,22,32768
tr 232,23,26624
tr 234,23,32768
dl 18,4,22,1024
s_waitcnt lgkmcnt(8)
mv 120,252,248
mv 128,252,228
mv 136,252,224
dl 17,8,22,2048
mv 144,252,220
rd 252,8,40960
mv 148,244,220
mv 140,244,224
mv 132,244,228
dl 18,8,22,3072
mv 124,244,248
rd 244,8,43008
s_waitcnt lgkmcnt(2)
mv 152,240,248
mv 160,240,228
mv 168,240,224
mv 176,240,220
s_add_u32 s23, s19, 6291456
rd 240,8,45056
mv 180,236,220
mv 172,236,224
mv 164,236,228
s_add_u32 m0, s16, 0
mv 156,236,248
dn 13,60,23,0
mv 184,216,248
rd 236,8,47104
mv 192,216,228
mv 200,216,224
mv 208,216,220
dn 13,64,23,1024
tr 216,22,36864
tr 218,22,43008
mv 212,232,220
tr 220,23,36864
tr 222,23,43008
mv 204,232,224
tr 224,22,37888
tr 226,22,44032
mv 196,232,228
tr 228,23,37888
tr 230,23,44032
dn 13,68,23,2048
mv 188,232,248
tr 232,22,38912
tr 234,22,45056
tr 248,23,38912
tr 250,23,45056
v_add_u32 v22, 0x8000, v22
v_add_u32 v23, 0x8000, v23
s_waitcnt lgkmcnt(8)
mv 120,216,252
mv 128,216,244
s_waitcnt vmcnt(17) lgkmcnt(0)
s_barrier
mv 136,216,240
mv 144,216,236
rd 216,7,24576
s_add_u32 m0, s16, 3072
mv 148,220,236
dn 14,60,23,0
mv 140,220,240
mv 132,220,244
mv 124,220,252
rd 220,7,26624
mv 152,224,252
dn 14,64,23,1024
mv 160,224,244
mv 168,224,240
mv 176,224,236
rd 224,7,28672
mv 180,228,236
dn 14,68,23,2048
mv 172,228,240
mv 164,228,244
mv 156,228,252
rd 228,7,30720
mv 184,232,252
mv 192,232,244
mv 200,232,240
mv 208,232,236
s_add_u32 s24, s21, 1152
tr 232,22,16384
tr 234,22,22528
mv 212,248,236
tr 236,23,16384
tr 238,23,22528
s_add_u32 m0, s17, 131072
mv 204,248,240
dl 17,4,24,0
mv 196,248,244
tr 240,22,17408
tr 242,22,23552
mv 188,248,252
tr 244,23,17408
tr 246,23,23552
tr 248,22,18432
tr 250,22,24576
tr 252,23,18432
tr 254,23,24576
dl 18,4,24,1024
s_waitcnt lgkmcnt(8)
mv 120,232,216
mv 128,232,220
mv 136,232,224
dl 17,8,24,2048
mv 144,232,228
rd 232,8,24576
mv 148,236,228
mv 140,236,224
mv 132,236,220
dl 18,8,24,3072
mv 124,236,216
rd 236,8,26624
s_waitcnt lgkmcnt(2)
mv 152,240,216
mv 160,240,220
mv 168,240,224
mv 176,240,228
s_add_u32 s25, s19, 7077888
rd 240,8,28672
mv 180,244,228
mv 172,244,224
mv 164,244,220
s_add_u32 m0, s16, 24576
mv 156,244,216
dn 13,60,25,0
mv 184,248,216
rd 244,8,30720
mv 192,248,220
mv 200,248,224
mv 208,248,228
dn 13,64,25,1024
tr 248,22,28672
tr 250,22,34816
mv 212,252,228
tr 228,23,28672
tr 230,23,34816
mv 204,252,224
tr 224,22,29696
tr 226,22,35840
mv 196,252,220
tr 220,23,29696
tr 222,23,35840
dn 13,68,25,2048
mv 188,252,216
tr 252,22,30720
tr 254,22,36864
tr 216,23,30720
tr 218,23,36864
s_waitcnt lgkmcnt(8)
mv 120,248,232
mv 128,248,236
s_waitcnt vmcnt(17) lgkmcnt(0)
s_barrier
mv 136,248,240
mv 144,248,244
rd 248,7,8192
s_add_u32 m0, s16, 27648
mv 148,228,244
dn 14,60,25,0
mv 140,228,240
mv 132,228,236
mv 124,228,232
rd 228,7,10240
mv 152,224,232
dn 14,64,25,1024
mv 160,224,236
mv 168,224,240
mv 176,224,244
rd 224,7,12288
mv 180,220,244
dn 14,68,25,2048
mv 172,220,240
mv 164,220,236
mv 156,220,232
rd 220,7,14336
mv 184,252,232
mv 192,252,236
mv 200,252,240
mv 208,252,244
tr 252,22,40960
tr 254,22,47104
mv 212,216,244
tr 244,23,40960
tr 246,23,47104
mv 204,216,240
tr 240,22,41984
tr 242,22,48128
mv 196,216,236
tr 236,23,41984
tr 238,23,48128
mv 188,216,232
tr 216,22,43008
tr 218,22,49152
tr 232,23,43008
tr 234,23,49152
s_waitcnt lgkmcnt(8)
mv 120,252,248
mv 128,252,228
mv 136,252,224
mv 144,252,220
rd 252,8,8192
mv 148,244,220
mv 140,244,224
mv 132,244,228
mv 124,244,248
rd 244,8,10240
s_waitcnt lgkmcnt(2)
mv 152,240,248
mv 160,240,228
mv 168,240,224
mv 176,240,220
rd 240,8,12288
mv 180,236,220
mv 172,236,224
mv 164,236,228
mv 156,236,248
rd 236,8,14336
mv 184,216,248
mv 192,216,228
mv 200,216,224
mv 208,216,220
tr 216,22,53248
tr 218,22,59392
mv 212,232,220
tr 220,23,53248
tr 222,23,59392
mv 204,232,224
tr 224,22,54272
tr 226,22,60416
mv 196,232,228
tr 228,23,54272
tr 230,23,60416
mv 188,232,248
tr 232,22,55296
tr 234,22,61440
tr 248,23,55296
tr 250,23,61440
v_add_u32 v22, 0xffff8000, v22
v_add_u32 v23, 0xffff8000, v23
s_waitcnt lgkmcnt(8)
mv 120,216,252
mv 128,216,244
s_waitcnt vmcnt(10) lgkmcnt(0)
s_barrier
mv 136,216,240
mv 144,216,236
rd 216,7,57344
mv 148,220,236
mv 140,220,240
mv 132,220,244
mv 124,220,252
rd 220,7,59392
mv 152,224,252
mv 160,224,244
mv 168,224,240
mv 176,224,236
rd 224,7,61440
mv 180,228,236
mv 172,228,240
mv 164,228,244
mv 156,228,252
rd 228,7,63488
mv 184,232,252
mv 192,232,244
mv 200,232,240
mv 208,232,236
s_add_u32 m0, s17, 81920
s_add_u32 s22, s21, 0
dl 17,4,22,0
tr 232,22,0
tr 234,22,6144
mv 212,248,236
tr 236,23,0
tr 238,23,6144
mv 204,248,240
tr 240,22,1024
tr 242,22,7168
mv 196,248,244
tr 244,23,1024
tr 246,23,7168
dl 18,4,22,1024
mv 188,248,252
tr 248,22,2048
tr 250,22,8192
tr 252,23,2048
tr 254,23,8192
dl 17,8,22,2048
s_waitcnt lgkmcnt(8)
mv 120,232,216
mv 128,232,220
mv 136,232,224
mv 144,232,228
dl 18,8,22,3072
mv 148,236,228
rd 232,8,57344
mv 140,236,224
mv 132,236,220
mv 124,236,216
rd 236,8,59392
s_waitcnt lgkmcnt(2)
mv 152,240,216
mv 160,240,220
mv 168,240,224
mv 176,240,228
s_add_u32 s23, s19, 768
rd 240,8,61440
s_add_u32 m0, s16, 49152
mv 180,244,228
dn 13,60,23,0
mv 172,244,224
mv 164,244,220
mv 156,244,216
rd 244,8,63488
mv 184,248,216
dn 13,64,23,1024
mv 192,248,220
mv 200,248,224
mv 208,248,228
tr 248,22,12288
tr 250,22,18432
mv 212,252,228
dn 13,68,23,2048
mv 204,252,224
tr 228,23,12288
tr 230,23,18432
mv 196,252,220
tr 224,22,13312
tr 226,22,19456
mv 188,252,216
tr 220,23,13312
tr 222,23,19456
tr 252,22,14336
tr 254,22,20480
tr 216,23,14336
tr 218,23,20480
s_waitcnt lgkmcnt(10)
mv 120,248,232
mv 128,248,236
s_waitcnt vmcnt(7) lgkmcnt(0)
s_barrier
mv 136,248,240
mv 144,248,244
s_add_u32 m0, s16, 52224
rd 248,7,40960
dn 14,60,23,0
mv 148,228,244
mv 140,228,240
mv 132,228,236
mv 124,228,232
dn 14,64,23,1024
mv 152,224,232
rd 228,7,43008
mv 160,224,236
mv 168,224,240
mv 176,224,244
dn 14,68,23,2048
mv 180,220,244
rd 224,7,45056
mv 172,220,240
mv 164,220,236
mv 156,220,232
rd 220,7,47104
v_add_u32 v7, 0xffff0000, v7
mv 184,252,232
mv 192,252,236
mv 200,252,240
mv 208,252,244
s_add_u32 s24, s19, 384
tr 252,22,24576
tr 254,22,30720
s_add_u32 m0, s16, 0
mv 212,216,244
dn 13,60,24,0
mv 204,216,240
tr 244,23,24576
tr 246,23,30720
mv 196,216,236
tr 240,22,25600
tr 242,22,31744
mv 188,216,232
tr 236,23,25600
tr 238,23,31744
tr 216,22,26624
tr 218,22,32768
dn 13,64,24,1024
tr 232,23,26624
tr 234,23,32768
s_waitcnt lgkmcnt(10)
mv 120,252,248
mv 128,252,228
dn 13,68,24,2048
mv 136,252,224
mv 144,252,220
rd 252,8,40960
s_waitcnt lgkmcnt(2)
mv 148,244,220
mv 140,244,224
mv 132,244,228
mv 124,244,248
rd 244,8,43008
mv 152,240,248
mv 160,240,228
mv 168,240,224
mv 176,240,220
rd 240,8,45056
mv 180,236,220
mv 172,236,224
mv 164,236,228
mv 156,236,248
rd 236,8,47104
v_add_u32 v8, 0xffff0000, v8
mv 184,216,248
mv 192,216,228
mv 200,216,224
mv 208,216,220
s_add_u32 m0, s16, 3072
tr 216,22,36864
dn 14,60,24,0
tr 218,22,43008
s_waitcnt lgkmcnt(3)
mv 212,232,220
tr 220,23,36864
tr 222,23,43008
mv 204,232,224
tr 224,22,37888
tr 226,22,44032
mv 196,232,228
tr 228,23,37888
tr 230,23,44032
dn 14,64,24,1024
mv 188,232,248
tr 232,22,38912
tr 234,22,45056
tr 248,23,38912
tr 250,23,45056
v_add_u32 v22, 0x2000, v22
v_add_u32 v23, 0x2000, v23
dn 14,68,24,2048
s_waitcnt lgkmcnt(8)
mv 120,216,252
mv 128,216,244
s_waitcnt vmcnt(6) lgkmcnt(0)
s_barrier
mv 136,216,240
ds_read_b128 a[192:195], v7 offset:57344
ds_read_b128 a[196:199], v7 offset:59392
mv 144,216,236
tr 216,22,40960
tr 218,22,47104
mv 148,220,236
ds_read_b128 a[200:203], v7 offset:61440
ds_read_b128 a[204:207], v7 offset:63488
mv 140,220,240
ds_read_b128 a[208:211], v8 offset:57344
ds_read_b128 a[212:215], v8 offset:59392
mv 132,220,244
ds_read_b128 a[216:219], v8 offset:61440
ds_read_b128 a[220:223], v8 offset:63488
mv 124,220,252
tr 220,23,40960
tr 222,23,47104
mv 152,224,252
v_mul_f32 v120, s26, v120
v_mul_f32 v121, s26, v121
mv 160,224,244
v_mul_f32 v122, s26, v122
v_mul_f32 v123, s26, v123
mv 168,224,240
cv 120,120,121
v_mul_f32 v124, s26, v124
mv 176,224,236
tr 224,22,41984
tr 226,22,48128
mv 180,228,236
v_mul_f32 v125, s26, v125
v_mul_f32 v126, s26, v126
mv 172,228,240
v_mul_f32 v127, s26, v127
cv 121,122,123
mv 164,228,244
cv 122,124,125
cv 123,126,127
mv 156,228,252
tr 228,23,41984
tr 230,23,48128
mv 184,232,252
v_mul_f32 v128, s26, v128
v_mul_f32 v129, s26, v129
mv 192,232,244
v_permlane32_swap_b32 v120, v122
v_permlane32_swap_b32 v121, v123
mv 200,232,240
v_mul_f32 v130, s26, v130
v_mul_f32 v131, s26, v131
mv 208,232,236
s_add_u32 m0, s16, 24576
s_add_u32 s25, s18, 0
dn 9,36,25,0
mv 212,248,236
tr 232,22,43008
tr 234,22,49152
mv 204,248,240
tr 236,23,43008
tr 238,23,49152
mv 196,248,244
tr 240,22,53248
tr 242,22,59392
mv 188,248,252
dn 9,40,25,1024
s_waitcnt lgkmcnt(6)
v_mfma_f32_16x16x32_bf16 a[96:99], v[216:219], a[192:195], 0
tr 244,23,53248
tr 246,23,59392
v_mfma_f32_16x16x32_bf16 a[104:107], v[216:219], a[196:199], 0
tr 248,22,54272
tr 250,22,60416
v_mfma_f32_16x16x32_bf16 a[112:115], v[216:219], a[200:203], 0
tr 252,23,54272
tr 254,23,60416
v_mfma_f32_16x16x32_bf16 a[120:123], v[216:219], a[204:207], 0
dn 9,44,25,2048
v_mfma_f32_16x16x32_bf16 a[124:127], v[220:223], a[204:207], 0
tr 216,22,55296
tr 218,22,61440
v_mfma_f32_16x16x32_bf16 a[116:119], v[220:223], a[200:203], 0
v_add_u32 v22, 0xffffe000, v22
cv 128,128,129
v_mfma_f32_16x16x32_bf16 a[108:111], v[220:223], a[196:199], 0
v_mul_f32 v132, s26, v132
v_mul_f32 v133, s26, v133
v_mfma_f32_16x16x32_bf16 a[100:103], v[220:223], a[192:195], 0
tr 220,23,55296
tr 222,23,61440
v_mfma_f32_16x16x32_bf16 a[128:131], v[224:227], a[192:195], 0
v_add_u32 v23, 0xffffe000, v23
v_mul_f32 v134, s26, v134
v_mfma_f32_16x16x32_bf16 a[136:139], v[224:227], a[196:199], 0
v_mul_f32 v135, s26, v135
cv 129,130,131
v_mfma_f32_16x16x32_bf16 a[144:147], v[224:227], a[200:203], 0
cv 130,132,133
cv 131,134,135
v_mfma_f32_16x16x32_bf16 a[152:155], v[224:227], a[204:207], 0
v_add_u32 v124, s89, v19
v_permlane16_swap_b32 v120, v122
v_mfma_f32_16x16x32_bf16 a[156:159], v[228:231], a[204:207], 0
s_waitcnt vmcnt(3) lgkmcnt(0)
s_barrier
v_mfma_f32_16x16x32_bf16 a[148:151], v[228:231], a[200:203], 0
tr 224,22,0
tr 226,22,6144
v_mfma_f32_16x16x32_bf16 a[140:143], v[228:231], a[196:199], 0
v_permlane16_swap_b32 v121, v123
ds_write_b128 v124, v[120:123] offset:0
v_mfma_f32_16x16x32_bf16 a[132:135], v[228:231], a[192:195], 0
tr 228,23,0
tr 230,23,6144
v_mfma_f32_16x16x32_bf16 a[160:163], v[232:235], a[192:195], 0
v_mul_f32 v136, s26, v136
v_mul_f32 v137, s26, v137
v_mfma_f32_16x16x32_bf16 a[168:171], v[232:235], a[196:199], 0
v_permlane32_swap_b32 v128, v130
v_permlane32_swap_b32 v129, v131
v_mfma_f32_16x16x32_bf16 a[176:179], v[232:235], a[200:203], 0
v_mul_f32 v138, s26, v138
v_mul_f32 v139, s26, v139
v_mfma_f32_16x16x32_bf16 a[184:187], v[232:235], a[204:207], 0
s_add_u32 m0, s16, 27648
cv 136,136,137
dn 10,36,25,0
v_mfma_f32_16x16x32_bf16 a[188:191], v[236:239], a[204:207], 0
v_mul_f32 v140, s26, v140
v_mul_f32 v141, s26, v141
v_mfma_f32_16x16x32_bf16 a[180:183], v[236:239], a[200:203], 0
v_mul_f32 v142, s26, v142
v_mul_f32 v143, s26, v143
v_mfma_f32_16x16x32_bf16 a[172:175], v[236:239], a[196:199], 0
cv 137,138,139
cv 138,140,141
v_mfma_f32_16x16x32_bf16 a[164:167], v[236:239], a[192:195], 0
dn 10,40,25,1024
v_mfma_f32_16x16x32_bf16 a[96:99], v[240:243], a[208:211], a[96:99]
cv 139,142,143
v_add_u32 v132, s89, v19
v_mfma_f32_16x16x32_bf16 a[104:107], v[240:243], a[212:215], a[104:107]
v_permlane16_swap_b32 v128, v130
v_permlane16_swap_b32 v129, v131
v_mfma_f32_16x16x32_bf16 a[112:115], v[240:243], a[216:219], a[112:115]
ds_write_b128 v132, v[128:131] offset:4096
v_mul_f32 v144, s26, v144
v_mfma_f32_16x16x32_bf16 a[120:123], v[240:243], a[220:223], a[120:123]
dn 10,44,25,2048
v_mfma_f32_16x16x32_bf16 a[124:127], v[244:247], a[220:223], a[124:127]
v_mul_f32 v145, s26, v145
v_permlane32_swap_b32 v136, v138
v_mfma_f32_16x16x32_bf16 a[116:119], v[244:247], a[216:219], a[116:119]
v_permlane32_swap_b32 v137, v139
v_mul_f32 v146, s26, v146
v_mfma_f32_16x16x32_bf16 a[108:111], v[244:247], a[212:215], a[108:111]
v_mul_f32 v147, s26, v147
cv 144,144,145
v_mfma_f32_16x16x32_bf16 a[100:103], v[244:247], a[208:211], a[100:103]
v_mul_f32 v148, s26, v148
v_mul_f32 v149, s26, v149
v_mfma_f32_16x16x32_bf16 a[128:131], v[248:251], a[208:211], a[128:131]
v_mul_f32 v150, s26, v150
v_mul_f32 v151, s26, v151
v_mfma_f32_16x16x32_bf16 a[136:139], v[248:251], a[212:215], a[136:139]
cv 145,146,147
cv 146,148,149
v_mfma_f32_16x16x32_bf16 a[144:147], v[248:251], a[216:219], a[144:147]
cv 147,150,151
v_add_u32 v140, s89, v19
v_mfma_f32_16x16x32_bf16 a[152:155], v[248:251], a[220:223], a[152:155]
v_permlane16_swap_b32 v136, v138
v_permlane16_swap_b32 v137, v139
v_mfma_f32_16x16x32_bf16 a[156:159], v[252:255], a[220:223], a[156:159]
ds_write_b128 v140, v[136:139] offset:1024
v_permlane32_swap_b32 v144, v146
v_mfma_f32_16x16x32_bf16 a[148:151], v[252:255], a[216:219], a[148:151]
v_permlane32_swap_b32 v145, v147
tr 232,22,1024
v_mfma_f32_16x16x32_bf16 a[140:143], v[252:255], a[212:215], a[140:143]
tr 234,22,7168
v_add_u32 v148, s89, v19
v_mfma_f32_16x16x32_bf16 a[132:135], v[252:255], a[208:211], a[132:135]
v_permlane16_swap_b32 v144, v146
v_permlane16_swap_b32 v145, v147
v_mfma_f32_16x16x32_bf16 a[160:163], v[216:219], a[208:211], a[160:163]
ds_write_b128 v148, v[144:147] offset:5120
v_mul_f32 v152, s26, v152
v_mfma_f32_16x16x32_bf16 a[168:171], v[216:219], a[212:215], a[168:171]
v_mul_f32 v153, s26, v153
v_mul_f32 v154, s26, v154
v_mfma_f32_16x16x32_bf16 a[176:179], v[216:219], a[216:219], a[176:179]
v_mul_f32 v155, s26, v155
cv 152,152,153
v_mfma_f32_16x16x32_bf16 a[184:187], v[216:219], a[220:223], a[184:187]
s_add_u32 m0, s17, 147456
s_add_u32 s22, s21, 128
dl 17,4,22,0
)"
R"(v_mfma_f32_16x16x32_bf16 a[188:191], v[220:223], a[220:223], a[188:191]
tr 236,23,1024
tr 238,23,7168
v_mfma_f32_16x16x32_bf16 a[180:183], v[220:223], a[216:219], a[180:183]
v_mul_f32 v156, s26, v156
v_mul_f32 v157, s26, v157
v_mfma_f32_16x16x32_bf16 a[172:175], v[220:223], a[212:215], a[172:175]
v_mul_f32 v158, s26, v158
v_mul_f32 v159, s26, v159
v_mfma_f32_16x16x32_bf16 a[164:167], v[220:223], a[208:211], a[164:167]
dl 18,4,22,1024
s_waitcnt lgkmcnt(2)
v_mfma_f32_16x16x32_bf16 v[120:123], v[224:227], a[192:195], 0
cv 153,154,155
cv 154,156,157
v_mfma_f32_16x16x32_bf16 v[128:131], v[224:227], a[196:199], 0
cv 155,158,159
v_mul_f32 v160, s26, v160
v_mfma_f32_16x16x32_bf16 v[136:139], v[224:227], a[200:203], 0
v_mul_f32 v161, s26, v161
v_permlane32_swap_b32 v152, v154
v_mfma_f32_16x16x32_bf16 v[144:147], v[224:227], a[204:207], 0
dl 17,8,22,2048
v_mfma_f32_16x16x32_bf16 v[148:151], v[228:231], a[204:207], 0
s_add_u32 s23, s19, 786816
v_permlane32_swap_b32 v153, v155
v_mfma_f32_16x16x32_bf16 v[140:143], v[228:231], a[200:203], 0
v_mul_f32 v162, s26, v162
v_mul_f32 v163, s26, v163
v_mfma_f32_16x16x32_bf16 v[132:135], v[228:231], a[196:199], 0
cv 160,160,161
v_mul_f32 v164, s26, v164
v_mfma_f32_16x16x32_bf16 v[124:127], v[228:231], a[192:195], 0
dl 18,8,22,3072
v_mul_f32 v165, s26, v165
v_mul_f32 v166, s26, v166
v_mul_f32 v167, s26, v167
cv 161,162,163
cv 162,164,165
cv 163,166,167
v_add_u32 v156, s89, v19
v_permlane16_swap_b32 v152, v154
v_permlane16_swap_b32 v153, v155
ds_write_b128 v156, v[152:155] offset:16384
v_mul_f32 v168, s26, v168
v_mul_f32 v169, s26, v169
v_permlane32_swap_b32 v160, v162
v_permlane32_swap_b32 v161, v163
v_mul_f32 v170, s26, v170
v_mul_f32 v171, s26, v171
cv 168,168,169
v_mfma_f32_16x16x32_bf16 v[152:155], v[232:235], a[192:195], 0
v_mul_f32 v172, s26, v172
v_mul_f32 v173, s26, v173
v_mul_f32 v174, s26, v174
v_mul_f32 v175, s26, v175
cv 169,170,171
cv 170,172,173
cv 171,174,175
v_add_u32 v164, s89, v19
v_permlane16_swap_b32 v160, v162
v_permlane16_swap_b32 v161, v163
ds_write_b128 v164, v[160:163] offset:20480
v_mul_f32 v176, s26, v176
v_mul_f32 v177, s26, v177
v_permlane32_swap_b32 v168, v170
s_add_u32 m0, s16, 49152
v_permlane32_swap_b32 v169, v171
dn 13,60,23,0
v_mul_f32 v178, s26, v178
v_mul_f32 v179, s26, v179
cv 176,176,177
v_mfma_f32_16x16x32_bf16 v[160:163], v[232:235], a[196:199], 0
v_mul_f32 v180, s26, v180
v_mul_f32 v181, s26, v181
v_mul_f32 v182, s26, v182
v_mul_f32 v183, s26, v183
cv 177,178,179
cv 178,180,181
cv 179,182,183
v_add_u32 v172, s89, v19
v_permlane16_swap_b32 v168, v170
dn 13,64,23,1024
v_permlane16_swap_b32 v169, v171
ds_write_b128 v172, v[168:171] offset:17408
v_permlane32_swap_b32 v176, v178
v_permlane32_swap_b32 v177, v179
tr 240,22,2048
tr 242,22,8192
v_mfma_f32_16x16x32_bf16 v[168:171], v[232:235], a[200:203], 0
v_add_u32 v180, s89, v19
v_permlane16_swap_b32 v176, v178
v_permlane16_swap_b32 v177, v179
ds_write_b128 v180, v[176:179] offset:21504
v_mfma_f32_16x16x32_bf16 v[176:179], v[232:235], a[204:207], 0
dn 13,68,23,2048
s_waitcnt lgkmcnt(4)
v_mfma_f32_16x16x32_bf16 v[180:183], v[236:239], a[204:207], 0
v_mul_f32 v184, s26, v184
v_mul_f32 v185, s26, v185
v_mfma_f32_16x16x32_bf16 v[172:175], v[236:239], a[200:203], 0
v_mul_f32 v186, s26, v186
v_mul_f32 v187, s26, v187
cv 184,184,185
tr 244,23,2048
tr 246,23,8192
v_mfma_f32_16x16x32_bf16 v[164:167], v[236:239], a[196:199], 0
v_mul_f32 v188, s26, v188
v_mul_f32 v189, s26, v189
v_mul_f32 v190, s26, v190
v_mul_f32 v191, s26, v191
cv 185,186,187
cv 186,188,189
cv 187,190,191
v_mul_f32 v192, s26, v192
v_mul_f32 v193, s26, v193
v_permlane32_swap_b32 v184, v186
v_permlane32_swap_b32 v185, v187
v_mul_f32 v194, s26, v194
v_mul_f32 v195, s26, v195
cv 192,192,193
v_mfma_f32_16x16x32_bf16 v[156:159], v[236:239], a[192:195], 0
v_mul_f32 v196, s26, v196
v_mul_f32 v197, s26, v197
v_mul_f32 v198, s26, v198
s_add_u32 m0, s16, 52224
v_mul_f32 v199, s26, v199
dn 14,60,23,0
cv 193,194,195
cv 194,196,197
cv 195,198,199
v_add_u32 v188, s89, v19
v_permlane16_swap_b32 v184, v186
v_permlane16_swap_b32 v185, v187
ds_write_b128 v188, v[184:187] offset:32768
v_mul_f32 v200, s26, v200
v_mul_f32 v201, s26, v201
v_permlane32_swap_b32 v192, v194
v_permlane32_swap_b32 v193, v195
v_mul_f32 v202, s26, v202
v_mul_f32 v203, s26, v203
cv 200,200,201
dn 14,64,23,1024
s_waitcnt lgkmcnt(1)
v_mfma_f32_16x16x32_bf16 v[184:187], v[240:243], a[192:195], 0
v_mul_f32 v204, s26, v204
v_mul_f32 v205, s26, v205
v_mul_f32 v206, s26, v206
v_mul_f32 v207, s26, v207
cv 201,202,203
cv 202,204,205
cv 203,206,207
v_add_u32 v196, s89, v19
v_permlane16_swap_b32 v192, v194
v_permlane16_swap_b32 v193, v195
ds_write_b128 v196, v[192:195] offset:36864
dn 14,68,23,2048
v_mul_f32 v208, s26, v208
v_mul_f32 v209, s26, v209
v_permlane32_swap_b32 v200, v202
v_permlane32_swap_b32 v201, v203
v_mul_f32 v210, s26, v210
v_mul_f32 v211, s26, v211
cv 208,208,209
v_mfma_f32_16x16x32_bf16 v[192:195], v[240:243], a[196:199], 0
v_mul_f32 v212, s26, v212
v_mul_f32 v213, s26, v213
v_mul_f32 v214, s26, v214
v_mul_f32 v215, s26, v215
cv 209,210,211
cv 210,212,213
cv 211,214,215
v_add_u32 v204, s89, v19
v_permlane16_swap_b32 v200, v202
v_permlane16_swap_b32 v201, v203
ds_write_b128 v204, v[200:203] offset:33792
v_permlane32_swap_b32 v208, v210
v_permlane32_swap_b32 v209, v211
tr 248,22,12288
tr 250,22,18432
v_mfma_f32_16x16x32_bf16 v[200:203], v[240:243], a[200:203], 0
v_add_u32 v212, s89, v19
v_permlane16_swap_b32 v208, v210
v_permlane16_swap_b32 v209, v211
ds_write_b128 v212, v[208:211] offset:37888
v_mfma_f32_16x16x32_bf16 v[208:211], v[240:243], a[204:207], 0
tr 252,23,12288
tr 254,23,18432
v_mfma_f32_16x16x32_bf16 v[212:215], v[244:247], a[204:207], 0
tr 216,22,13312
tr 218,22,19456
v_mfma_f32_16x16x32_bf16 v[204:207], v[244:247], a[200:203], 0
tr 220,23,13312
tr 222,23,19456
v_mfma_f32_16x16x32_bf16 v[196:199], v[244:247], a[196:199], 0
tr 224,22,14336
tr 226,22,20480
v_mfma_f32_16x16x32_bf16 v[188:191], v[244:247], a[192:195], 0
s_add_u32 m0, s16, 73728
s_add_u32 s24, s18, 0
dn 11,48,24,0
tr 228,23,14336
s_waitcnt lgkmcnt(10)
v_mfma_f32_16x16x32_bf16 v[120:123], v[248:251], a[208:211], v[120:123]
tr 230,23,20480
v_add_u32 v22, 0x2000, v22
v_mfma_f32_16x16x32_bf16 v[128:131], v[248:251], a[212:215], v[128:131]
v_add_u32 v7, 0x10000, v7
v_add_u32 v23, 0x2000, v23
v_mfma_f32_16x16x32_bf16 v[136:139], v[248:251], a[216:219], v[136:139]
v_add_u32 v8, 0x10000, v8
dn 11,52,24,1024
v_mfma_f32_16x16x32_bf16 v[144:147], v[248:251], a[220:223], v[144:147]
s_waitcnt lgkmcnt(2)
v_mfma_f32_16x16x32_bf16 v[148:151], v[252:255], a[220:223], v[148:151]
s_waitcnt vmcnt(12) lgkmcnt(0)
s_barrier
v_mfma_f32_16x16x32_bf16 v[140:143], v[252:255], a[216:219], v[140:143]
rd 72,19,0
rd 232,5,24576
dn 11,56,24,2048
v_mfma_f32_16x16x32_bf16 v[132:135], v[252:255], a[212:215], v[132:135]
rd 80,19,8192
rd 236,6,24576
v_mfma_f32_16x16x32_bf16 v[124:127], v[252:255], a[208:211], v[124:127]
rd 84,19,12288
rd 76,19,4096
v_mfma_f32_16x16x32_bf16 v[152:155], v[216:219], a[208:211], v[152:155]
rd 92,19,20480
rd 240,5,25600
v_mfma_f32_16x16x32_bf16 v[160:163], v[216:219], a[212:215], v[160:163]
rd 100,19,28672
rd 244,6,25600
v_mfma_f32_16x16x32_bf16 v[168:171], v[216:219], a[216:219], v[168:171]
rd 96,19,24576
rd 88,19,16384
v_mfma_f32_16x16x32_bf16 v[176:179], v[216:219], a[220:223], v[176:179]
rd 104,19,32768
rd 248,5,26624
v_mfma_f32_16x16x32_bf16 v[180:183], v[220:223], a[220:223], v[180:183]
rd 112,19,40960
rd 252,6,26624
v_mfma_f32_16x16x32_bf16 v[172:175], v[220:223], a[216:219], v[172:175]
rd 116,19,45056
rd 108,19,36864
v_mfma_f32_16x16x32_bf16 v[164:167], v[220:223], a[212:215], v[164:167]
s_add_u32 m0, s16, 76800
rd 216,5,30720
dn 12,48,24,0
v_mfma_f32_16x16x32_bf16 v[156:159], v[220:223], a[208:211], v[156:159]
rd 220,6,30720
v_mfma_f32_16x16x32_bf16 v[184:187], v[224:227], a[208:211], v[184:187]
v_mfma_f32_16x16x32_bf16 v[192:195], v[224:227], a[212:215], v[192:195]
v_mfma_f32_16x16x32_bf16 v[200:203], v[224:227], a[216:219], v[200:203]
dn 12,52,24,1024
v_mfma_f32_16x16x32_bf16 v[208:211], v[224:227], a[220:223], v[208:211]
s_add_u32 s25, s19, 787200
rd 224,5,31744
v_mfma_f32_16x16x32_bf16 v[212:215], v[228:231], a[220:223], v[212:215]
v_mfma_f32_16x16x32_bf16 v[204:207], v[228:231], a[216:219], v[204:207]
v_mfma_f32_16x16x32_bf16 v[196:199], v[228:231], a[212:215], v[196:199]
dn 12,56,24,2048
v_mfma_f32_16x16x32_bf16 v[188:191], v[228:231], a[208:211], v[188:191]
rd 228,6,31744
s_waitcnt lgkmcnt(2)
mvz 40,232,72
mv 40,236,80
mvz 48,236,84
rd 236,5,32768
mv 48,232,76
rd 232,6,32768
mv 48,240,92
mv 48,244,100
mv 40,244,96
rd 244,5,36864
s_add_u32 m0, s16, 0
mv 40,240,88
dn 13,60,25,0
mv 40,248,104
rd 240,6,36864
mv 40,252,112
mv 48,252,116
rd 252,5,37888
mv 48,248,108
dn 13,64,25,1024
mvz 44,216,72
rd 248,6,37888
s_nop 1
v_max3_f32 v20, v40, v41, v42
mv 44,220,80
mvz 52,220,84
rd 220,5,38912
v_max3_f32 v21, v48, v49, v50
mv 52,216,76
dn 13,68,25,2048
s_waitcnt lgkmcnt(3)
mv 52,224,92
rd 216,6,38912
mv 52,228,100
mv 44,228,96
rd 228,5,43008
mv 44,224,88
rd 224,6,43008
mv 44,236,104
mv 44,232,112
mv 52,232,116
rd 232,5,44032
mv 52,236,108
rd 236,6,44032
s_add_u32 m0, s16, 3072
mvz 56,244,72
dn 14,60,25,0
mv 56,240,80
v_max3_f32 v20, v20, v43, v44
v_max3_f32 v20, v20, v45, v46
mvz 64,240,84
rd 240,5,45056
v_max3_f32 v21, v21, v51, v52
mv 64,244,76
rd 244,6,45056
v_max3_f32 v21, v21, v53, v54
s_waitcnt lgkmcnt(4)
mv 64,252,92
dn 14,64,25,1024
mv 64,248,100
mv 56,248,96
mv 56,252,88
s_waitcnt vmcnt(11) lgkmcnt(0)
s_barrier
dn 14,68,25,2048
mv 56,220,104
ds_read_b128 a[192:195], v7 offset:57344
tr 248,22,40960
mv 56,216,112
tr 250,22,47104
ds_read_b128 a[196:199], v7 offset:59392
mv 64,216,116
ds_read_b128 a[200:203], v7 offset:61440
ds_read_b128 a[204:207], v7 offset:63488
mv 64,220,108
tr 252,23,40960
tr 254,23,47104
mvz 60,228,72
tr 216,22,41984
tr 218,22,48128
mv 60,224,80
tr 220,23,41984
tr 222,23,48128
mvz 68,224,84
tr 224,22,43008
tr 226,22,49152
mv 68,228,76
tr 228,23,43008
tr 230,23,49152
mv 68,232,92
ds_read_b128 a[208:211], v8 offset:57344
ds_read_b128 a[212:215], v8 offset:59392
mv 68,236,100
ds_read_b128 a[216:219], v8 offset:61440
ds_read_b128 a[220:223], v8 offset:63488
mv 60,236,96
tr 236,22,53248
tr 238,22,59392
mv 60,232,88
s_add_u32 m0, s16, 24576
s_add_u32 s22, s18, 24576
dn 9,36,22,0
mv 60,240,104
tr 232,23,53248
tr 234,23,59392
mv 60,244,112
v_max3_f32 v20, v20, v47, v56
v_max3_f32 v20, v20, v57, v58
mv 68,244,116
tr 244,22,54272
tr 246,22,60416
mv 68,240,108
dn 9,40,22,1024
s_waitcnt lgkmcnt(6)
v_mfma_f32_16x16x32_bf16 v[120:123], v[248:251], a[192:195], v[120:123]
tr 240,23,54272
tr 242,23,60416
v_mfma_f32_16x16x32_bf16 v[128:131], v[248:251], a[196:199], v[128:131]
v_max3_f32 v20, v20, v59, v60
v_max3_f32 v20, v20, v61, v62
v_mfma_f32_16x16x32_bf16 v[136:139], v[248:251], a[200:203], v[136:139]
v_max_f32 v20, v20, v63
v_max3_f32 v21, v21, v55, v64
v_mfma_f32_16x16x32_bf16 v[144:147], v[248:251], a[204:207], v[144:147]
dn 9,44,22,2048
v_mfma_f32_16x16x32_bf16 v[148:151], v[252:255], a[204:207], v[148:151]
tr 248,22,55296
tr 250,22,61440
v_mfma_f32_16x16x32_bf16 v[140:143], v[252:255], a[200:203], v[140:143]
v_mov_b32 v32, v20
v_max3_f32 v21, v21, v65, v66
v_mfma_f32_16x16x32_bf16 v[132:135], v[252:255], a[196:199], v[132:135]
v_max3_f32 v21, v21, v67, v68
v_max3_f32 v21, v21, v69, v70
v_mfma_f32_16x16x32_bf16 v[124:127], v[252:255], a[192:195], v[124:127]
tr 252,23,55296
tr 254,23,61440
v_mfma_f32_16x16x32_bf16 v[152:155], v[216:219], a[192:195], v[152:155]
v_permlane32_swap_b32 v20, v32
v_max_f32 v21, v21, v71
v_mfma_f32_16x16x32_bf16 v[160:163], v[216:219], a[196:199], v[160:163]
v_max_f32 v20, v20, v32
v_mov_b32 v32, v20
v_mfma_f32_16x16x32_bf16 v[168:171], v[216:219], a[200:203], v[168:171]
v_mov_b32 v36, v21
v_permlane16_swap_b32 v20, v32
v_mfma_f32_16x16x32_bf16 v[176:179], v[216:219], a[204:207], v[176:179]
v_permlane32_swap_b32 v21, v36
v_max_f32 v20, v20, v32
v_mfma_f32_16x16x32_bf16 v[180:183], v[220:223], a[204:207], v[180:183]
s_waitcnt vmcnt(9) lgkmcnt(0)
s_barrier
v_mfma_f32_16x16x32_bf16 v[172:175], v[220:223], a[200:203], v[172:175]
tr 216,1,40960
tr 218,1,47104
v_mfma_f32_16x16x32_bf16 v[164:167], v[220:223], a[196:199], v[164:167]
v_max_f32 v21, v21, v36
v_mov_b32 v36, v21
v_mfma_f32_16x16x32_bf16 v[156:159], v[220:223], a[192:195], v[156:159]
tr 220,2,40960
tr 222,2,47104
v_mfma_f32_16x16x32_bf16 v[184:187], v[224:227], a[192:195], v[184:187]
v_sub_f32 v40, v40, v20
v_sub_f32 v41, v41, v20
v_mfma_f32_16x16x32_bf16 v[192:195], v[224:227], a[196:199], v[192:195]
v_sub_f32 v42, v42, v20
v_sub_f32 v43, v43, v20
v_mfma_f32_16x16x32_bf16 v[200:203], v[224:227], a[200:203], v[200:203]
v_exp_f32 v40, v40
v_mfma_f32_16x16x32_bf16 v[208:211], v[224:227], a[204:207], v[208:211]
s_add_u32 m0, s16, 27648
v_exp_f32 v41, v41
dn 10,36,22,0
v_mfma_f32_16x16x32_bf16 v[212:215], v[228:231], a[204:207], v[212:215]
v_sub_f32 v44, v44, v20
v_sub_f32 v45, v45, v20
v_mfma_f32_16x16x32_bf16 v[204:207], v[228:231], a[200:203], v[204:207]
v_sub_f32 v46, v46, v20
v_sub_f32 v47, v47, v20
v_mfma_f32_16x16x32_bf16 v[196:199], v[228:231], a[196:199], v[196:199]
v_exp_f32 v42, v42
v_mfma_f32_16x16x32_bf16 v[188:191], v[228:231], a[192:195], v[188:191]
dn 10,40,22,1024
v_mfma_f32_16x16x32_bf16 v[120:123], v[236:239], a[208:211], v[120:123]
v_exp_f32 v43, v43
v_mfma_f32_16x16x32_bf16 v[128:131], v[236:239], a[212:215], v[128:131]
v_permlane16_swap_b32 v21, v36
v_exp_f32 v44, v44
v_mfma_f32_16x16x32_bf16 v[136:139], v[236:239], a[216:219], v[136:139]
v_exp_f32 v45, v45
v_mfma_f32_16x16x32_bf16 v[144:147], v[236:239], a[220:223], v[144:147]
dn 10,44,22,2048
v_mfma_f32_16x16x32_bf16 v[148:151], v[232:235], a[220:223], v[148:151]
v_exp_f32 v46, v46
v_mfma_f32_16x16x32_bf16 v[140:143], v[232:235], a[216:219], v[140:143]
v_exp_f32 v47, v47
v_mfma_f32_16x16x32_bf16 v[132:135], v[232:235], a[212:215], v[132:135]
v_max_f32 v21, v21, v36
cv 40,40,41
v_mfma_f32_16x16x32_bf16 v[124:127], v[232:235], a[208:211], v[124:127]
v_sub_f32 v48, v48, v21
v_sub_f32 v49, v49, v21
v_mfma_f32_16x16x32_bf16 v[152:155], v[244:247], a[208:211], v[152:155]
cv 41,42,43
v_sub_f32 v50, v50, v21
v_mfma_f32_16x16x32_bf16 v[160:163], v[244:247], a[212:215], v[160:163]
v_sub_f32 v51, v51, v21
cv 42,44,45
v_mfma_f32_16x16x32_bf16 v[168:171], v[244:247], a[216:219], v[168:171]
cv 43,46,47
v_exp_f32 v48, v48
v_mfma_f32_16x16x32_bf16 v[176:179], v[244:247], a[220:223], v[176:179]
v_exp_f32 v49, v49
v_mfma_f32_16x16x32_bf16 v[180:183], v[240:243], a[220:223], v[180:183]
v_sub_f32 v52, v52, v21
v_sub_f32 v53, v53, v21
v_mfma_f32_16x16x32_bf16 v[172:175], v[240:243], a[216:219], v[172:175]
v_sub_f32 v54, v54, v21
v_sub_f32 v55, v55, v21
v_mfma_f32_16x16x32_bf16 v[164:167], v[240:243], a[212:215], v[164:167]
v_exp_f32 v50, v50
v_mfma_f32_16x16x32_bf16 v[156:159], v[240:243], a[208:211], v[156:159]
v_exp_f32 v51, v51
v_mfma_f32_16x16x32_bf16 v[184:187], v[248:251], a[208:211], v[184:187]
v_exp_f32 v52, v52
v_mfma_f32_16x16x32_bf16 v[192:195], v[248:251], a[212:215], v[192:195]
v_exp_f32 v53, v53
v_mfma_f32_16x16x32_bf16 v[200:203], v[248:251], a[216:219], v[200:203]
v_exp_f32 v54, v54
v_mfma_f32_16x16x32_bf16 v[208:211], v[248:251], a[220:223], v[208:211]
s_add_u32 m0, s17, 147456
s_add_u32 s23, s21, 256
dl 17,4,23,0
v_mfma_f32_16x16x32_bf16 v[212:215], v[252:255], a[220:223], v[212:215]
v_exp_f32 v55, v55
v_mfma_f32_16x16x32_bf16 v[204:207], v[252:255], a[216:219], v[204:207]
cv 48,48,49
tr 224,3,40960
v_mfma_f32_16x16x32_bf16 v[196:199], v[252:255], a[212:215], v[196:199]
tr 226,3,47104
cv 49,50,51
v_mfma_f32_16x16x32_bf16 v[188:191], v[252:255], a[208:211], v[188:191]
dl 18,4,23,1024
v_mfma_f32_16x16x32_bf16 a[244:247], a[240:243], v[40:43], 0
cv 50,52,53
cv 51,54,55
tr 228,4,40960
tr 230,4,47104
v_mfma_f32_16x16x32_bf16 a[248:251], a[240:243], v[48:51], 0
tr 236,1,41984
tr 238,1,48128
s_waitcnt lgkmcnt(6)
maz 0,216,40
tr 232,2,41984
dl 17,8,23,2048
maz 8,216,48
tr 234,2,48128
tr 244,3,41984
maz 4,220,40
tr 246,3,48128
tr 240,4,41984
maz 12,220,48
tr 242,4,48128
tr 248,1,43008
s_waitcnt lgkmcnt(11)
maz 16,224,40
dl 18,8,23,3072
maz 24,224,48
tr 250,1,49152
tr 252,2,43008
s_waitcnt lgkmcnt(9)
maz 20,228,40
tr 254,2,49152
tr 216,3,43008
maz 28,228,48
tr 218,3,49152
tr 220,4,43008
maz 32,236,40
tr 222,4,49152
tr 224,1,53248
maz 40,236,48
tr 226,1,59392
tr 228,2,53248
s_waitcnt lgkmcnt(10)
maz 36,232,40
s_add_u32 s24, s19, 1573248
tr 230,2,59392
maz 44,232,48
v_sub_f32 v56, v56, v20
v_sub_f32 v57, v57, v20
maz 48,244,40
v_sub_f32 v58, v58, v20
s_add_u32 m0, s16, 49152
maz 56,244,48
dn 13,60,24,0
maz 52,240,40
v_sub_f32 v59, v59, v20
v_exp_f32 v56, v56
maz 60,240,48
v_exp_f32 v57, v57
s_waitcnt lgkmcnt(1)
maz 64,248,40
v_sub_f32 v60, v60, v20
v_sub_f32 v61, v61, v20
dn 13,64,24,1024
maz 72,248,48
v_sub_f32 v62, v62, v20
v_sub_f32 v63, v63, v20
maz 68,252,40
v_exp_f32 v58, v58
maz 76,252,48
v_exp_f32 v59, v59
maz 80,216,40
dn 13,68,24,2048
maz 88,216,48
v_exp_f32 v60, v60
maz 84,220,40
v_exp_f32 v61, v61
v_exp_f32 v62, v62
v_exp_f32 v63, v63
cv 56,56,57
maz 92,220,48
v_sub_f32 v64, v64, v21
v_sub_f32 v65, v65, v21
cv 57,58,59
v_sub_f32 v66, v66, v21
v_sub_f32 v67, v67, v21
cv 58,60,61
cv 59,62,63
v_exp_f32 v64, v64
v_exp_f32 v65, v65
v_sub_f32 v68, v68, v21
v_sub_f32 v69, v69, v21
v_sub_f32 v70, v70, v21
v_sub_f32 v71, v71, v21
v_exp_f32 v66, v66
v_exp_f32 v67, v67
v_exp_f32 v68, v68
v_exp_f32 v69, v69
v_exp_f32 v70, v70
v_exp_f32 v71, v71
cv 64,64,65
tr 236,3,53248
tr 238,3,59392
v_mfma_f32_16x16x32_bf16 a[244:247], a[240:243], v[56:59], a[244:247]
cv 65,66,67
cv 66,68,69
cv 67,70,71
tr 232,4,53248
tr 234,4,59392
v_mfma_f32_16x16x32_bf16 a[248:251], a[240:243], v[64:67], a[248:251]
tr 244,1,54272
tr 246,1,60416
ma 0,224,56
tr 240,2,54272
tr 242,2,60416
ma 8,224,64
s_add_u32 m0, s16, 52224
tr 248,3,54272
dn 14,60,24,0
s_waitcnt lgkmcnt(9)
ma 4,228,56
tr 250,3,60416
tr 252,4,54272
ma 12,228,64
tr 254,4,60416
tr 216,1,55296
s_waitcnt lgkmcnt(10)
ma 16,236,56
tr 218,1,61440
tr 220,2,55296
dn 14,64,24,1024
ma 24,236,64
tr 222,2,61440
tr 224,3,55296
s_waitcnt lgkmcnt(9)
ma 20,232,56
tr 226,3,61440
tr 228,4,55296
ma 28,232,64
tr 230,4,61440
v_add_u32 v22, 0xffffe000, v22
ma 32,244,56
dn 14,68,24,2048
ma 40,244,64
v_add_u32 v23, 0xffffe000, v23
v_sub_f32 v32, 0, v20
ma 36,240,56
v_sub_f32 v33, 0, v20
v_sub_f32 v34, 0, v20
ma 44,240,64
v_sub_f32 v35, 0, v20
v_sub_f32 v36, 0, v21
s_waitcnt lgkmcnt(3)
ma 48,248,56
s_waitcnt vmcnt(16) lgkmcnt(0)
s_barrier
ma 56,248,64
tr 236,22,0
tr 238,22,6144
ma 52,252,56
tr 232,23,0
tr 234,23,6144
ma 60,252,64
tr 244,22,1024
tr 246,22,7168
ma 64,216,56
tr 240,23,1024
tr 242,23,7168
ma 72,216,64
tr 248,22,2048
tr 250,22,8192
ma 68,220,56
tr 252,23,2048
tr 254,23,8192
ma 76,220,64
s_add_u32 m0, s16, 73728
s_add_u32 s25, s18, 24576
dn 11,48,25,0
ma 80,224,56
tr 216,22,12288
tr 218,22,18432
ma 88,224,64
tr 220,23,12288
tr 222,23,18432
ma 84,228,56
tr 224,22,13312
tr 226,22,19456
ma 92,228,64
dn 11,52,25,1024
s_waitcnt lgkmcnt(8)
v_mfma_f32_16x16x32_bf16 a[96:99], v[236:239], a[192:195], a[96:99]
tr 228,23,13312
tr 230,23,19456
v_mfma_f32_16x16x32_bf16 a[104:107], v[236:239], a[196:199], a[104:107]
v_sub_f32 v37, 0, v21
v_sub_f32 v38, 0, v21
v_mfma_f32_16x16x32_bf16 a[112:115], v[236:239], a[200:203], a[112:115]
v_sub_f32 v39, 0, v21
v_mfma_f32_16x16x32_bf16 a[120:123], v[236:239], a[204:207], a[120:123]
dn 11,56,25,2048
v_mfma_f32_16x16x32_bf16 a[124:127], v[232:235], a[204:207], a[124:127]
tr 236,22,14336
tr 238,22,20480
v_mfma_f32_16x16x32_bf16 a[116:119], v[232:235], a[200:203], a[116:119]
v_add_u32 v22, 0x2000, v22
v_mfma_f32_16x16x32_bf16 a[108:111], v[232:235], a[196:199], a[108:111]
v_mfma_f32_16x16x32_bf16 a[100:103], v[232:235], a[192:195], a[100:103]
tr 232,23,14336
tr 234,23,20480
v_mfma_f32_16x16x32_bf16 a[128:131], v[244:247], a[192:195], a[128:131]
v_add_u32 v23, 0x2000, v23
v_mfma_f32_16x16x32_bf16 a[136:139], v[244:247], a[196:199], a[136:139]
v_mfma_f32_16x16x32_bf16 a[144:147], v[244:247], a[200:203], a[144:147]
v_mfma_f32_16x16x32_bf16 a[152:155], v[244:247], a[204:207], a[152:155]
v_mfma_f32_16x16x32_bf16 a[156:159], v[240:243], a[204:207], a[156:159]
s_waitcnt vmcnt(13) lgkmcnt(0)
s_barrier
v_mfma_f32_16x16x32_bf16 a[148:151], v[240:243], a[200:203], a[148:151]
rd 244,5,24576
v_mfma_f32_16x16x32_bf16 a[140:143], v[240:243], a[196:199], a[140:143]
v_mfma_f32_16x16x32_bf16 a[132:135], v[240:243], a[192:195], a[132:135]
rd 240,6,24576
v_mfma_f32_16x16x32_bf16 a[160:163], v[248:251], a[192:195], a[160:163]
v_mfma_f32_16x16x32_bf16 a[168:171], v[248:251], a[196:199], a[168:171]
v_mfma_f32_16x16x32_bf16 a[176:179], v[248:251], a[200:203], a[176:179]
v_mfma_f32_16x16x32_bf16 a[184:187], v[248:251], a[204:207], a[184:187]
s_add_u32 m0, s16, 76800
rd 248,5,25600
dn 12,48,25,0
v_mfma_f32_16x16x32_bf16 a[188:191], v[252:255], a[204:207], a[188:191]
v_mfma_f32_16x16x32_bf16 a[180:183], v[252:255], a[200:203], a[180:183]
v_mfma_f32_16x16x32_bf16 a[172:175], v[252:255], a[196:199], a[172:175]
v_mfma_f32_16x16x32_bf16 a[164:167], v[252:255], a[192:195], a[164:167]
dn 12,52,25,1024
v_mfma_f32_16x16x32_bf16 a[96:99], v[216:219], a[208:211], a[96:99]
rd 252,6,25600
v_mfma_f32_16x16x32_bf16 a[104:107], v[216:219], a[212:215], a[104:107]
v_mfma_f32_16x16x32_bf16 a[112:115], v[216:219], a[216:219], a[112:115]
v_mfma_f32_16x16x32_bf16 a[120:123], v[216:219], a[220:223], a[120:123]
dn 12,56,25,2048
v_mfma_f32_16x16x32_bf16 a[124:127], v[220:223], a[220:223], a[124:127]
rd 216,5,26624
v_mfma_f32_16x16x32_bf16 a[116:119], v[220:223], a[216:219], a[116:119]
v_mfma_f32_16x16x32_bf16 a[108:111], v[220:223], a[212:215], a[108:111]
v_mfma_f32_16x16x32_bf16 a[100:103], v[220:223], a[208:211], a[100:103]
rd 220,6,26624
v_mfma_f32_16x16x32_bf16 a[128:131], v[224:227], a[208:211], a[128:131]
v_mfma_f32_16x16x32_bf16 a[136:139], v[224:227], a[212:215], a[136:139]
v_mfma_f32_16x16x32_bf16 a[144:147], v[224:227], a[216:219], a[144:147]
v_mfma_f32_16x16x32_bf16 a[152:155], v[224:227], a[220:223], a[152:155]
rd 224,5,30720
v_mfma_f32_16x16x32_bf16 a[156:159], v[228:231], a[220:223], a[156:159]
v_mfma_f32_16x16x32_bf16 a[148:151], v[228:231], a[216:219], a[148:151]
v_mfma_f32_16x16x32_bf16 a[140:143], v[228:231], a[212:215], a[140:143]
v_mfma_f32_16x16x32_bf16 a[132:135], v[228:231], a[208:211], a[132:135]
rd 228,6,30720
v_mfma_f32_16x16x32_bf16 a[160:163], v[236:239], a[208:211], a[160:163]
v_mfma_f32_16x16x32_bf16 a[168:171], v[236:239], a[212:215], a[168:171]
v_mfma_f32_16x16x32_bf16 a[176:179], v[236:239], a[216:219], a[176:179]
v_mfma_f32_16x16x32_bf16 a[184:187], v[236:239], a[220:223], a[184:187]
s_add_u32 m0, s16, 0
s_add_u32 s22, s19, 1573632
dn 13,60,22,0
v_mfma_f32_16x16x32_bf16 a[188:191], v[232:235], a[220:223], a[188:191]
rd 236,5,31744
v_mfma_f32_16x16x32_bf16 a[180:183], v[232:235], a[216:219], a[180:183]
v_mfma_f32_16x16x32_bf16 a[172:175], v[232:235], a[212:215], a[172:175]
v_mfma_f32_16x16x32_bf16 a[164:167], v[232:235], a[208:211], a[164:167]
dn 13,64,22,1024
s_waitcnt lgkmcnt(1)
mvc 40,244,72,32
rd 232,6,31744
mv 40,240,80
mvc 48,240,84,36
rd 240,5,32768
mv 48,244,76
dn 13,68,22,2048
mv 48,248,92
rd 244,6,32768
mv 48,252,100
mv 40,252,96
rd 252,5,36864
mv 40,248,88
rd 248,6,36864
mv 40,216,104
mv 40,220,112
mv 48,220,116
rd 220,5,37888
mv 48,216,108
rd 216,6,37888
mvc 44,224,72,32
s_nop 2
v_max3_f32 v20, v40, v41, v42
mv 44,228,80
mvc 52,228,84,36
rd 228,5,38912
v_max3_f32 v20, v20, v43, v48
mv 52,224,76
rd 224,6,38912
v_max3_f32 v20, v20, v49, v50
s_waitcnt lgkmcnt(4)
mv 52,236,92
mv 52,232,100
mv 44,232,96
rd 232,5,43008
mv 44,236,88
s_add_u32 m0, s16, 3072
rd 236,6,43008
dn 14,60,22,0
mv 44,240,104
mv 44,244,112
mv 52,244,116
rd 244,5,44032
mv 52,240,108
dn 14,64,22,1024
mvc 56,252,72,32
rd 240,6,44032
s_nop 1
v_max3_f32 v20, v20, v51, v44
mv 56,248,80
v_max3_f32 v20, v20, v45, v46
mvc 64,248,84,36
rd 248,5,45056
v_max3_f32 v20, v20, v47, v52
mv 64,252,76
dn 14,68,22,2048
s_waitcnt lgkmcnt(3)
mv 64,220,92
rd 252,6,45056
v_max3_f32 v20, v20, v53, v54
mv 64,216,100
v_add_u32 v5, 0x8000, v5
v_add_u32 v6, 0x8000, v6
mv 56,216,96
mv 56,220,88
mv 56,228,104
mv 56,224,112
s_waitcnt vmcnt(12) lgkmcnt(0)
s_barrier
mv 64,224,116
ds_read_b128 a[192:195], v7 offset:57344
tr 216,22,40960
mv 64,228,108
tr 218,22,47104
ds_read_b128 a[196:199], v7 offset:59392
mvc 60,232,72,32
ds_read_b128 a[200:203], v7 offset:61440
ds_read_b128 a[204:207], v7 offset:63488
mv 60,236,80
tr 220,23,40960
tr 222,23,47104
mvc 68,236,84,36
tr 224,22,41984
tr 226,22,48128
mv 68,232,76
tr 228,23,41984
tr 230,23,48128
mv 68,244,92
tr 236,22,43008
tr 238,22,49152
mv 68,240,100
tr 232,23,43008
tr 234,23,49152
mv 60,240,96
ds_read_b128 a[208:211], v8 offset:57344
tr 240,22,53248
mv 60,244,88
s_add_u32 m0, s17, 131072
s_add_u32 s23, s21, 384
dl 17,4,23,0
mv 60,248,104
tr 242,22,59392
ds_read_b128 a[212:215], v8 offset:59392
mv 60,252,112
ds_read_b128 a[216:219], v8 offset:61440
ds_read_b128 a[220:223], v8 offset:63488
mv 68,252,116
tr 244,23,53248
tr 246,23,59392
mv 68,248,108
dl 18,4,23,1024
s_waitcnt lgkmcnt(8)
v_mfma_f32_16x16x32_bf16 v[120:123], v[216:219], a[192:195], v[120:123]
tr 252,22,54272
tr 254,22,60416
v_mfma_f32_16x16x32_bf16 v[128:131], v[216:219], a[196:199], v[128:131]
tr 248,23,54272
tr 250,23,60416
v_mfma_f32_16x16x32_bf16 v[136:139], v[216:219], a[200:203], v[136:139]
v_max3_f32 v20, v20, v55, v56
v_max3_f32 v20, v20, v57, v58
v_mfma_f32_16x16x32_bf16 v[144:147], v[216:219], a[204:207], v[144:147]
dl 17,8,23,2048
v_mfma_f32_16x16x32_bf16 v[148:151], v[220:223], a[204:207], v[148:151]
tr 216,22,55296
tr 218,22,61440
v_mfma_f32_16x16x32_bf16 v[140:143], v[220:223], a[200:203], v[140:143]
v_max3_f32 v20, v20, v59, v64
v_max3_f32 v20, v20, v65, v66
v_mfma_f32_16x16x32_bf16 v[132:135], v[220:223], a[196:199], v[132:135]
v_max3_f32 v20, v20, v67, v60
v_max3_f32 v20, v20, v61, v62
v_mfma_f32_16x16x32_bf16 v[124:127], v[220:223], a[192:195], v[124:127]
dl 18,8,23,3072
v_mfma_f32_16x16x32_bf16 v[152:155], v[224:227], a[192:195], v[152:155]
tr 220,23,55296
tr 222,23,61440
v_mfma_f32_16x16x32_bf16 v[160:163], v[224:227], a[196:199], v[160:163]
v_max3_f32 v20, v20, v63, v68
v_max3_f32 v20, v20, v69, v70
v_mfma_f32_16x16x32_bf16 v[168:171], v[224:227], a[200:203], v[168:171]
v_max_f32 v20, v20, v71
v_cmp_lt_f32 vcc, s27, v20
s_cbranch_vccz Lc0
s_swappc_b64 s[30:31], s[28:29]
Lc0:
v_mfma_f32_16x16x32_bf16 v[176:179], v[224:227], a[204:207], v[176:179]
s_add_u32 s24, s19, 2359680
v_exp_f32 v40, v40
v_mfma_f32_16x16x32_bf16 v[180:183], v[228:231], a[204:207], v[180:183]
v_exp_f32 v41, v41
v_mfma_f32_16x16x32_bf16 v[172:175], v[228:231], a[200:203], v[172:175]
s_waitcnt vmcnt(10) lgkmcnt(0)
s_barrier
v_mfma_f32_16x16x32_bf16 v[164:167], v[228:231], a[196:199], v[164:167]
tr 224,1,40960
tr 226,1,47104
v_mfma_f32_16x16x32_bf16 v[156:159], v[228:231], a[192:195], v[156:159]
s_add_u32 m0, s16, 24576
tr 228,2,40960
dn 13,60,24,0
v_mfma_f32_16x16x32_bf16 v[184:187], v[236:239], a[192:195], v[184:187]
tr 230,2,47104
v_exp_f32 v42, v42
v_mfma_f32_16x16x32_bf16 v[192:195], v[236:239], a[196:199], v[192:195]
v_exp_f32 v43, v43
v_mfma_f32_16x16x32_bf16 v[200:203], v[236:239], a[200:203], v[200:203]
v_exp_f32 v44, v44
v_mfma_f32_16x16x32_bf16 v[208:211], v[236:239], a[204:207], v[208:211]
dn 13,64,24,1024
v_mfma_f32_16x16x32_bf16 v[212:215], v[232:235], a[204:207], v[212:215]
v_exp_f32 v45, v45
v_mfma_f32_16x16x32_bf16 v[204:207], v[232:235], a[200:203], v[204:207]
v_exp_f32 v46, v46
v_mfma_f32_16x16x32_bf16 v[196:199], v[232:235], a[196:199], v[196:199]
v_exp_f32 v47, v47
v_mfma_f32_16x16x32_bf16 v[188:191], v[232:235], a[192:195], v[188:191]
dn 13,68,24,2048
v_mfma_f32_16x16x32_bf16 v[120:123], v[240:243], a[208:211], v[120:123]
cv 40,40,41
cv 41,42,43
v_mfma_f32_16x16x32_bf16 v[128:131], v[240:243], a[212:215], v[128:131]
cv 42,44,45
cv 43,46,47
v_mfma_f32_16x16x32_bf16 v[136:139], v[240:243], a[216:219], v[136:139]
v_exp_f32 v48, v48
v_mfma_f32_16x16x32_bf16 v[144:147], v[240:243], a[220:223], v[144:147]
v_exp_f32 v49, v49
v_mfma_f32_16x16x32_bf16 v[148:151], v[244:247], a[220:223], v[148:151]
v_exp_f32 v50, v50
v_mfma_f32_16x16x32_bf16 v[140:143], v[244:247], a[216:219], v[140:143]
v_exp_f32 v51, v51
v_mfma_f32_16x16x32_bf16 v[132:135], v[244:247], a[212:215], v[132:135]
v_exp_f32 v52, v52
v_mfma_f32_16x16x32_bf16 v[124:127], v[244:247], a[208:211], v[124:127]
v_exp_f32 v53, v53
v_mfma_f32_16x16x32_bf16 v[152:155], v[252:255], a[208:211], v[152:155]
s_add_u32 m0, s16, 27648
v_exp_f32 v54, v54
dn 14,60,24,0
v_mfma_f32_16x16x32_bf16 v[160:163], v[252:255], a[212:215], v[160:163]
v_exp_f32 v55, v55
v_mfma_f32_16x16x32_bf16 v[168:171], v[252:255], a[216:219], v[168:171]
cv 48,48,49
tr 236,3,40960
v_mfma_f32_16x16x32_bf16 v[176:179], v[252:255], a[220:223], v[176:179]
tr 238,3,47104
cv 49,50,51
v_mfma_f32_16x16x32_bf16 v[180:183], v[248:251], a[220:223], v[180:183]
dn 14,64,24,1024
v_mfma_f32_16x16x32_bf16 v[172:175], v[248:251], a[216:219], v[172:175]
cv 50,52,53
cv 51,54,55
v_mfma_f32_16x16x32_bf16 v[164:167], v[248:251], a[212:215], v[164:167]
tr 232,4,40960
tr 234,4,47104
v_mfma_f32_16x16x32_bf16 v[156:159], v[248:251], a[208:211], v[156:159]
tr 240,1,41984
tr 242,1,48128
v_mfma_f32_16x16x32_bf16 v[184:187], v[216:219], a[208:211], v[184:187]
dn 14,68,24,2048
v_mfma_f32_16x16x32_bf16 v[192:195], v[216:219], a[212:215], v[192:195]
tr 244,2,41984
tr 246,2,48128
v_mfma_f32_16x16x32_bf16 v[200:203], v[216:219], a[216:219], v[200:203]
tr 252,3,41984
tr 254,3,48128
v_mfma_f32_16x16x32_bf16 v[208:211], v[216:219], a[220:223], v[208:211]
s_add_u32 s25, s18, 49152
tr 248,4,41984
v_mfma_f32_16x16x32_bf16 v[212:215], v[220:223], a[220:223], v[212:215]
tr 250,4,48128
tr 216,1,43008
v_mfma_f32_16x16x32_bf16 v[204:207], v[220:223], a[216:219], v[204:207]
tr 218,1,49152
v_exp_f32 v56, v56
v_mfma_f32_16x16x32_bf16 v[196:199], v[220:223], a[212:215], v[196:199]
v_exp_f32 v57, v57
v_mfma_f32_16x16x32_bf16 v[188:191], v[220:223], a[208:211], v[188:191]
tr 220,2,43008
tr 222,2,49152
v_mfma_f32_16x16x32_bf16 a[244:247], a[240:243], v[40:43], a[244:247]
v_exp_f32 v58, v58
s_add_u32 m0, s16, 49152
v_mfma_f32_16x16x32_bf16 a[248:251], a[240:243], v[48:51], a[248:251]
dn 9,36,25,0
s_waitcnt lgkmcnt(5)
ma 0,224,40
v_exp_f32 v59, v59
ma 8,224,48
tr 224,3,43008
tr 226,3,49152
ma 4,228,40
v_exp_f32 v60, v60
ma 12,228,48
dn 9,40,25,1024
ma 16,236,40
tr 228,4,43008
tr 230,4,49152
ma 24,236,48
tr 236,1,53248
tr 238,1,59392
ma 20,232,40
v_exp_f32 v61, v61
ma 28,232,48
dn 9,44,25,2048
ma 32,240,40
tr 232,2,53248
tr 234,2,59392
ma 40,240,48
v_exp_f32 v62, v62
ma 36,244,40
v_exp_f32 v63, v63
ma 44,244,48
cv 56,56,57
cv 57,58,59
ma 48,252,40
cv 58,60,61
cv 59,62,63
ma 56,252,48
v_exp_f32 v64, v64
s_waitcnt lgkmcnt(2)
ma 52,248,40
v_exp_f32 v65, v65
ma 60,248,48
v_exp_f32 v66, v66
ma 64,216,40
v_exp_f32 v67, v67
ma 72,216,48
v_exp_f32 v68, v68
ma 68,220,40
v_exp_f32 v69, v69
ma 76,220,48
s_add_u32 m0, s16, 52224
v_exp_f32 v70, v70
dn 10,36,25,0
ma 80,224,40
v_exp_f32 v71, v71
ma 88,224,48
cv 64,64,65
tr 240,3,53248
ma 84,228,40
tr 242,3,59392
cv 65,66,67
ma 92,228,48
dn 10,40,25,1024
v_mfma_f32_16x16x32_bf16 a[244:247], a[240:243], v[56:59], a[244:247]
cv 66,68,69
cv 67,70,71
tr 244,4,53248
tr 246,4,59392
v_mfma_f32_16x16x32_bf16 a[248:251], a[240:243], v[64:67], a[248:251]
tr 252,1,54272
tr 254,1,60416
ma 0,236,56
tr 248,2,54272
tr 250,2,60416
dn 10,44,25,2048
ma 8,236,64
tr 216,3,54272
tr 218,3,60416
s_waitcnt lgkmcnt(8)
ma 4,232,56
tr 220,4,54272
tr 222,4,60416
ma 12,232,64
tr 224,1,55296
tr 226,1,61440
ma 16,240,56
tr 228,2,55296
tr 230,2,61440
ma 24,240,64
tr 236,3,55296
tr 238,3,61440
s_waitcnt lgkmcnt(11)
ma 20,244,56
tr 232,4,55296
tr 234,4,61440
ma 28,244,64
v_add_u32 v22, 0xffffe000, v22
v_add_u32 v23, 0xffffe000, v23
ma 32,252,56
v_add_u32 v1, 0xffff8000, v1
v_add_u32 v2, 0xffff8000, v2
ma 40,252,64
v_add_u32 v3, 0xffff8000, v3
v_add_u32 v4, 0xffff8000, v4
s_waitcnt lgkmcnt(6)
ma 36,248,56
ma 44,248,64
s_waitcnt vmcnt(16) lgkmcnt(0)
s_barrier
ma 48,216,56
tr 240,22,0
tr 242,22,6144
ma 56,216,64
tr 244,23,0
tr 246,23,6144
ma 52,220,56
tr 252,22,1024
tr 254,22,7168
ma 60,220,64
tr 248,23,1024
tr 250,23,7168
ma 64,224,56
tr 216,22,2048
tr 218,22,8192
ma 72,224,64
tr 220,23,2048
tr 222,23,8192
ma 68,228,56
tr 224,22,12288
tr 226,22,18432
ma 76,228,64
s_add_u32 m0, s16, 73728
s_add_u32 s22, s19, 2360064
dn 13,60,22,0
ma 80,236,56
tr 228,23,12288
tr 230,23,18432
ma 88,236,64
tr 236,22,13312
tr 238,22,19456
ma 84,232,56
ma 92,232,64
dn 13,64,22,1024
s_waitcnt lgkmcnt(6)
v_mfma_f32_16x16x32_bf16 a[96:99], v[240:243], a[192:195], a[96:99]
tr 232,23,13312
tr 234,23,19456
v_mfma_f32_16x16x32_bf16 a[104:107], v[240:243], a[196:199], a[104:107]
v_mfma_f32_16x16x32_bf16 a[112:115], v[240:243], a[200:203], a[112:115]
v_mfma_f32_16x16x32_bf16 a[120:123], v[240:243], a[204:207], a[120:123]
dn 13,68,22,2048
v_mfma_f32_16x16x32_bf16 a[124:127], v[244:247], a[204:207], a[124:127]
tr 240,22,14336
tr 242,22,20480
v_mfma_f32_16x16x32_bf16 a[116:119], v[244:247], a[200:203], a[116:119]
v_mfma_f32_16x16x32_bf16 a[108:111], v[244:247], a[196:199], a[108:111]
v_mfma_f32_16x16x32_bf16 a[100:103], v[244:247], a[192:195], a[100:103]
tr 244,23,14336
tr 246,23,20480
v_mfma_f32_16x16x32_bf16 a[128:131], v[252:255], a[192:195], a[128:131]
v_mfma_f32_16x16x32_bf16 a[136:139], v[252:255], a[196:199], a[136:139]
v_mfma_f32_16x16x32_bf16 a[144:147], v[252:255], a[200:203], a[144:147]
v_mfma_f32_16x16x32_bf16 a[152:155], v[252:255], a[204:207], a[152:155]
v_mfma_f32_16x16x32_bf16 a[156:159], v[248:251], a[204:207], a[156:159]
s_waitcnt vmcnt(9) lgkmcnt(0)
s_barrier
v_mfma_f32_16x16x32_bf16 a[148:151], v[248:251], a[200:203], a[148:151]
tr 252,22,24576
tr 254,22,30720
v_mfma_f32_16x16x32_bf16 a[140:143], v[248:251], a[196:199], a[140:143]
v_mfma_f32_16x16x32_bf16 a[132:135], v[248:251], a[192:195], a[132:135]
tr 248,23,24576
tr 250,23,30720
v_mfma_f32_16x16x32_bf16 a[160:163], v[216:219], a[192:195], a[160:163]
v_mfma_f32_16x16x32_bf16 a[168:171], v[216:219], a[196:199], a[168:171]
v_mfma_f32_16x16x32_bf16 a[176:179], v[216:219], a[200:203], a[176:179]
v_mfma_f32_16x16x32_bf16 a[184:187], v[216:219], a[204:207], a[184:187]
s_add_u32 m0, s16, 76800
tr 216,22,25600
dn 14,60,22,0
v_mfma_f32_16x16x32_bf16 a[188:191], v[220:223], a[204:207], a[188:191]
ds_read_b128 a[204:207], v7 offset:47104
tr 218,22,31744
v_mfma_f32_16x16x32_bf16 a[180:183], v[220:223], a[200:203], a[180:183]
ds_read_b128 a[200:203], v7 offset:45056
v_mfma_f32_16x16x32_bf16 a[172:175], v[220:223], a[196:199], a[172:175]
ds_read_b128 a[196:199], v7 offset:43008
v_mfma_f32_16x16x32_bf16 a[164:167], v[220:223], a[192:195], a[164:167]
dn 14,64,22,1024
v_mfma_f32_16x16x32_bf16 a[96:99], v[224:227], a[208:211], a[96:99]
ds_read_b128 a[192:195], v7 offset:40960
tr 220,23,25600
v_mfma_f32_16x16x32_bf16 a[104:107], v[224:227], a[212:215], a[104:107]
tr 222,23,31744
v_mfma_f32_16x16x32_bf16 a[112:115], v[224:227], a[216:219], a[112:115]
v_mfma_f32_16x16x32_bf16 a[120:123], v[224:227], a[220:223], a[120:123]
dn 14,68,22,2048
v_mfma_f32_16x16x32_bf16 a[124:127], v[228:231], a[220:223], a[124:127]
tr 224,22,26624
tr 226,22,32768
v_mfma_f32_16x16x32_bf16 a[116:119], v[228:231], a[216:219], a[116:119]
v_mfma_f32_16x16x32_bf16 a[108:111], v[228:231], a[212:215], a[108:111]
v_mfma_f32_16x16x32_bf16 a[100:103], v[228:231], a[208:211], a[100:103]
tr 228,23,26624
tr 230,23,32768
v_mfma_f32_16x16x32_bf16 a[128:131], v[236:239], a[208:211], a[128:131]
v_mfma_f32_16x16x32_bf16 a[136:139], v[236:239], a[212:215], a[136:139]
v_mfma_f32_16x16x32_bf16 a[144:147], v[236:239], a[216:219], a[144:147]
v_mfma_f32_16x16x32_bf16 a[152:155], v[236:239], a[220:223], a[152:155]
tr 236,22,36864
tr 238,22,43008
v_mfma_f32_16x16x32_bf16 a[156:159], v[232:235], a[220:223], a[156:159]
v_mfma_f32_16x16x32_bf16 a[148:151], v[232:235], a[216:219], a[148:151]
v_mfma_f32_16x16x32_bf16 a[140:143], v[232:235], a[212:215], a[140:143]
v_mfma_f32_16x16x32_bf16 a[132:135], v[232:235], a[208:211], a[132:135]
tr 232,23,36864
tr 234,23,43008
v_mfma_f32_16x16x32_bf16 a[160:163], v[240:243], a[208:211], a[160:163]
v_mfma_f32_16x16x32_bf16 a[168:171], v[240:243], a[212:215], a[168:171]
v_mfma_f32_16x16x32_bf16 a[176:179], v[240:243], a[216:219], a[176:179]
v_mfma_f32_16x16x32_bf16 a[184:187], v[240:243], a[220:223], a[184:187]
s_add_u32 m0, s16, 0
s_add_u32 s23, s18, 49152
dn 11,48,23,0
v_mfma_f32_16x16x32_bf16 a[188:191], v[244:247], a[220:223], a[188:191]
ds_read_b128 a[220:223], v8 offset:47104
tr 240,22,37888
v_mfma_f32_16x16x32_bf16 a[180:183], v[244:247], a[216:219], a[180:183]
ds_read_b128 a[216:219], v8 offset:45056
tr 242,22,44032
v_mfma_f32_16x16x32_bf16 a[172:175], v[244:247], a[212:215], a[172:175]
ds_read_b128 a[212:215], v8 offset:43008
v_mfma_f32_16x16x32_bf16 a[164:167], v[244:247], a[208:211], a[164:167]
dn 11,52,23,1024
s_waitcnt lgkmcnt(5)
v_mfma_f32_16x16x32_bf16 v[120:123], v[252:255], a[192:195], v[120:123]
ds_read_b128 a[208:211], v8 offset:40960
tr 244,23,37888
v_mfma_f32_16x16x32_bf16 v[128:131], v[252:255], a[196:199], v[128:131]
tr 246,23,44032
v_mfma_f32_16x16x32_bf16 v[136:139], v[252:255], a[200:203], v[136:139]
v_mfma_f32_16x16x32_bf16 v[144:147], v[252:255], a[204:207], v[144:147]
dn 11,56,23,2048
v_mfma_f32_16x16x32_bf16 v[148:151], v[248:251], a[204:207], v[148:151]
tr 252,22,38912
tr 254,22,45056
v_mfma_f32_16x16x32_bf16 v[140:143], v[248:251], a[200:203], v[140:143]
v_add_u32 v22, 0x8000, v22
v_mfma_f32_16x16x32_bf16 v[132:135], v[248:251], a[196:199], v[132:135]
v_mfma_f32_16x16x32_bf16 v[124:127], v[248:251], a[192:195], v[124:127]
tr 248,23,38912
tr 250,23,45056
v_mfma_f32_16x16x32_bf16 v[152:155], v[216:219], a[192:195], v[152:155]
v_add_u32 v23, 0x8000, v23
v_mfma_f32_16x16x32_bf16 v[160:163], v[216:219], a[196:199], v[160:163]
v_mfma_f32_16x16x32_bf16 v[168:171], v[216:219], a[200:203], v[168:171]
v_mfma_f32_16x16x32_bf16 v[176:179], v[216:219], a[204:207], v[176:179]
v_mfma_f32_16x16x32_bf16 v[180:183], v[220:223], a[204:207], v[180:183]
s_waitcnt vmcnt(9) lgkmcnt(0)
s_barrier
v_mfma_f32_16x16x32_bf16 v[172:175], v[220:223], a[200:203], v[172:175]
rd 216,5,16384
v_mfma_f32_16x16x32_bf16 v[164:167], v[220:223], a[196:199], v[164:167]
v_mfma_f32_16x16x32_bf16 v[156:159], v[220:223], a[192:195], v[156:159]
rd 220,6,16384
v_mfma_f32_16x16x32_bf16 v[184:187], v[224:227], a[192:195], v[184:187]
v_mfma_f32_16x16x32_bf16 v[192:195], v[224:227], a[196:199], v[192:195]
v_mfma_f32_16x16x32_bf16 v[200:203], v[224:227], a[200:203], v[200:203]
v_mfma_f32_16x16x32_bf16 v[208:211], v[224:227], a[204:207], v[208:211]
s_add_u32 m0, s16, 3072
rd 224,5,17408
dn 12,48,23,0
v_mfma_f32_16x16x32_bf16 v[212:215], v[228:231], a[204:207], v[212:215]
v_mfma_f32_16x16x32_bf16 v[204:207], v[228:231], a[200:203], v[204:207]
v_mfma_f32_16x16x32_bf16 v[196:199], v[228:231], a[196:199], v[196:199]
v_mfma_f32_16x16x32_bf16 v[188:191], v[228:231], a[192:195], v[188:191]
dn 12,52,23,1024
v_mfma_f32_16x16x32_bf16 v[120:123], v[236:239], a[208:211], v[120:123]
rd 228,6,17408
v_mfma_f32_16x16x32_bf16 v[128:131], v[236:239], a[212:215], v[128:131]
v_mfma_f32_16x16x32_bf16 v[136:139], v[236:239], a[216:219], v[136:139]
v_mfma_f32_16x16x32_bf16 v[144:147], v[236:239], a[220:223], v[144:147]
dn 12,56,23,2048
v_mfma_f32_16x16x32_bf16 v[148:151], v[232:235], a[220:223], v[148:151]
rd 236,5,18432
v_mfma_f32_16x16x32_bf16 v[140:143], v[232:235], a[216:219], v[140:143]
v_mfma_f32_16x16x32_bf16 v[132:135], v[232:235], a[212:215], v[132:135]
v_mfma_f32_16x16x32_bf16 v[124:127], v[232:235], a[208:211], v[124:127]
rd 232,6,18432
v_mfma_f32_16x16x32_bf16 v[152:155], v[240:243], a[208:211], v[152:155]
v_mfma_f32_16x16x32_bf16 v[160:163], v[240:243], a[212:215], v[160:163]
v_mfma_f32_16x16x32_bf16 v[168:171], v[240:243], a[216:219], v[168:171]
v_mfma_f32_16x16x32_bf16 v[176:179], v[240:243], a[220:223], v[176:179]
rd 240,5,22528
v_mfma_f32_16x16x32_bf16 v[180:183], v[244:247], a[220:223], v[180:183]
v_mfma_f32_16x16x32_bf16 v[172:175], v[244:247], a[216:219], v[172:175]
v_mfma_f32_16x16x32_bf16 v[164:167], v[244:247], a[212:215], v[164:167]
v_mfma_f32_16x16x32_bf16 v[156:159], v[244:247], a[208:211], v[156:159]
rd 244,6,22528
v_mfma_f32_16x16x32_bf16 v[184:187], v[252:255], a[208:211], v[184:187]
v_mfma_f32_16x16x32_bf16 v[192:195], v[252:255], a[212:215], v[192:195]
v_mfma_f32_16x16x32_bf16 v[200:203], v[252:255], a[216:219], v[200:203]
v_mfma_f32_16x16x32_bf16 v[208:211], v[252:255], a[220:223], v[208:211]
s_add_u32 m0, s17, 147456
s_add_u32 s24, s21, 512
dl 17,4,24,0
v_mfma_f32_16x16x32_bf16 v[212:215], v[248:251], a[220:223], v[212:215]
rd 252,5,23552
v_mfma_f32_16x16x32_bf16 v[204:207], v[248:251], a[216:219], v[204:207]
v_mfma_f32_16x16x32_bf16 v[196:199], v[248:251], a[212:215], v[196:199]
v_mfma_f32_16x16x32_bf16 v[188:191], v[248:251], a[208:211], v[188:191]
dl 18,4,24,1024
s_waitcnt lgkmcnt(1)
mvc 40,216,72,32
rd 248,6,23552
mv 40,220,80
mvc 48,220,84,36
rd 220,5,24576
mv 48,216,76
dl 17,8,24,2048
mv 48,224,92
rd 216,6,24576
mv 48,228,100
mv 40,228,96
rd 228,5,28672
mv 40,224,88
dl 18,8,24,3072
mv 40,236,104
rd 224,6,28672
mv 40,232,112
mv 48,232,116
rd 232,5,29696
mv 48,236,108
s_add_u32 s25, s19, 3146112
rd 236,6,29696
mvc 44,240,72,32
s_nop 1
v_max3_f32 v20, v40, v41, v42
mv 44,244,80
mvc 52,244,84,36
rd 244,5,30720
v_max3_f32 v20, v20, v43, v48
mv 52,240,76
rd 240,6,30720
v_max3_f32 v20, v20, v49, v50
s_waitcnt lgkmcnt(4)
mv 52,252,92
s_add_u32 m0, s16, 24576
s_nop 0
dn 13,60,25,0
mv 52,248,100
mv 44,248,96
rd 248,5,34816
mv 44,252,88
rd 252,6,34816
mv 44,220,104
dn 13,64,25,1024
mv 44,216,112
mv 52,216,116
rd 216,5,35840
mv 52,220,108
rd 220,6,35840
mvc 56,228,72,32
dn 13,68,25,2048
mv 56,224,80
s_nop 0
v_max3_f32 v20, v20, v51, v44
v_max3_f32 v20, v20, v45, v46
mvc 64,224,84,36
rd 224,5,36864
v_max3_f32 v20, v20, v47, v52
mv 64,228,76
rd 228,6,36864
v_max3_f32 v20, v20, v53, v54
s_waitcnt lgkmcnt(4)
mv 64,232,92
mv 64,236,100
mv 56,236,96
mv 56,232,88
s_waitcnt vmcnt(13) lgkmcnt(0)
s_barrier
mv 56,244,104
s_add_u32 m0, s16, 27648
tr 236,22,40960
dn 14,60,25,0
mv 56,240,112
tr 238,22,47104
tr 232,23,40960
mv 64,240,116
tr 234,23,47104
tr 240,22,41984
mv 64,244,108
tr 242,22,48128
tr 244,23,41984
mvc 60,248,72,32
dn 14,64,25,1024
mv 60,252,80
tr 246,23,48128
v_max3_f32 v20, v20, v55, v56
mvc 68,252,84,36
tr 252,22,43008
tr 254,22,49152
mv 68,248,76
tr 248,23,43008
tr 250,23,49152
mv 68,216,92
dn 14,68,25,2048
mv 68,220,100
v_max3_f32 v20, v20, v57, v58
v_max3_f32 v20, v20, v59, v64
mv 60,220,96
tr 220,22,53248
tr 222,22,59392
mv 60,216,88
s_add_u32 s22, s18, 73728
tr 216,23,53248
mv 60,224,104
tr 218,23,59392
v_max3_f32 v20, v20, v65, v66
mv 60,228,112
mv 68,228,116
tr 228,22,54272
tr 230,22,60416
mv 68,224,108
tr 224,23,54272
tr 226,23,60416
s_waitcnt lgkmcnt(8)
v_mfma_f32_16x16x32_bf16 a[96:99], v[236:239], a[192:195], a[96:99]
v_max3_f32 v20, v20, v67, v60
v_max3_f32 v20, v20, v61, v62
s_add_u32 m0, s16, 49152
v_mfma_f32_16x16x32_bf16 a[104:107], v[236:239], a[196:199], a[104:107]
dn 9,36,22,0
v_mfma_f32_16x16x32_bf16 a[112:115], v[236:239], a[200:203], a[112:115]
v_max3_f32 v20, v20, v63, v68
v_max3_f32 v20, v20, v69, v70
v_mfma_f32_16x16x32_bf16 a[120:123], v[236:239], a[204:207], a[120:123]
tr 236,22,55296
tr 238,22,61440
v_mfma_f32_16x16x32_bf16 a[124:127], v[232:235], a[204:207], a[124:127]
v_max_f32 v20, v20, v71
v_cmp_lt_f32 vcc, s27, v20
s_cbranch_vccz Lc1
s_swappc_b64 s[30:31], s[28:29]
Lc1:
v_mfma_f32_16x16x32_bf16 a[116:119], v[232:235], a[200:203], a[116:119]
dn 9,40,22,1024
v_mfma_f32_16x16x32_bf16 a[108:111], v[232:235], a[196:199], a[108:111]
v_exp_f32 v40, v40
v_mfma_f32_16x16x32_bf16 a[100:103], v[232:235], a[192:195], a[100:103]
tr 232,23,55296
tr 234,23,61440
v_mfma_f32_16x16x32_bf16 a[128:131], v[240:243], a[192:195], a[128:131]
v_exp_f32 v41, v41
v_mfma_f32_16x16x32_bf16 a[136:139], v[240:243], a[196:199], a[136:139]
dn 9,44,22,2048
v_mfma_f32_16x16x32_bf16 a[144:147], v[240:243], a[200:203], a[144:147]
v_exp_f32 v42, v42
v_mfma_f32_16x16x32_bf16 a[152:155], v[240:243], a[204:207], a[152:155]
v_exp_f32 v43, v43
v_mfma_f32_16x16x32_bf16 a[156:159], v[244:247], a[204:207], a[156:159]
s_waitcnt vmcnt(13) lgkmcnt(0)
s_barrier
v_mfma_f32_16x16x32_bf16 a[148:151], v[244:247], a[200:203], a[148:151]
tr 240,1,0
tr 242,1,6144
v_mfma_f32_16x16x32_bf16 a[140:143], v[244:247], a[196:199], a[140:143]
v_exp_f32 v44, v44
v_mfma_f32_16x16x32_bf16 a[132:135], v[244:247], a[192:195], a[132:135]
tr 244,2,0
tr 246,2,6144
v_mfma_f32_16x16x32_bf16 a[160:163], v[252:255], a[192:195], a[160:163]
v_exp_f32 v45, v45
v_mfma_f32_16x16x32_bf16 a[168:171], v[252:255], a[196:199], a[168:171]
v_exp_f32 v46, v46
v_mfma_f32_16x16x32_bf16 a[176:179], v[252:255], a[200:203], a[176:179]
v_exp_f32 v47, v47
v_mfma_f32_16x16x32_bf16 a[184:187], v[252:255], a[204:207], a[184:187]
s_add_u32 m0, s16, 52224
cv 40,40,41
dn 10,36,22,0
)"
R"(v_mfma_f32_16x16x32_bf16 a[188:191], v[248:251], a[204:207], a[188:191]
cv 41,42,43
cv 42,44,45
v_mfma_f32_16x16x32_bf16 a[180:183], v[248:251], a[200:203], a[180:183]
cv 43,46,47
v_exp_f32 v48, v48
v_mfma_f32_16x16x32_bf16 a[172:175], v[248:251], a[196:199], a[172:175]
v_exp_f32 v49, v49
v_mfma_f32_16x16x32_bf16 a[164:167], v[248:251], a[192:195], a[164:167]
dn 10,40,22,1024
v_mfma_f32_16x16x32_bf16 a[96:99], v[220:223], a[208:211], a[96:99]
v_exp_f32 v50, v50
v_mfma_f32_16x16x32_bf16 a[104:107], v[220:223], a[212:215], a[104:107]
v_exp_f32 v51, v51
v_mfma_f32_16x16x32_bf16 a[112:115], v[220:223], a[216:219], a[112:115]
v_exp_f32 v52, v52
v_mfma_f32_16x16x32_bf16 a[120:123], v[220:223], a[220:223], a[120:123]
dn 10,44,22,2048
v_mfma_f32_16x16x32_bf16 a[124:127], v[216:219], a[220:223], a[124:127]
v_exp_f32 v53, v53
v_mfma_f32_16x16x32_bf16 a[116:119], v[216:219], a[216:219], a[116:119]
v_exp_f32 v54, v54
v_mfma_f32_16x16x32_bf16 a[108:111], v[216:219], a[212:215], a[108:111]
v_exp_f32 v55, v55
v_mfma_f32_16x16x32_bf16 a[100:103], v[216:219], a[208:211], a[100:103]
cv 48,48,49
tr 252,3,0
v_mfma_f32_16x16x32_bf16 a[128:131], v[228:231], a[208:211], a[128:131]
tr 254,3,6144
cv 49,50,51
v_mfma_f32_16x16x32_bf16 a[136:139], v[228:231], a[212:215], a[136:139]
cv 50,52,53
cv 51,54,55
v_mfma_f32_16x16x32_bf16 a[144:147], v[228:231], a[216:219], a[144:147]
tr 248,4,0
tr 250,4,6144
v_mfma_f32_16x16x32_bf16 a[152:155], v[228:231], a[220:223], a[152:155]
tr 220,1,1024
tr 222,1,7168
v_mfma_f32_16x16x32_bf16 a[156:159], v[224:227], a[220:223], a[156:159]
tr 216,2,1024
tr 218,2,7168
v_mfma_f32_16x16x32_bf16 a[148:151], v[224:227], a[216:219], a[148:151]
tr 228,3,1024
tr 230,3,7168
v_mfma_f32_16x16x32_bf16 a[140:143], v[224:227], a[212:215], a[140:143]
v_exp_f32 v56, v56
v_mfma_f32_16x16x32_bf16 a[132:135], v[224:227], a[208:211], a[132:135]
tr 224,4,1024
tr 226,4,7168
v_mfma_f32_16x16x32_bf16 a[160:163], v[236:239], a[208:211], a[160:163]
v_exp_f32 v57, v57
v_mfma_f32_16x16x32_bf16 a[168:171], v[236:239], a[212:215], a[168:171]
v_exp_f32 v58, v58
v_mfma_f32_16x16x32_bf16 a[176:179], v[236:239], a[216:219], a[176:179]
v_exp_f32 v59, v59
v_mfma_f32_16x16x32_bf16 a[184:187], v[236:239], a[220:223], a[184:187]
s_add_u32 m0, s16, 73728
s_add_u32 s23, s19, 3146496
dn 13,60,23,0
v_mfma_f32_16x16x32_bf16 a[188:191], v[232:235], a[220:223], a[188:191]
tr 236,1,2048
tr 238,1,8192
v_mfma_f32_16x16x32_bf16 a[180:183], v[232:235], a[216:219], a[180:183]
v_exp_f32 v60, v60
v_mfma_f32_16x16x32_bf16 a[172:175], v[232:235], a[212:215], a[172:175]
v_exp_f32 v61, v61
v_mfma_f32_16x16x32_bf16 a[164:167], v[232:235], a[208:211], a[164:167]
dn 13,64,23,1024
v_mfma_f32_16x16x32_bf16 a[244:247], a[240:243], v[40:43], a[244:247]
tr 232,2,2048
tr 234,2,8192
v_mfma_f32_16x16x32_bf16 a[248:251], a[240:243], v[48:51], a[248:251]
v_exp_f32 v62, v62
s_waitcnt lgkmcnt(4)
ma 0,240,40
v_exp_f32 v63, v63
ma 8,240,48
dn 13,68,23,2048
ma 4,244,40
tr 240,3,2048
tr 242,3,8192
ma 12,244,48
tr 244,4,2048
tr 246,4,8192
ma 16,252,40
cv 56,56,57
cv 57,58,59
ma 24,252,48
tr 252,1,12288
tr 254,1,18432
ma 20,248,40
cv 58,60,61
cv 59,62,63
ma 28,248,48
tr 248,2,12288
tr 250,2,18432
ma 32,220,40
v_exp_f32 v64, v64
ma 40,220,48
v_exp_f32 v65, v65
ma 36,216,40
v_exp_f32 v66, v66
ma 44,216,48
v_exp_f32 v67, v67
ma 48,228,40
v_exp_f32 v68, v68
ma 56,228,48
v_exp_f32 v69, v69
ma 52,224,40
v_exp_f32 v70, v70
ma 60,224,48
v_exp_f32 v71, v71
s_waitcnt lgkmcnt(0)
ma 64,236,40
cv 64,64,65
tr 220,3,12288
ma 72,236,48
tr 222,3,18432
cv 65,66,67
ma 68,232,40
cv 66,68,69
cv 67,70,71
ma 76,232,48
s_add_u32 m0, s16, 76800
tr 216,4,12288
dn 14,60,23,0
ma 80,240,40
tr 218,4,18432
tr 228,1,13312
ma 88,240,48
tr 230,1,19456
tr 224,2,13312
ma 84,244,40
tr 226,2,19456
tr 236,3,13312
ma 92,244,48
dn 14,64,23,1024
v_mfma_f32_16x16x32_bf16 a[244:247], a[240:243], v[56:59], a[244:247]
tr 238,3,19456
tr 232,4,13312
v_mfma_f32_16x16x32_bf16 a[248:251], a[240:243], v[64:67], a[248:251]
tr 234,4,19456
tr 240,1,14336
ma 0,252,56
tr 242,1,20480
tr 244,2,14336
ma 8,252,64
dn 14,68,23,2048
ma 4,248,56
tr 246,2,20480
tr 252,3,14336
ma 12,248,64
tr 254,3,20480
tr 248,4,14336
s_waitcnt lgkmcnt(10)
ma 16,220,56
tr 250,4,20480
v_add_u32 v22, 0xffff8000, v22
ma 24,220,64
v_add_u32 v23, 0xffff8000, v23
ma 20,216,56
ma 28,216,64
ma 32,228,56
ma 40,228,64
s_waitcnt vmcnt(12) lgkmcnt(0)
s_barrier
ma 36,224,56
ds_read_b128 a[192:195], v7 offset:57344
tr 220,22,24576
ma 44,224,64
tr 222,22,30720
ds_read_b128 a[196:199], v7 offset:59392
ma 48,236,56
ds_read_b128 a[200:203], v7 offset:61440
ds_read_b128 a[204:207], v7 offset:63488
ma 56,236,64
tr 216,23,24576
tr 218,23,30720
ma 52,232,56
tr 228,22,25600
tr 230,22,31744
ma 60,232,64
tr 224,23,25600
tr 226,23,31744
ma 64,240,56
tr 236,22,26624
tr 238,22,32768
ma 72,240,64
tr 232,23,26624
tr 234,23,32768
ma 68,244,56
ds_read_b128 a[208:211], v8 offset:57344
tr 240,22,36864
ma 76,244,64
s_add_u32 m0, s16, 0
s_add_u32 s24, s18, 73728
dn 11,48,24,0
ma 80,252,56
tr 242,22,43008
ds_read_b128 a[212:215], v8 offset:59392
ma 88,252,64
ds_read_b128 a[216:219], v8 offset:61440
ds_read_b128 a[220:223], v8 offset:63488
ma 84,248,56
tr 244,23,36864
tr 246,23,43008
ma 92,248,64
dn 11,52,24,1024
s_waitcnt lgkmcnt(8)
v_mfma_f32_16x16x32_bf16 v[120:123], v[220:223], a[192:195], v[120:123]
tr 252,22,37888
tr 254,22,44032
v_mfma_f32_16x16x32_bf16 v[128:131], v[220:223], a[196:199], v[128:131]
tr 248,23,37888
tr 250,23,44032
v_mfma_f32_16x16x32_bf16 v[136:139], v[220:223], a[200:203], v[136:139]
v_mfma_f32_16x16x32_bf16 v[144:147], v[220:223], a[204:207], v[144:147]
dn 11,56,24,2048
v_mfma_f32_16x16x32_bf16 v[148:151], v[216:219], a[204:207], v[148:151]
tr 220,22,38912
tr 222,22,45056
v_mfma_f32_16x16x32_bf16 v[140:143], v[216:219], a[200:203], v[140:143]
v_add_u32 v22, 0x8000, v22
v_mfma_f32_16x16x32_bf16 v[132:135], v[216:219], a[196:199], v[132:135]
v_mfma_f32_16x16x32_bf16 v[124:127], v[216:219], a[192:195], v[124:127]
tr 216,23,38912
tr 218,23,45056
v_mfma_f32_16x16x32_bf16 v[152:155], v[228:231], a[192:195], v[152:155]
v_add_u32 v23, 0x8000, v23
v_mfma_f32_16x16x32_bf16 v[160:163], v[228:231], a[196:199], v[160:163]
v_mfma_f32_16x16x32_bf16 v[168:171], v[228:231], a[200:203], v[168:171]
v_mfma_f32_16x16x32_bf16 v[176:179], v[228:231], a[204:207], v[176:179]
v_mfma_f32_16x16x32_bf16 v[180:183], v[224:227], a[204:207], v[180:183]
s_waitcnt vmcnt(9) lgkmcnt(0)
s_barrier
v_mfma_f32_16x16x32_bf16 v[172:175], v[224:227], a[200:203], v[172:175]
rd 228,5,16384
v_mfma_f32_16x16x32_bf16 v[164:167], v[224:227], a[196:199], v[164:167]
v_mfma_f32_16x16x32_bf16 v[156:159], v[224:227], a[192:195], v[156:159]
rd 224,6,16384
v_mfma_f32_16x16x32_bf16 v[184:187], v[236:239], a[192:195], v[184:187]
v_mfma_f32_16x16x32_bf16 v[192:195], v[236:239], a[196:199], v[192:195]
v_mfma_f32_16x16x32_bf16 v[200:203], v[236:239], a[200:203], v[200:203]
v_mfma_f32_16x16x32_bf16 v[208:211], v[236:239], a[204:207], v[208:211]
s_add_u32 m0, s16, 3072
rd 236,5,17408
dn 12,48,24,0
v_mfma_f32_16x16x32_bf16 v[212:215], v[232:235], a[204:207], v[212:215]
v_mfma_f32_16x16x32_bf16 v[204:207], v[232:235], a[200:203], v[204:207]
v_mfma_f32_16x16x32_bf16 v[196:199], v[232:235], a[196:199], v[196:199]
v_mfma_f32_16x16x32_bf16 v[188:191], v[232:235], a[192:195], v[188:191]
dn 12,52,24,1024
v_mfma_f32_16x16x32_bf16 v[120:123], v[240:243], a[208:211], v[120:123]
rd 232,6,17408
v_mfma_f32_16x16x32_bf16 v[128:131], v[240:243], a[212:215], v[128:131]
v_mfma_f32_16x16x32_bf16 v[136:139], v[240:243], a[216:219], v[136:139]
v_mfma_f32_16x16x32_bf16 v[144:147], v[240:243], a[220:223], v[144:147]
dn 12,56,24,2048
v_mfma_f32_16x16x32_bf16 v[148:151], v[244:247], a[220:223], v[148:151]
rd 240,5,18432
v_mfma_f32_16x16x32_bf16 v[140:143], v[244:247], a[216:219], v[140:143]
v_mfma_f32_16x16x32_bf16 v[132:135], v[244:247], a[212:215], v[132:135]
v_mfma_f32_16x16x32_bf16 v[124:127], v[244:247], a[208:211], v[124:127]
rd 244,6,18432
v_mfma_f32_16x16x32_bf16 v[152:155], v[252:255], a[208:211], v[152:155]
v_mfma_f32_16x16x32_bf16 v[160:163], v[252:255], a[212:215], v[160:163]
v_mfma_f32_16x16x32_bf16 v[168:171], v[252:255], a[216:219], v[168:171]
v_mfma_f32_16x16x32_bf16 v[176:179], v[252:255], a[220:223], v[176:179]
rd 252,5,22528
v_mfma_f32_16x16x32_bf16 v[180:183], v[248:251], a[220:223], v[180:183]
v_mfma_f32_16x16x32_bf16 v[172:175], v[248:251], a[216:219], v[172:175]
v_mfma_f32_16x16x32_bf16 v[164:167], v[248:251], a[212:215], v[164:167]
v_mfma_f32_16x16x32_bf16 v[156:159], v[248:251], a[208:211], v[156:159]
rd 248,6,22528
v_mfma_f32_16x16x32_bf16 v[184:187], v[220:223], a[208:211], v[184:187]
v_mfma_f32_16x16x32_bf16 v[192:195], v[220:223], a[212:215], v[192:195]
v_mfma_f32_16x16x32_bf16 v[200:203], v[220:223], a[216:219], v[200:203]
v_mfma_f32_16x16x32_bf16 v[208:211], v[220:223], a[220:223], v[208:211]
s_add_u32 m0, s17, 147456
s_add_u32 s25, s21, 640
dl 17,4,25,0
v_mfma_f32_16x16x32_bf16 v[212:215], v[216:219], a[220:223], v[212:215]
rd 220,5,23552
v_mfma_f32_16x16x32_bf16 v[204:207], v[216:219], a[216:219], v[204:207]
v_mfma_f32_16x16x32_bf16 v[196:199], v[216:219], a[212:215], v[196:199]
v_mfma_f32_16x16x32_bf16 v[188:191], v[216:219], a[208:211], v[188:191]
dl 18,4,25,1024
s_waitcnt lgkmcnt(1)
mvc 40,228,72,32
rd 216,6,23552
mv 40,224,80
mvc 48,224,84,36
rd 224,5,24576
mv 48,228,76
dl 17,8,25,2048
mv 48,236,92
rd 228,6,24576
mv 48,232,100
mv 40,232,96
rd 232,5,28672
mv 40,236,88
dl 18,8,25,3072
mv 40,240,104
rd 236,6,28672
mv 40,244,112
mv 48,244,116
rd 244,5,29696
mv 48,240,108
s_add_u32 s22, s19, 3932544
rd 240,6,29696
mvc 44,252,72,32
s_nop 1
v_max3_f32 v20, v40, v41, v42
mv 44,248,80
mvc 52,248,84,36
rd 248,5,30720
v_max3_f32 v20, v20, v43, v48
mv 52,252,76
rd 252,6,30720
v_max3_f32 v20, v20, v49, v50
s_waitcnt lgkmcnt(4)
mv 52,220,92
s_add_u32 m0, s16, 24576
s_nop 0
dn 13,60,22,0
mv 52,216,100
mv 44,216,96
rd 216,5,34816
mv 44,220,88
rd 220,6,34816
mv 44,224,104
dn 13,64,22,1024
mv 44,228,112
mv 52,228,116
rd 228,5,35840
mv 52,224,108
rd 224,6,35840
mvc 56,232,72,32
dn 13,68,22,2048
mv 56,236,80
s_nop 0
v_max3_f32 v20, v20, v51, v44
v_max3_f32 v20, v20, v45, v46
mvc 64,236,84,36
rd 236,5,36864
v_max3_f32 v20, v20, v47, v52
mv 64,232,76
rd 232,6,36864
v_max3_f32 v20, v20, v53, v54
s_waitcnt lgkmcnt(4)
mv 64,244,92
mv 64,240,100
mv 56,240,96
mv 56,244,88
s_waitcnt vmcnt(13) lgkmcnt(0)
s_barrier
mv 56,248,104
s_add_u32 m0, s16, 27648
tr 240,22,40960
dn 14,60,22,0
mv 56,252,112
tr 242,22,47104
tr 244,23,40960
mv 64,252,116
tr 246,23,47104
tr 252,22,41984
mv 64,248,108
tr 254,22,48128
tr 248,23,41984
mvc 60,216,72,32
dn 14,64,22,1024
mv 60,220,80
tr 250,23,48128
v_max3_f32 v20, v20, v55, v56
mvc 68,220,84,36
tr 220,22,43008
tr 222,22,49152
mv 68,216,76
tr 216,23,43008
tr 218,23,49152
mv 68,228,92
dn 14,68,22,2048
mv 68,224,100
v_max3_f32 v20, v20, v57, v58
v_max3_f32 v20, v20, v59, v64
mv 60,224,96
tr 224,22,53248
tr 226,22,59392
mv 60,228,88
s_add_u32 s23, s19, 3932928
tr 228,23,53248
mv 60,236,104
tr 230,23,59392
v_max3_f32 v20, v20, v65, v66
mv 60,232,112
mv 68,232,116
tr 232,22,54272
tr 234,22,60416
mv 68,236,108
tr 236,23,54272
tr 238,23,60416
s_waitcnt lgkmcnt(8)
v_mfma_f32_16x16x32_bf16 a[96:99], v[240:243], a[192:195], a[96:99]
v_max3_f32 v20, v20, v67, v60
v_max3_f32 v20, v20, v61, v62
s_add_u32 m0, s16, 49152
v_mfma_f32_16x16x32_bf16 a[104:107], v[240:243], a[196:199], a[104:107]
dn 13,60,23,0
v_mfma_f32_16x16x32_bf16 a[112:115], v[240:243], a[200:203], a[112:115]
v_max3_f32 v20, v20, v63, v68
v_max3_f32 v20, v20, v69, v70
v_mfma_f32_16x16x32_bf16 a[120:123], v[240:243], a[204:207], a[120:123]
tr 240,22,55296
tr 242,22,61440
v_mfma_f32_16x16x32_bf16 a[124:127], v[244:247], a[204:207], a[124:127]
v_max_f32 v20, v20, v71
v_cmp_lt_f32 vcc, s27, v20
s_cbranch_vccz Lc2
s_swappc_b64 s[30:31], s[28:29]
Lc2:
v_mfma_f32_16x16x32_bf16 a[116:119], v[244:247], a[200:203], a[116:119]
dn 13,64,23,1024
v_mfma_f32_16x16x32_bf16 a[108:111], v[244:247], a[196:199], a[108:111]
v_exp_f32 v40, v40
v_mfma_f32_16x16x32_bf16 a[100:103], v[244:247], a[192:195], a[100:103]
tr 244,23,55296
tr 246,23,61440
v_mfma_f32_16x16x32_bf16 a[128:131], v[252:255], a[192:195], a[128:131]
v_exp_f32 v41, v41
v_mfma_f32_16x16x32_bf16 a[136:139], v[252:255], a[196:199], a[136:139]
dn 13,68,23,2048
v_mfma_f32_16x16x32_bf16 a[144:147], v[252:255], a[200:203], a[144:147]
v_exp_f32 v42, v42
v_mfma_f32_16x16x32_bf16 a[152:155], v[252:255], a[204:207], a[152:155]
v_exp_f32 v43, v43
v_mfma_f32_16x16x32_bf16 a[156:159], v[248:251], a[204:207], a[156:159]
s_waitcnt vmcnt(13) lgkmcnt(0)
s_barrier
v_mfma_f32_16x16x32_bf16 a[148:151], v[248:251], a[200:203], a[148:151]
tr 252,1,0
tr 254,1,6144
v_mfma_f32_16x16x32_bf16 a[140:143], v[248:251], a[196:199], a[140:143]
v_exp_f32 v44, v44
v_mfma_f32_16x16x32_bf16 a[132:135], v[248:251], a[192:195], a[132:135]
tr 248,2,0
tr 250,2,6144
v_mfma_f32_16x16x32_bf16 a[160:163], v[220:223], a[192:195], a[160:163]
v_exp_f32 v45, v45
v_mfma_f32_16x16x32_bf16 a[168:171], v[220:223], a[196:199], a[168:171]
v_exp_f32 v46, v46
v_mfma_f32_16x16x32_bf16 a[176:179], v[220:223], a[200:203], a[176:179]
v_exp_f32 v47, v47
v_mfma_f32_16x16x32_bf16 a[184:187], v[220:223], a[204:207], a[184:187]
s_add_u32 m0, s16, 52224
cv 40,40,41
dn 14,60,23,0
v_mfma_f32_16x16x32_bf16 a[188:191], v[216:219], a[204:207], a[188:191]
cv 41,42,43
cv 42,44,45
v_mfma_f32_16x16x32_bf16 a[180:183], v[216:219], a[200:203], a[180:183]
cv 43,46,47
v_exp_f32 v48, v48
v_mfma_f32_16x16x32_bf16 a[172:175], v[216:219], a[196:199], a[172:175]
v_exp_f32 v49, v49
v_mfma_f32_16x16x32_bf16 a[164:167], v[216:219], a[192:195], a[164:167]
dn 14,64,23,1024
v_mfma_f32_16x16x32_bf16 a[96:99], v[224:227], a[208:211], a[96:99]
v_exp_f32 v50, v50
v_mfma_f32_16x16x32_bf16 a[104:107], v[224:227], a[212:215], a[104:107]
v_exp_f32 v51, v51
v_mfma_f32_16x16x32_bf16 a[112:115], v[224:227], a[216:219], a[112:115]
v_exp_f32 v52, v52
v_mfma_f32_16x16x32_bf16 a[120:123], v[224:227], a[220:223], a[120:123]
dn 14,68,23,2048
v_mfma_f32_16x16x32_bf16 a[124:127], v[228:231], a[220:223], a[124:127]
v_exp_f32 v53, v53
v_mfma_f32_16x16x32_bf16 a[116:119], v[228:231], a[216:219], a[116:119]
v_exp_f32 v54, v54
v_mfma_f32_16x16x32_bf16 a[108:111], v[228:231], a[212:215], a[108:111]
v_exp_f32 v55, v55
v_mfma_f32_16x16x32_bf16 a[100:103], v[228:231], a[208:211], a[100:103]
cv 48,48,49
tr 220,3,0
v_mfma_f32_16x16x32_bf16 a[128:131], v[232:235], a[208:211], a[128:131]
tr 222,3,6144
cv 49,50,51
v_mfma_f32_16x16x32_bf16 a[136:139], v[232:235], a[212:215], a[136:139]
cv 50,52,53
cv 51,54,55
v_mfma_f32_16x16x32_bf16 a[144:147], v[232:235], a[216:219], a[144:147]
tr 216,4,0
tr 218,4,6144
v_mfma_f32_16x16x32_bf16 a[152:155], v[232:235], a[220:223], a[152:155]
tr 224,1,1024
tr 226,1,7168
v_mfma_f32_16x16x32_bf16 a[156:159], v[236:239], a[220:223], a[156:159]
tr 228,2,1024
tr 230,2,7168
v_mfma_f32_16x16x32_bf16 a[148:151], v[236:239], a[216:219], a[148:151]
tr 232,3,1024
tr 234,3,7168
v_mfma_f32_16x16x32_bf16 a[140:143], v[236:239], a[212:215], a[140:143]
v_exp_f32 v56, v56
v_mfma_f32_16x16x32_bf16 a[132:135], v[236:239], a[208:211], a[132:135]
tr 236,4,1024
tr 238,4,7168
v_mfma_f32_16x16x32_bf16 a[160:163], v[240:243], a[208:211], a[160:163]
v_exp_f32 v57, v57
v_mfma_f32_16x16x32_bf16 a[168:171], v[240:243], a[212:215], a[168:171]
v_exp_f32 v58, v58
v_mfma_f32_16x16x32_bf16 a[176:179], v[240:243], a[216:219], a[176:179]
v_exp_f32 v59, v59
v_mfma_f32_16x16x32_bf16 a[184:187], v[240:243], a[220:223], a[184:187]
s_add_u32 m0, s16, 73728
s_add_u32 s24, s18, 98304
dn 9,36,24,0
v_mfma_f32_16x16x32_bf16 a[188:191], v[244:247], a[220:223], a[188:191]
tr 240,1,2048
tr 242,1,8192
v_mfma_f32_16x16x32_bf16 a[180:183], v[244:247], a[216:219], a[180:183]
v_exp_f32 v60, v60
v_mfma_f32_16x16x32_bf16 a[172:175], v[244:247], a[212:215], a[172:175]
v_exp_f32 v61, v61
v_mfma_f32_16x16x32_bf16 a[164:167], v[244:247], a[208:211], a[164:167]
dn 9,40,24,1024
v_mfma_f32_16x16x32_bf16 a[244:247], a[240:243], v[40:43], a[244:247]
tr 244,2,2048
tr 246,2,8192
v_mfma_f32_16x16x32_bf16 a[248:251], a[240:243], v[48:51], a[248:251]
v_exp_f32 v62, v62
s_waitcnt lgkmcnt(4)
ma 0,252,40
v_exp_f32 v63, v63
ma 8,252,48
dn 9,44,24,2048
ma 4,248,40
tr 252,3,2048
tr 254,3,8192
ma 12,248,48
tr 248,4,2048
tr 250,4,8192
ma 16,220,40
cv 56,56,57
cv 57,58,59
ma 24,220,48
tr 220,1,12288
tr 222,1,18432
ma 20,216,40
cv 58,60,61
cv 59,62,63
ma 28,216,48
tr 216,2,12288
tr 218,2,18432
ma 32,224,40
v_exp_f32 v64, v64
ma 40,224,48
v_exp_f32 v65, v65
ma 36,228,40
v_exp_f32 v66, v66
ma 44,228,48
v_exp_f32 v67, v67
ma 48,232,40
v_exp_f32 v68, v68
ma 56,232,48
v_exp_f32 v69, v69
ma 52,236,40
v_exp_f32 v70, v70
ma 60,236,48
v_exp_f32 v71, v71
s_waitcnt lgkmcnt(0)
ma 64,240,40
cv 64,64,65
tr 224,3,12288
ma 72,240,48
tr 226,3,18432
cv 65,66,67
ma 68,244,40
cv 66,68,69
cv 67,70,71
ma 76,244,48
s_add_u32 m0, s16, 76800
tr 228,4,12288
dn 10,36,24,0
ma 80,252,40
tr 230,4,18432
tr 232,1,13312
ma 88,252,48
tr 234,1,19456
tr 236,2,13312
ma 84,248,40
tr 238,2,19456
tr 240,3,13312
ma 92,248,48
dn 10,40,24,1024
v_mfma_f32_16x16x32_bf16 a[244:247], a[240:243], v[56:59], a[244:247]
tr 242,3,19456
tr 244,4,13312
v_mfma_f32_16x16x32_bf16 a[248:251], a[240:243], v[64:67], a[248:251]
tr 246,4,19456
tr 252,1,14336
ma 0,220,56
tr 254,1,20480
tr 248,2,14336
ma 8,220,64
dn 10,44,24,2048
ma 4,216,56
tr 250,2,20480
tr 220,3,14336
ma 12,216,64
tr 222,3,20480
tr 216,4,14336
s_waitcnt lgkmcnt(10)
ma 16,224,56
tr 218,4,20480
v_add_u32 v22, 0xffffa000, v22
ma 24,224,64
v_add_u32 v23, 0xffffa000, v23
ma 20,228,56
ma 28,228,64
ma 32,232,56
ma 40,232,64
s_waitcnt vmcnt(12) lgkmcnt(0)
s_barrier
ma 36,236,56
ds_read_b128 a[192:195], v7 offset:57344
tr 224,22,16384
ma 44,236,64
tr 226,22,22528
ds_read_b128 a[196:199], v7 offset:59392
ma 48,240,56
ds_read_b128 a[200:203], v7 offset:61440
ds_read_b128 a[204:207], v7 offset:63488
ma 56,240,64
tr 228,23,16384
tr 230,23,22528
ma 52,244,56
tr 232,22,17408
tr 234,22,23552
ma 60,244,64
tr 236,23,17408
tr 238,23,23552
ma 64,252,56
tr 240,22,18432
tr 242,22,24576
ma 72,252,64
tr 244,23,18432
tr 246,23,24576
ma 68,248,56
ds_read_b128 a[208:211], v8 offset:57344
tr 252,22,28672
ma 76,248,64
s_add_u32 m0, s17, 131072
s_add_u32 s25, s21, 768
dl 17,4,25,0
ma 80,220,56
tr 254,22,34816
ds_read_b128 a[212:215], v8 offset:59392
ma 88,220,64
ds_read_b128 a[216:219], v8 offset:61440
ds_read_b128 a[220:223], v8 offset:63488
ma 84,216,56
tr 248,23,28672
tr 250,23,34816
ma 92,216,64
dl 18,4,25,1024
s_waitcnt lgkmcnt(8)
v_mfma_f32_16x16x32_bf16 v[120:123], v[224:227], a[192:195], v[120:123]
tr 220,22,29696
tr 222,22,35840
v_mfma_f32_16x16x32_bf16 v[128:131], v[224:227], a[196:199], v[128:131]
tr 216,23,29696
tr 218,23,35840
v_mfma_f32_16x16x32_bf16 v[136:139], v[224:227], a[200:203], v[136:139]
v_mfma_f32_16x16x32_bf16 v[144:147], v[224:227], a[204:207], v[144:147]
dl 17,8,25,2048
v_mfma_f32_16x16x32_bf16 v[148:151], v[228:231], a[204:207], v[148:151]
tr 224,22,30720
tr 226,22,36864
v_mfma_f32_16x16x32_bf16 v[140:143], v[228:231], a[200:203], v[140:143]
v_mfma_f32_16x16x32_bf16 v[132:135], v[228:231], a[196:199], v[132:135]
v_mfma_f32_16x16x32_bf16 v[124:127], v[228:231], a[192:195], v[124:127]
dl 18,8,25,3072
v_mfma_f32_16x16x32_bf16 v[152:155], v[232:235], a[192:195], v[152:155]
tr 228,23,30720
tr 230,23,36864
v_mfma_f32_16x16x32_bf16 v[160:163], v[232:235], a[196:199], v[160:163]
v_mfma_f32_16x16x32_bf16 v[168:171], v[232:235], a[200:203], v[168:171]
v_mfma_f32_16x16x32_bf16 v[176:179], v[232:235], a[204:207], v[176:179]
s_add_u32 s22, s19, 4718976
v_mfma_f32_16x16x32_bf16 v[180:183], v[236:239], a[204:207], v[180:183]
v_mfma_f32_16x16x32_bf16 v[172:175], v[236:239], a[200:203], v[172:175]
s_waitcnt vmcnt(10) lgkmcnt(0)
s_barrier
v_mfma_f32_16x16x32_bf16 v[164:167], v[236:239], a[196:199], v[164:167]
tr 232,22,40960
tr 234,22,47104
v_mfma_f32_16x16x32_bf16 v[156:159], v[236:239], a[192:195], v[156:159]
tr 236,23,40960
tr 238,23,47104
s_add_u32 m0, s16, 0
v_mfma_f32_16x16x32_bf16 v[184:187], v[240:243], a[192:195], v[184:187]
dn 13,60,22,0
v_mfma_f32_16x16x32_bf16 v[192:195], v[240:243], a[196:199], v[192:195]
v_mfma_f32_16x16x32_bf16 v[200:203], v[240:243], a[200:203], v[200:203]
v_mfma_f32_16x16x32_bf16 v[208:211], v[240:243], a[204:207], v[208:211]
tr 240,22,41984
tr 242,22,48128
v_mfma_f32_16x16x32_bf16 v[212:215], v[244:247], a[204:207], v[212:215]
dn 13,64,22,1024
v_mfma_f32_16x16x32_bf16 v[204:207], v[244:247], a[200:203], v[204:207]
v_mfma_f32_16x16x32_bf16 v[196:199], v[244:247], a[196:199], v[196:199]
v_mfma_f32_16x16x32_bf16 v[188:191], v[244:247], a[192:195], v[188:191]
tr 244,23,41984
tr 246,23,48128
v_mfma_f32_16x16x32_bf16 v[120:123], v[252:255], a[208:211], v[120:123]
dn 13,68,22,2048
v_mfma_f32_16x16x32_bf16 v[128:131], v[252:255], a[212:215], v[128:131]
v_mfma_f32_16x16x32_bf16 v[136:139], v[252:255], a[216:219], v[136:139]
v_mfma_f32_16x16x32_bf16 v[144:147], v[252:255], a[220:223], v[144:147]
tr 252,22,43008
tr 254,22,49152
v_mfma_f32_16x16x32_bf16 v[148:151], v[248:251], a[220:223], v[148:151]
v_mfma_f32_16x16x32_bf16 v[140:143], v[248:251], a[216:219], v[140:143]
v_mfma_f32_16x16x32_bf16 v[132:135], v[248:251], a[212:215], v[132:135]
v_mfma_f32_16x16x32_bf16 v[124:127], v[248:251], a[208:211], v[124:127]
tr 248,23,43008
tr 250,23,49152
v_mfma_f32_16x16x32_bf16 v[152:155], v[220:223], a[208:211], v[152:155]
v_mfma_f32_16x16x32_bf16 v[160:163], v[220:223], a[212:215], v[160:163]
s_add_u32 m0, s16, 3072
s_nop 0
dn 14,60,22,0
v_mfma_f32_16x16x32_bf16 v[168:171], v[220:223], a[216:219], v[168:171]
v_mfma_f32_16x16x32_bf16 v[176:179], v[220:223], a[220:223], v[176:179]
tr 220,22,53248
tr 222,22,59392
v_mfma_f32_16x16x32_bf16 v[180:183], v[216:219], a[220:223], v[180:183]
v_mfma_f32_16x16x32_bf16 v[172:175], v[216:219], a[216:219], v[172:175]
dn 14,64,22,1024
v_mfma_f32_16x16x32_bf16 v[164:167], v[216:219], a[212:215], v[164:167]
v_mfma_f32_16x16x32_bf16 v[156:159], v[216:219], a[208:211], v[156:159]
tr 216,23,53248
tr 218,23,59392
v_mfma_f32_16x16x32_bf16 v[184:187], v[224:227], a[208:211], v[184:187]
v_mfma_f32_16x16x32_bf16 v[192:195], v[224:227], a[212:215], v[192:195]
dn 14,68,22,2048
v_mfma_f32_16x16x32_bf16 v[200:203], v[224:227], a[216:219], v[200:203]
v_mfma_f32_16x16x32_bf16 v[208:211], v[224:227], a[220:223], v[208:211]
s_add_u32 s23, s18, 98304
tr 224,22,54272
v_mfma_f32_16x16x32_bf16 v[212:215], v[228:231], a[220:223], v[212:215]
tr 226,22,60416
v_mfma_f32_16x16x32_bf16 v[204:207], v[228:231], a[216:219], v[204:207]
v_mfma_f32_16x16x32_bf16 v[196:199], v[228:231], a[212:215], v[196:199]
v_mfma_f32_16x16x32_bf16 v[188:191], v[228:231], a[208:211], v[188:191]
tr 228,23,54272
tr 230,23,60416
s_waitcnt lgkmcnt(4)
v_mfma_f32_16x16x32_bf16 a[96:99], v[232:235], a[192:195], a[96:99]
v_mfma_f32_16x16x32_bf16 a[104:107], v[232:235], a[196:199], a[104:107]
s_add_u32 m0, s16, 24576
v_mfma_f32_16x16x32_bf16 a[112:115], v[232:235], a[200:203], a[112:115]
dn 11,48,23,0
v_mfma_f32_16x16x32_bf16 a[120:123], v[232:235], a[204:207], a[120:123]
tr 232,22,55296
tr 234,22,61440
v_mfma_f32_16x16x32_bf16 a[124:127], v[236:239], a[204:207], a[124:127]
v_add_u32 v22, 0xffffe000, v22
v_mfma_f32_16x16x32_bf16 a[116:119], v[236:239], a[200:203], a[116:119]
v_mfma_f32_16x16x32_bf16 a[108:111], v[236:239], a[196:199], a[108:111]
dn 11,52,23,1024
v_mfma_f32_16x16x32_bf16 a[100:103], v[236:239], a[192:195], a[100:103]
tr 236,23,55296
tr 238,23,61440
v_mfma_f32_16x16x32_bf16 a[128:131], v[240:243], a[192:195], a[128:131]
v_add_u32 v23, 0xffffe000, v23
v_mfma_f32_16x16x32_bf16 a[136:139], v[240:243], a[196:199], a[136:139]
v_mfma_f32_16x16x32_bf16 a[144:147], v[240:243], a[200:203], a[144:147]
dn 11,56,23,2048
v_mfma_f32_16x16x32_bf16 a[152:155], v[240:243], a[204:207], a[152:155]
v_mfma_f32_16x16x32_bf16 a[156:159], v[244:247], a[204:207], a[156:159]
s_waitcnt vmcnt(13) lgkmcnt(0)
s_barrier
v_mfma_f32_16x16x32_bf16 a[148:151], v[244:247], a[200:203], a[148:151]
rd 240,5,40960
v_mfma_f32_16x16x32_bf16 a[140:143], v[244:247], a[196:199], a[140:143]
v_mfma_f32_16x16x32_bf16 a[132:135], v[244:247], a[192:195], a[132:135]
rd 244,6,40960
v_mfma_f32_16x16x32_bf16 a[160:163], v[252:255], a[192:195], a[160:163]
v_mfma_f32_16x16x32_bf16 a[168:171], v[252:255], a[196:199], a[168:171]
v_mfma_f32_16x16x32_bf16 a[176:179], v[252:255], a[200:203], a[176:179]
v_mfma_f32_16x16x32_bf16 a[184:187], v[252:255], a[204:207], a[184:187]
s_add_u32 m0, s16, 27648
rd 252,5,41984
dn 12,48,23,0
v_mfma_f32_16x16x32_bf16 a[188:191], v[248:251], a[204:207], a[188:191]
v_mfma_f32_16x16x32_bf16 a[180:183], v[248:251], a[200:203], a[180:183]
v_mfma_f32_16x16x32_bf16 a[172:175], v[248:251], a[196:199], a[172:175]
v_mfma_f32_16x16x32_bf16 a[164:167], v[248:251], a[192:195], a[164:167]
dn 12,52,23,1024
v_mfma_f32_16x16x32_bf16 a[96:99], v[220:223], a[208:211], a[96:99]
rd 248,6,41984
v_mfma_f32_16x16x32_bf16 a[104:107], v[220:223], a[212:215], a[104:107]
v_mfma_f32_16x16x32_bf16 a[112:115], v[220:223], a[216:219], a[112:115]
v_mfma_f32_16x16x32_bf16 a[120:123], v[220:223], a[220:223], a[120:123]
dn 12,56,23,2048
v_mfma_f32_16x16x32_bf16 a[124:127], v[216:219], a[220:223], a[124:127]
rd 220,5,43008
v_mfma_f32_16x16x32_bf16 a[116:119], v[216:219], a[216:219], a[116:119]
v_mfma_f32_16x16x32_bf16 a[108:111], v[216:219], a[212:215], a[108:111]
v_mfma_f32_16x16x32_bf16 a[100:103], v[216:219], a[208:211], a[100:103]
rd 216,6,43008
v_mfma_f32_16x16x32_bf16 a[128:131], v[224:227], a[208:211], a[128:131]
v_mfma_f32_16x16x32_bf16 a[136:139], v[224:227], a[212:215], a[136:139]
v_mfma_f32_16x16x32_bf16 a[144:147], v[224:227], a[216:219], a[144:147]
v_mfma_f32_16x16x32_bf16 a[152:155], v[224:227], a[220:223], a[152:155]
rd 224,5,47104
v_mfma_f32_16x16x32_bf16 a[156:159], v[228:231], a[220:223], a[156:159]
v_mfma_f32_16x16x32_bf16 a[148:151], v[228:231], a[216:219], a[148:151]
v_mfma_f32_16x16x32_bf16 a[140:143], v[228:231], a[212:215], a[140:143]
v_mfma_f32_16x16x32_bf16 a[132:135], v[228:231], a[208:211], a[132:135]
rd 228,6,47104
v_mfma_f32_16x16x32_bf16 a[160:163], v[232:235], a[208:211], a[160:163]
v_mfma_f32_16x16x32_bf16 a[168:171], v[232:235], a[212:215], a[168:171]
v_mfma_f32_16x16x32_bf16 a[176:179], v[232:235], a[216:219], a[176:179]
v_mfma_f32_16x16x32_bf16 a[184:187], v[232:235], a[220:223], a[184:187]
s_add_u32 m0, s16, 49152
s_add_u32 s24, s19, 4719360
dn 13,60,24,0
v_mfma_f32_16x16x32_bf16 a[188:191], v[236:239], a[220:223], a[188:191]
rd 232,5,48128
v_mfma_f32_16x16x32_bf16 a[180:183], v[236:239], a[216:219], a[180:183]
v_mfma_f32_16x16x32_bf16 a[172:175], v[236:239], a[212:215], a[172:175]
v_mfma_f32_16x16x32_bf16 a[164:167], v[236:239], a[208:211], a[164:167]
dn 13,64,24,1024
s_waitcnt lgkmcnt(1)
mvc 40,240,72,32
rd 236,6,48128
mv 40,244,80
mvc 48,244,84,36
rd 244,5,49152
mv 48,240,76
dn 13,68,24,2048
mv 48,252,92
rd 240,6,49152
mv 48,248,100
mv 40,248,96
rd 248,5,53248
mv 40,252,88
rd 252,6,53248
mv 40,220,104
mv 40,216,112
mv 48,216,116
rd 216,5,54272
mv 48,220,108
rd 220,6,54272
mvc 44,224,72,32
s_nop 2
v_max3_f32 v20, v40, v41, v42
mv 44,228,80
mvc 52,228,84,36
rd 228,5,55296
v_max3_f32 v20, v20, v43, v48
mv 52,224,76
rd 224,6,55296
v_max3_f32 v20, v20, v49, v50
s_waitcnt lgkmcnt(4)
mv 52,232,92
mv 52,236,100
mv 44,236,96
rd 236,5,59392
mv 44,232,88
s_add_u32 m0, s16, 52224
rd 232,6,59392
dn 14,60,24,0
mv 44,244,104
mv 44,240,112
mv 52,240,116
rd 240,5,60416
mv 52,244,108
dn 14,64,24,1024
mvc 56,248,72,32
rd 244,6,60416
s_nop 1
v_max3_f32 v20, v20, v51, v44
mv 56,252,80
v_max3_f32 v20, v20, v45, v46
mvc 64,252,84,36
rd 252,5,61440
v_max3_f32 v20, v20, v47, v52
mv 64,248,76
dn 14,68,24,2048
s_waitcnt lgkmcnt(3)
mv 64,216,92
rd 248,6,61440
v_max3_f32 v20, v20, v53, v54
mv 64,220,100
mv 56,220,96
mv 56,216,88
mv 56,228,104
mv 56,224,112
s_waitcnt vmcnt(12) lgkmcnt(0)
s_barrier
mv 64,224,116
ds_read_b128 a[192:195], v7 offset:40960
tr 220,22,0
mv 64,228,108
tr 222,22,6144
ds_read_b128 a[196:199], v7 offset:43008
mvc 60,236,72,32
ds_read_b128 a[200:203], v7 offset:45056
ds_read_b128 a[204:207], v7 offset:47104
mv 60,232,80
tr 216,23,0
tr 218,23,6144
mvc 68,232,84,36
tr 224,22,1024
tr 226,22,7168
mv 68,236,76
tr 228,23,1024
tr 230,23,7168
mv 68,240,92
tr 232,22,2048
tr 234,22,8192
mv 68,244,100
tr 236,23,2048
tr 238,23,8192
mv 60,244,96
ds_read_b128 a[208:211], v8 offset:40960
tr 244,22,12288
mv 60,240,88
s_add_u32 m0, s16, 73728
s_add_u32 s25, s18, 122880
dn 9,36,25,0
mv 60,252,104
tr 246,22,18432
ds_read_b128 a[212:215], v8 offset:43008
mv 60,248,112
ds_read_b128 a[216:219], v8 offset:45056
ds_read_b128 a[220:223], v8 offset:47104
mv 68,248,116
tr 240,23,12288
tr 242,23,18432
mv 68,252,108
dn 9,40,25,1024
s_waitcnt lgkmcnt(8)
v_mfma_f32_16x16x32_bf16 v[120:123], v[220:223], a[192:195], v[120:123]
tr 248,22,13312
tr 250,22,19456
v_mfma_f32_16x16x32_bf16 v[128:131], v[220:223], a[196:199], v[128:131]
tr 252,23,13312
tr 254,23,19456
v_mfma_f32_16x16x32_bf16 v[136:139], v[220:223], a[200:203], v[136:139]
v_max3_f32 v20, v20, v55, v56
v_max3_f32 v20, v20, v57, v58
v_mfma_f32_16x16x32_bf16 v[144:147], v[220:223], a[204:207], v[144:147]
dn 9,44,25,2048
v_mfma_f32_16x16x32_bf16 v[148:151], v[216:219], a[204:207], v[148:151]
tr 220,22,14336
tr 222,22,20480
v_mfma_f32_16x16x32_bf16 v[140:143], v[216:219], a[200:203], v[140:143]
v_max3_f32 v20, v20, v59, v64
v_max3_f32 v20, v20, v65, v66
v_mfma_f32_16x16x32_bf16 v[132:135], v[216:219], a[196:199], v[132:135]
v_max3_f32 v20, v20, v67, v60
v_max3_f32 v20, v20, v61, v62
v_mfma_f32_16x16x32_bf16 v[124:127], v[216:219], a[192:195], v[124:127]
tr 216,23,14336
tr 218,23,20480
v_mfma_f32_16x16x32_bf16 v[152:155], v[224:227], a[192:195], v[152:155]
v_max3_f32 v20, v20, v63, v68
v_max3_f32 v20, v20, v69, v70
v_mfma_f32_16x16x32_bf16 v[160:163], v[224:227], a[196:199], v[160:163]
v_max_f32 v20, v20, v71
v_cmp_lt_f32 vcc, s27, v20
s_cbranch_vccz Lc3
s_swappc_b64 s[30:31], s[28:29]
Lc3:
v_mfma_f32_16x16x32_bf16 v[168:171], v[224:227], a[200:203], v[168:171]
v_exp_f32 v40, v40
v_mfma_f32_16x16x32_bf16 v[176:179], v[224:227], a[204:207], v[176:179]
v_exp_f32 v41, v41
v_mfma_f32_16x16x32_bf16 v[180:183], v[228:231], a[204:207], v[180:183]
s_waitcnt vmcnt(9) lgkmcnt(0)
s_barrier
v_mfma_f32_16x16x32_bf16 v[172:175], v[228:231], a[200:203], v[172:175]
tr 224,1,24576
tr 226,1,30720
v_mfma_f32_16x16x32_bf16 v[164:167], v[228:231], a[196:199], v[164:167]
v_exp_f32 v42, v42
v_mfma_f32_16x16x32_bf16 v[156:159], v[228:231], a[192:195], v[156:159]
tr 228,2,24576
tr 230,2,30720
v_mfma_f32_16x16x32_bf16 v[184:187], v[232:235], a[192:195], v[184:187]
v_exp_f32 v43, v43
v_mfma_f32_16x16x32_bf16 v[192:195], v[232:235], a[196:199], v[192:195]
v_exp_f32 v44, v44
v_mfma_f32_16x16x32_bf16 v[200:203], v[232:235], a[200:203], v[200:203]
v_exp_f32 v45, v45
v_mfma_f32_16x16x32_bf16 v[208:211], v[232:235], a[204:207], v[208:211]
s_add_u32 m0, s16, 76800
v_exp_f32 v46, v46
dn 10,36,25,0
v_mfma_f32_16x16x32_bf16 v[212:215], v[236:239], a[204:207], v[212:215]
v_exp_f32 v47, v47
v_mfma_f32_16x16x32_bf16 v[204:207], v[236:239], a[200:203], v[204:207]
cv 40,40,41
cv 41,42,43
v_mfma_f32_16x16x32_bf16 v[196:199], v[236:239], a[196:199], v[196:199]
cv 42,44,45
cv 43,46,47
v_mfma_f32_16x16x32_bf16 v[188:191], v[236:239], a[192:195], v[188:191]
dn 10,40,25,1024
v_mfma_f32_16x16x32_bf16 v[120:123], v[244:247], a[208:211], v[120:123]
v_exp_f32 v48, v48
v_mfma_f32_16x16x32_bf16 v[128:131], v[244:247], a[212:215], v[128:131]
v_exp_f32 v49, v49
v_mfma_f32_16x16x32_bf16 v[136:139], v[244:247], a[216:219], v[136:139]
v_exp_f32 v50, v50
v_mfma_f32_16x16x32_bf16 v[144:147], v[244:247], a[220:223], v[144:147]
dn 10,44,25,2048
v_mfma_f32_16x16x32_bf16 v[148:151], v[240:243], a[220:223], v[148:151]
v_exp_f32 v51, v51
v_mfma_f32_16x16x32_bf16 v[140:143], v[240:243], a[216:219], v[140:143]
v_exp_f32 v52, v52
v_mfma_f32_16x16x32_bf16 v[132:135], v[240:243], a[212:215], v[132:135]
v_exp_f32 v53, v53
v_mfma_f32_16x16x32_bf16 v[124:127], v[240:243], a[208:211], v[124:127]
v_exp_f32 v54, v54
v_mfma_f32_16x16x32_bf16 v[152:155], v[248:251], a[208:211], v[152:155]
v_exp_f32 v55, v55
v_mfma_f32_16x16x32_bf16 v[160:163], v[248:251], a[212:215], v[160:163]
cv 48,48,49
tr 232,3,24576
v_mfma_f32_16x16x32_bf16 v[168:171], v[248:251], a[216:219], v[168:171]
tr 234,3,30720
cv 49,50,51
v_mfma_f32_16x16x32_bf16 v[176:179], v[248:251], a[220:223], v[176:179]
cv 50,52,53
cv 51,54,55
v_mfma_f32_16x16x32_bf16 v[180:183], v[252:255], a[220:223], v[180:183]
tr 236,4,24576
tr 238,4,30720
v_mfma_f32_16x16x32_bf16 v[172:175], v[252:255], a[216:219], v[172:175]
tr 244,1,25600
tr 246,1,31744
v_mfma_f32_16x16x32_bf16 v[164:167], v[252:255], a[212:215], v[164:167]
tr 240,2,25600
tr 242,2,31744
v_mfma_f32_16x16x32_bf16 v[156:159], v[252:255], a[208:211], v[156:159]
tr 248,3,25600
tr 250,3,31744
v_mfma_f32_16x16x32_bf16 v[184:187], v[220:223], a[208:211], v[184:187]
tr 252,4,25600
tr 254,4,31744
v_mfma_f32_16x16x32_bf16 v[192:195], v[220:223], a[212:215], v[192:195]
v_exp_f32 v56, v56
v_mfma_f32_16x16x32_bf16 v[200:203], v[220:223], a[216:219], v[200:203]
v_exp_f32 v57, v57
v_mfma_f32_16x16x32_bf16 v[208:211], v[220:223], a[220:223], v[208:211]
s_add_u32 m0, s17, 147456
s_add_u32 s22, s21, 896
dl 17,4,22,0
v_mfma_f32_16x16x32_bf16 v[212:215], v[216:219], a[220:223], v[212:215]
tr 220,1,26624
tr 222,1,32768
v_mfma_f32_16x16x32_bf16 v[204:207], v[216:219], a[216:219], v[204:207]
v_exp_f32 v58, v58
v_mfma_f32_16x16x32_bf16 v[196:199], v[216:219], a[212:215], v[196:199]
v_exp_f32 v59, v59
v_mfma_f32_16x16x32_bf16 v[188:191], v[216:219], a[208:211], v[188:191]
dl 18,4,22,1024
v_mfma_f32_16x16x32_bf16 a[244:247], a[240:243], v[40:43], a[244:247]
tr 216,2,26624
tr 218,2,32768
v_mfma_f32_16x16x32_bf16 a[248:251], a[240:243], v[48:51], a[248:251]
v_exp_f32 v60, v60
s_waitcnt lgkmcnt(4)
ma 0,224,40
v_exp_f32 v61, v61
ma 8,224,48
dl 17,8,22,2048
ma 4,228,40
tr 224,3,26624
tr 226,3,32768
ma 12,228,48
tr 228,4,26624
tr 230,4,32768
ma 16,232,40
v_exp_f32 v62, v62
ma 24,232,48
dl 18,8,22,3072
ma 20,236,40
tr 232,1,36864
tr 234,1,43008
ma 28,236,48
tr 236,2,36864
tr 238,2,43008
ma 32,244,40
v_exp_f32 v63, v63
ma 40,244,48
cv 56,56,57
cv 57,58,59
ma 36,240,40
s_add_u32 s23, s19, 5505408
cv 58,60,61
ma 44,240,48
cv 59,62,63
v_exp_f32 v64, v64
ma 48,248,40
v_exp_f32 v65, v65
ma 56,248,48
v_exp_f32 v66, v66
s_add_u32 m0, s16, 0
ma 52,252,40
dn 13,60,23,0
ma 60,252,48
v_exp_f32 v67, v67
s_waitcnt lgkmcnt(0)
ma 64,220,40
v_exp_f32 v68, v68
ma 72,220,48
v_exp_f32 v69, v69
ma 68,216,40
dn 13,64,23,1024
ma 76,216,48
v_exp_f32 v70, v70
ma 80,224,40
v_exp_f32 v71, v71
ma 88,224,48
cv 64,64,65
tr 244,3,36864
ma 84,228,40
dn 13,68,23,2048
ma 92,228,48
tr 246,3,43008
cv 65,66,67
v_mfma_f32_16x16x32_bf16 a[244:247], a[240:243], v[56:59], a[244:247]
cv 66,68,69
cv 67,70,71
tr 240,4,36864
tr 242,4,43008
v_mfma_f32_16x16x32_bf16 a[248:251], a[240:243], v[64:67], a[248:251]
tr 248,1,37888
tr 250,1,44032
ma 0,232,56
tr 252,2,37888
tr 254,2,44032
ma 8,232,64
tr 220,3,37888
tr 222,3,44032
ma 4,236,56
tr 216,4,37888
tr 218,4,44032
ma 12,236,64
tr 224,1,38912
tr 226,1,45056
tr 228,2,38912
tr 230,2,45056
s_waitcnt lgkmcnt(14)
ma 16,244,56
s_add_u32 m0, s16, 3072
tr 232,3,38912
dn 14,60,23,0
ma 24,244,64
tr 234,3,45056
tr 236,4,38912
s_waitcnt lgkmcnt(11)
ma 20,240,56
tr 238,4,45056
v_add_u32 v22, 0x2000, v22
ma 28,240,64
v_add_u32 v23, 0x2000, v23
ma 32,248,56
dn 14,64,23,1024
ma 40,248,64
ma 36,252,56
ma 44,252,64
s_waitcnt vmcnt(15) lgkmcnt(0)
s_barrier
ma 48,220,56
dn 14,68,23,2048
ma 56,220,64
tr 244,22,40960
tr 246,22,47104
ma 52,216,56
tr 240,23,40960
tr 242,23,47104
ma 60,216,64
tr 248,22,41984
tr 250,22,48128
ma 64,224,56
tr 252,23,41984
tr 254,23,48128
ma 72,224,64
tr 220,22,43008
tr 222,22,49152
ma 68,228,56
tr 216,23,43008
tr 218,23,49152
ma 76,228,64
s_add_u32 s24, s18, 122880
tr 224,22,53248
ma 80,232,56
tr 226,22,59392
tr 228,23,53248
ma 88,232,64
s_add_u32 m0, s16, 24576
tr 230,23,59392
dn 11,48,24,0
ma 84,236,56
tr 232,22,54272
tr 234,22,60416
ma 92,236,64
tr 236,23,54272
tr 238,23,60416
s_waitcnt lgkmcnt(10)
v_mfma_f32_16x16x32_bf16 a[96:99], v[244:247], a[192:195], a[96:99]
v_mfma_f32_16x16x32_bf16 a[104:107], v[244:247], a[196:199], a[104:107]
dn 11,52,24,1024
v_mfma_f32_16x16x32_bf16 a[112:115], v[244:247], a[200:203], a[112:115]
v_mfma_f32_16x16x32_bf16 a[120:123], v[244:247], a[204:207], a[120:123]
tr 244,22,55296
tr 246,22,61440
v_mfma_f32_16x16x32_bf16 a[124:127], v[240:243], a[204:207], a[124:127]
v_add_u32 v22, 0xffffe000, v22
v_mfma_f32_16x16x32_bf16 a[116:119], v[240:243], a[200:203], a[116:119]
dn 11,56,24,2048
v_mfma_f32_16x16x32_bf16 a[108:111], v[240:243], a[196:199], a[108:111]
v_mfma_f32_16x16x32_bf16 a[100:103], v[240:243], a[192:195], a[100:103]
tr 240,23,55296
tr 242,23,61440
v_mfma_f32_16x16x32_bf16 a[128:131], v[248:251], a[192:195], a[128:131]
v_add_u32 v23, 0xffffe000, v23
v_mfma_f32_16x16x32_bf16 a[136:139], v[248:251], a[196:199], a[136:139]
v_mfma_f32_16x16x32_bf16 a[144:147], v[248:251], a[200:203], a[144:147]
v_mfma_f32_16x16x32_bf16 a[152:155], v[248:251], a[204:207], a[152:155]
v_mfma_f32_16x16x32_bf16 a[156:159], v[252:255], a[204:207], a[156:159]
s_waitcnt vmcnt(13) lgkmcnt(0)
s_barrier
v_mfma_f32_16x16x32_bf16 a[148:151], v[252:255], a[200:203], a[148:151]
rd 248,5,40960
v_mfma_f32_16x16x32_bf16 a[140:143], v[252:255], a[196:199], a[140:143]
v_mfma_f32_16x16x32_bf16 a[132:135], v[252:255], a[192:195], a[132:135]
rd 252,6,40960
v_mfma_f32_16x16x32_bf16 a[160:163], v[220:223], a[192:195], a[160:163]
v_mfma_f32_16x16x32_bf16 a[168:171], v[220:223], a[196:199], a[168:171]
v_mfma_f32_16x16x32_bf16 a[176:179], v[220:223], a[200:203], a[176:179]
v_mfma_f32_16x16x32_bf16 a[184:187], v[220:223], a[204:207], a[184:187]
s_add_u32 m0, s16, 27648
rd 220,5,41984
dn 12,48,24,0
v_mfma_f32_16x16x32_bf16 a[188:191], v[216:219], a[204:207], a[188:191]
v_mfma_f32_16x16x32_bf16 a[180:183], v[216:219], a[200:203], a[180:183]
v_mfma_f32_16x16x32_bf16 a[172:175], v[216:219], a[196:199], a[172:175]
v_mfma_f32_16x16x32_bf16 a[164:167], v[216:219], a[192:195], a[164:167]
dn 12,52,24,1024
v_mfma_f32_16x16x32_bf16 a[96:99], v[224:227], a[208:211], a[96:99]
rd 216,6,41984
v_mfma_f32_16x16x32_bf16 a[104:107], v[224:227], a[212:215], a[104:107]
v_mfma_f32_16x16x32_bf16 a[112:115], v[224:227], a[216:219], a[112:115]
v_mfma_f32_16x16x32_bf16 a[120:123], v[224:227], a[220:223], a[120:123]
dn 12,56,24,2048
v_mfma_f32_16x16x32_bf16 a[124:127], v[228:231], a[220:223], a[124:127]
rd 224,5,43008
v_mfma_f32_16x16x32_bf16 a[116:119], v[228:231], a[216:219], a[116:119]
v_mfma_f32_16x16x32_bf16 a[108:111], v[228:231], a[212:215], a[108:111]
v_mfma_f32_16x16x32_bf16 a[100:103], v[228:231], a[208:211], a[100:103]
rd 228,6,43008
v_mfma_f32_16x16x32_bf16 a[128:131], v[232:235], a[208:211], a[128:131]
v_mfma_f32_16x16x32_bf16 a[136:139], v[232:235], a[212:215], a[136:139]
v_mfma_f32_16x16x32_bf16 a[144:147], v[232:235], a[216:219], a[144:147]
v_mfma_f32_16x16x32_bf16 a[152:155], v[232:235], a[220:223], a[152:155]
rd 232,5,47104
v_mfma_f32_16x16x32_bf16 a[156:159], v[236:239], a[220:223], a[156:159]
v_mfma_f32_16x16x32_bf16 a[148:151], v[236:239], a[216:219], a[148:151]
v_mfma_f32_16x16x32_bf16 a[140:143], v[236:239], a[212:215], a[140:143]
v_mfma_f32_16x16x32_bf16 a[132:135], v[236:239], a[208:211], a[132:135]
rd 236,6,47104
v_mfma_f32_16x16x32_bf16 a[160:163], v[244:247], a[208:211], a[160:163]
v_mfma_f32_16x16x32_bf16 a[168:171], v[244:247], a[212:215], a[168:171]
v_mfma_f32_16x16x32_bf16 a[176:179], v[244:247], a[216:219], a[176:179]
v_mfma_f32_16x16x32_bf16 a[184:187], v[244:247], a[220:223], a[184:187]
s_add_u32 m0, s16, 49152
s_add_u32 s25, s19, 5505792
dn 13,60,25,0
v_mfma_f32_16x16x32_bf16 a[188:191], v[240:243], a[220:223], a[188:191]
rd 244,5,48128
v_mfma_f32_16x16x32_bf16 a[180:183], v[240:243], a[216:219], a[180:183]
v_mfma_f32_16x16x32_bf16 a[172:175], v[240:243], a[212:215], a[172:175]
v_mfma_f32_16x16x32_bf16 a[164:167], v[240:243], a[208:211], a[164:167]
dn 13,64,25,1024
s_waitcnt lgkmcnt(1)
mvc 40,248,72,32
rd 240,6,48128
mv 40,252,80
mvc 48,252,84,36
rd 252,5,49152
mv 48,248,76
dn 13,68,25,2048
mv 48,220,92
rd 248,6,49152
mv 48,216,100
mv 40,216,96
rd 216,5,53248
mv 40,220,88
rd 220,6,53248
mv 40,224,104
mv 40,228,112
mv 48,228,116
rd 228,5,54272
mv 48,224,108
rd 224,6,54272
mvc 44,232,72,32
s_nop 2
v_max3_f32 v20, v40, v41, v42
mv 44,236,80
mvc 52,236,84,36
rd 236,5,55296
v_max3_f32 v20, v20, v43, v48
mv 52,232,76
rd 232,6,55296
v_max3_f32 v20, v20, v49, v50
s_waitcnt lgkmcnt(4)
mv 52,244,92
mv 52,240,100
mv 44,240,96
rd 240,5,59392
mv 44,244,88
s_add_u32 m0, s16, 52224
rd 244,6,59392
dn 14,60,25,0
mv 44,252,104
mv 44,248,112
mv 52,248,116
rd 248,5,60416
mv 52,252,108
dn 14,64,25,1024
mvc 56,216,72,32
rd 252,6,60416
s_nop 1
v_max3_f32 v20, v20, v51, v44
mv 56,220,80
v_max3_f32 v20, v20, v45, v46
mvc 64,220,84,36
rd 220,5,61440
v_max3_f32 v20, v20, v47, v52
mv 64,216,76
dn 14,68,25,2048
s_waitcnt lgkmcnt(3)
mv 64,228,92
rd 216,6,61440
v_max3_f32 v20, v20, v53, v54
mv 64,224,100
v_add_u32 v5, 0xffff8000, v5
v_add_u32 v6, 0xffff8000, v6
mv 56,224,96
mv 56,228,88
mv 56,236,104
mv 56,232,112
s_waitcnt vmcnt(12) lgkmcnt(0)
s_barrier
mv 64,232,116
ds_read_b128 a[192:195], v7 offset:57344
tr 224,22,0
mv 64,236,108
tr 226,22,6144
ds_read_b128 a[196:199], v7 offset:59392
mvc 60,240,72,32
ds_read_b128 a[200:203], v7 offset:61440
ds_read_b128 a[204:207], v7 offset:63488
mv 60,244,80
tr 228,23,0
tr 230,23,6144
mvc 68,244,84,36
tr 232,22,1024
tr 234,22,7168
mv 68,240,76
tr 236,23,1024
tr 238,23,7168
mv 68,248,92
tr 244,22,2048
tr 246,22,8192
mv 68,252,100
tr 240,23,2048
tr 242,23,8192
mv 60,252,96
ds_read_b128 a[208:211], v8 offset:57344
tr 252,22,12288
mv 60,248,88
s_add_u32 m0, s17, 131072
s_add_u32 s22, s21, 1024
dl 17,4,22,0
mv 60,220,104
tr 254,22,18432
ds_read_b128 a[212:215], v8 offset:59392
mv 60,216,112
ds_read_b128 a[216:219], v8 offset:61440
ds_read_b128 a[220:223], v8 offset:63488
mv 68,216,116
tr 248,23,12288
tr 250,23,18432
mv 68,220,108
dl 18,4,22,1024
s_waitcnt lgkmcnt(8)
v_mfma_f32_16x16x32_bf16 v[120:123], v[224:227], a[192:195], v[120:123]
tr 216,22,13312
tr 218,22,19456
v_mfma_f32_16x16x32_bf16 v[128:131], v[224:227], a[196:199], v[128:131]
tr 220,23,13312
tr 222,23,19456
v_mfma_f32_16x16x32_bf16 v[136:139], v[224:227], a[200:203], v[136:139]
v_max3_f32 v20, v20, v55, v56
v_max3_f32 v20, v20, v57, v58
v_mfma_f32_16x16x32_bf16 v[144:147], v[224:227], a[204:207], v[144:147]
dl 17,8,22,2048
v_mfma_f32_16x16x32_bf16 v[148:151], v[228:231], a[204:207], v[148:151]
tr 224,22,14336
tr 226,22,20480
v_mfma_f32_16x16x32_bf16 v[140:143], v[228:231], a[200:203], v[140:143]
v_max3_f32 v20, v20, v59, v64
v_max3_f32 v20, v20, v65, v66
v_mfma_f32_16x16x32_bf16 v[132:135], v[228:231], a[196:199], v[132:135]
v_max3_f32 v20, v20, v67, v60
v_max3_f32 v20, v20, v61, v62
v_mfma_f32_16x16x32_bf16 v[124:127], v[228:231], a[192:195], v[124:127]
dl 18,8,22,3072
v_mfma_f32_16x16x32_bf16 v[152:155], v[232:235], a[192:195], v[152:155]
tr 228,23,14336
tr 230,23,20480
v_mfma_f32_16x16x32_bf16 v[160:163], v[232:235], a[196:199], v[160:163]
v_max3_f32 v20, v20, v63, v68
v_max3_f32 v20, v20, v69, v70
v_mfma_f32_16x16x32_bf16 v[168:171], v[232:235], a[200:203], v[168:171]
v_max_f32 v20, v20, v71
v_cmp_lt_f32 vcc, s27, v20
s_cbranch_vccz Lc4
s_swappc_b64 s[30:31], s[28:29]
Lc4:
v_mfma_f32_16x16x32_bf16 v[176:179], v[232:235], a[204:207], v[176:179]
s_add_u32 s23, s19, 6291840
v_exp_f32 v40, v40
v_mfma_f32_16x16x32_bf16 v[180:183], v[236:239], a[204:207], v[180:183]
v_exp_f32 v41, v41
v_mfma_f32_16x16x32_bf16 v[172:175], v[236:239], a[200:203], v[172:175]
s_waitcnt vmcnt(10) lgkmcnt(0)
s_barrier
v_mfma_f32_16x16x32_bf16 v[164:167], v[236:239], a[196:199], v[164:167]
tr 232,1,24576
tr 234,1,30720
v_mfma_f32_16x16x32_bf16 v[156:159], v[236:239], a[192:195], v[156:159]
s_add_u32 m0, s16, 73728
tr 236,2,24576
dn 13,60,23,0
v_mfma_f32_16x16x32_bf16 v[184:187], v[244:247], a[192:195], v[184:187]
tr 238,2,30720
v_exp_f32 v42, v42
v_mfma_f32_16x16x32_bf16 v[192:195], v[244:247], a[196:199], v[192:195]
v_exp_f32 v43, v43
v_mfma_f32_16x16x32_bf16 v[200:203], v[244:247], a[200:203], v[200:203]
v_exp_f32 v44, v44
v_mfma_f32_16x16x32_bf16 v[208:211], v[244:247], a[204:207], v[208:211]
dn 13,64,23,1024
v_mfma_f32_16x16x32_bf16 v[212:215], v[240:243], a[204:207], v[212:215]
v_exp_f32 v45, v45
v_mfma_f32_16x16x32_bf16 v[204:207], v[240:243], a[200:203], v[204:207]
v_exp_f32 v46, v46
v_mfma_f32_16x16x32_bf16 v[196:199], v[240:243], a[196:199], v[196:199]
v_exp_f32 v47, v47
v_mfma_f32_16x16x32_bf16 v[188:191], v[240:243], a[192:195], v[188:191]
dn 13,68,23,2048
v_mfma_f32_16x16x32_bf16 v[120:123], v[252:255], a[208:211], v[120:123]
cv 40,40,41
cv 41,42,43
v_mfma_f32_16x16x32_bf16 v[128:131], v[252:255], a[212:215], v[128:131]
cv 42,44,45
cv 43,46,47
v_mfma_f32_16x16x32_bf16 v[136:139], v[252:255], a[216:219], v[136:139]
v_exp_f32 v48, v48
v_mfma_f32_16x16x32_bf16 v[144:147], v[252:255], a[220:223], v[144:147]
v_exp_f32 v49, v49
v_mfma_f32_16x16x32_bf16 v[148:151], v[248:251], a[220:223], v[148:151]
v_exp_f32 v50, v50
v_mfma_f32_16x16x32_bf16 v[140:143], v[248:251], a[216:219], v[140:143]
v_exp_f32 v51, v51
v_mfma_f32_16x16x32_bf16 v[132:135], v[248:251], a[212:215], v[132:135]
v_exp_f32 v52, v52
v_mfma_f32_16x16x32_bf16 v[124:127], v[248:251], a[208:211], v[124:127]
v_exp_f32 v53, v53
v_mfma_f32_16x16x32_bf16 v[152:155], v[216:219], a[208:211], v[152:155]
s_add_u32 m0, s16, 76800
v_exp_f32 v54, v54
dn 14,60,23,0
v_mfma_f32_16x16x32_bf16 v[160:163], v[216:219], a[212:215], v[160:163]
v_exp_f32 v55, v55
v_mfma_f32_16x16x32_bf16 v[168:171], v[216:219], a[216:219], v[168:171]
cv 48,48,49
tr 244,3,24576
v_mfma_f32_16x16x32_bf16 v[176:179], v[216:219], a[220:223], v[176:179]
)"
R"(tr 246,3,30720
cv 49,50,51
v_mfma_f32_16x16x32_bf16 v[180:183], v[220:223], a[220:223], v[180:183]
dn 14,64,23,1024
v_mfma_f32_16x16x32_bf16 v[172:175], v[220:223], a[216:219], v[172:175]
cv 50,52,53
cv 51,54,55
v_mfma_f32_16x16x32_bf16 v[164:167], v[220:223], a[212:215], v[164:167]
tr 240,4,24576
tr 242,4,30720
v_mfma_f32_16x16x32_bf16 v[156:159], v[220:223], a[208:211], v[156:159]
tr 252,1,25600
tr 254,1,31744
v_mfma_f32_16x16x32_bf16 v[184:187], v[224:227], a[208:211], v[184:187]
dn 14,68,23,2048
v_mfma_f32_16x16x32_bf16 v[192:195], v[224:227], a[212:215], v[192:195]
tr 248,2,25600
tr 250,2,31744
v_mfma_f32_16x16x32_bf16 v[200:203], v[224:227], a[216:219], v[200:203]
tr 216,3,25600
tr 218,3,31744
v_mfma_f32_16x16x32_bf16 v[208:211], v[224:227], a[220:223], v[208:211]
s_add_u32 s24, s18, 147456
tr 220,4,25600
v_mfma_f32_16x16x32_bf16 v[212:215], v[228:231], a[220:223], v[212:215]
tr 222,4,31744
tr 224,1,26624
v_mfma_f32_16x16x32_bf16 v[204:207], v[228:231], a[216:219], v[204:207]
tr 226,1,32768
v_exp_f32 v56, v56
v_mfma_f32_16x16x32_bf16 v[196:199], v[228:231], a[212:215], v[196:199]
v_exp_f32 v57, v57
v_mfma_f32_16x16x32_bf16 v[188:191], v[228:231], a[208:211], v[188:191]
tr 228,2,26624
tr 230,2,32768
v_mfma_f32_16x16x32_bf16 a[244:247], a[240:243], v[40:43], a[244:247]
v_exp_f32 v58, v58
s_add_u32 m0, s16, 0
v_mfma_f32_16x16x32_bf16 a[248:251], a[240:243], v[48:51], a[248:251]
dn 9,36,24,0
s_waitcnt lgkmcnt(5)
ma 0,232,40
v_exp_f32 v59, v59
ma 8,232,48
tr 232,3,26624
tr 234,3,32768
ma 4,236,40
v_exp_f32 v60, v60
ma 12,236,48
dn 9,40,24,1024
ma 16,244,40
tr 236,4,26624
tr 238,4,32768
ma 24,244,48
tr 244,1,36864
tr 246,1,43008
ma 20,240,40
v_exp_f32 v61, v61
ma 28,240,48
dn 9,44,24,2048
ma 32,252,40
tr 240,2,36864
tr 242,2,43008
ma 40,252,48
v_exp_f32 v62, v62
ma 36,248,40
v_exp_f32 v63, v63
ma 44,248,48
cv 56,56,57
cv 57,58,59
ma 48,216,40
cv 58,60,61
cv 59,62,63
ma 56,216,48
v_exp_f32 v64, v64
s_waitcnt lgkmcnt(2)
ma 52,220,40
v_exp_f32 v65, v65
ma 60,220,48
v_exp_f32 v66, v66
ma 64,224,40
v_exp_f32 v67, v67
ma 72,224,48
v_exp_f32 v68, v68
ma 68,228,40
v_exp_f32 v69, v69
ma 76,228,48
s_add_u32 m0, s16, 3072
v_exp_f32 v70, v70
dn 10,36,24,0
ma 80,232,40
v_exp_f32 v71, v71
ma 88,232,48
cv 64,64,65
tr 252,3,36864
ma 84,236,40
tr 254,3,43008
cv 65,66,67
ma 92,236,48
dn 10,40,24,1024
v_mfma_f32_16x16x32_bf16 a[244:247], a[240:243], v[56:59], a[244:247]
cv 66,68,69
cv 67,70,71
tr 248,4,36864
tr 250,4,43008
v_mfma_f32_16x16x32_bf16 a[248:251], a[240:243], v[64:67], a[248:251]
tr 216,1,37888
tr 218,1,44032
ma 0,244,56
tr 220,2,37888
tr 222,2,44032
dn 10,44,24,2048
ma 8,244,64
tr 224,3,37888
tr 226,3,44032
s_waitcnt lgkmcnt(8)
ma 4,240,56
tr 228,4,37888
tr 230,4,44032
ma 12,240,64
tr 232,1,38912
tr 234,1,45056
ma 16,252,56
tr 236,2,38912
tr 238,2,45056
ma 24,252,64
tr 244,3,38912
tr 246,3,45056
s_waitcnt lgkmcnt(11)
ma 20,248,56
tr 240,4,38912
tr 242,4,45056
ma 28,248,64
v_add_u32 v22, 0x8000, v22
v_add_u32 v23, 0x8000, v23
ma 32,216,56
v_add_u32 v1, 0x2000, v1
v_add_u32 v2, 0x2000, v2
ma 40,216,64
v_add_u32 v3, 0x2000, v3
v_add_u32 v4, 0x2000, v4
s_waitcnt lgkmcnt(6)
ma 36,220,56
ma 44,220,64
s_waitcnt vmcnt(16) lgkmcnt(0)
s_barrier
ma 48,224,56
tr 252,22,16384
tr 254,22,22528
ma 56,224,64
tr 248,23,16384
tr 250,23,22528
ma 52,228,56
tr 216,22,17408
tr 218,22,23552
ma 60,228,64
tr 220,23,17408
tr 222,23,23552
ma 64,232,56
tr 224,22,18432
tr 226,22,24576
ma 72,232,64
tr 228,23,18432
tr 230,23,24576
ma 68,236,56
tr 232,22,28672
tr 234,22,34816
ma 76,236,64
s_add_u32 m0, s16, 24576
s_add_u32 s25, s19, 6292224
dn 13,60,25,0
ma 80,244,56
tr 236,23,28672
tr 238,23,34816
ma 88,244,64
tr 244,22,29696
tr 246,22,35840
ma 84,240,56
ma 92,240,64
dn 13,64,25,1024
s_waitcnt lgkmcnt(6)
v_mfma_f32_16x16x32_bf16 a[96:99], v[252:255], a[192:195], a[96:99]
tr 240,23,29696
tr 242,23,35840
v_mfma_f32_16x16x32_bf16 a[104:107], v[252:255], a[196:199], a[104:107]
v_mfma_f32_16x16x32_bf16 a[112:115], v[252:255], a[200:203], a[112:115]
v_mfma_f32_16x16x32_bf16 a[120:123], v[252:255], a[204:207], a[120:123]
dn 13,68,25,2048
v_mfma_f32_16x16x32_bf16 a[124:127], v[248:251], a[204:207], a[124:127]
tr 252,22,30720
tr 254,22,36864
v_mfma_f32_16x16x32_bf16 a[116:119], v[248:251], a[200:203], a[116:119]
v_mfma_f32_16x16x32_bf16 a[108:111], v[248:251], a[196:199], a[108:111]
v_mfma_f32_16x16x32_bf16 a[100:103], v[248:251], a[192:195], a[100:103]
tr 248,23,30720
tr 250,23,36864
v_mfma_f32_16x16x32_bf16 a[128:131], v[216:219], a[192:195], a[128:131]
v_mfma_f32_16x16x32_bf16 a[136:139], v[216:219], a[196:199], a[136:139]
v_mfma_f32_16x16x32_bf16 a[144:147], v[216:219], a[200:203], a[144:147]
v_mfma_f32_16x16x32_bf16 a[152:155], v[216:219], a[204:207], a[152:155]
v_mfma_f32_16x16x32_bf16 a[156:159], v[220:223], a[204:207], a[156:159]
s_waitcnt vmcnt(9) lgkmcnt(0)
s_barrier
v_mfma_f32_16x16x32_bf16 a[148:151], v[220:223], a[200:203], a[148:151]
tr 216,22,40960
tr 218,22,47104
v_mfma_f32_16x16x32_bf16 a[140:143], v[220:223], a[196:199], a[140:143]
v_mfma_f32_16x16x32_bf16 a[132:135], v[220:223], a[192:195], a[132:135]
tr 220,23,40960
tr 222,23,47104
v_mfma_f32_16x16x32_bf16 a[160:163], v[224:227], a[192:195], a[160:163]
v_mfma_f32_16x16x32_bf16 a[168:171], v[224:227], a[196:199], a[168:171]
v_mfma_f32_16x16x32_bf16 a[176:179], v[224:227], a[200:203], a[176:179]
v_mfma_f32_16x16x32_bf16 a[184:187], v[224:227], a[204:207], a[184:187]
s_add_u32 m0, s16, 27648
tr 224,22,41984
dn 14,60,25,0
v_mfma_f32_16x16x32_bf16 a[188:191], v[228:231], a[204:207], a[188:191]
ds_read_b128 a[204:207], v7 offset:47104
tr 226,22,48128
v_mfma_f32_16x16x32_bf16 a[180:183], v[228:231], a[200:203], a[180:183]
ds_read_b128 a[200:203], v7 offset:45056
v_mfma_f32_16x16x32_bf16 a[172:175], v[228:231], a[196:199], a[172:175]
ds_read_b128 a[196:199], v7 offset:43008
v_mfma_f32_16x16x32_bf16 a[164:167], v[228:231], a[192:195], a[164:167]
dn 14,64,25,1024
v_mfma_f32_16x16x32_bf16 a[96:99], v[232:235], a[208:211], a[96:99]
ds_read_b128 a[192:195], v7 offset:40960
tr 228,23,41984
v_mfma_f32_16x16x32_bf16 a[104:107], v[232:235], a[212:215], a[104:107]
tr 230,23,48128
v_mfma_f32_16x16x32_bf16 a[112:115], v[232:235], a[216:219], a[112:115]
v_mfma_f32_16x16x32_bf16 a[120:123], v[232:235], a[220:223], a[120:123]
dn 14,68,25,2048
v_mfma_f32_16x16x32_bf16 a[124:127], v[236:239], a[220:223], a[124:127]
tr 232,22,43008
tr 234,22,49152
v_mfma_f32_16x16x32_bf16 a[116:119], v[236:239], a[216:219], a[116:119]
v_mfma_f32_16x16x32_bf16 a[108:111], v[236:239], a[212:215], a[108:111]
v_mfma_f32_16x16x32_bf16 a[100:103], v[236:239], a[208:211], a[100:103]
tr 236,23,43008
tr 238,23,49152
v_mfma_f32_16x16x32_bf16 a[128:131], v[244:247], a[208:211], a[128:131]
v_mfma_f32_16x16x32_bf16 a[136:139], v[244:247], a[212:215], a[136:139]
v_mfma_f32_16x16x32_bf16 a[144:147], v[244:247], a[216:219], a[144:147]
v_mfma_f32_16x16x32_bf16 a[152:155], v[244:247], a[220:223], a[152:155]
tr 244,22,53248
tr 246,22,59392
v_mfma_f32_16x16x32_bf16 a[156:159], v[240:243], a[220:223], a[156:159]
v_mfma_f32_16x16x32_bf16 a[148:151], v[240:243], a[216:219], a[148:151]
v_mfma_f32_16x16x32_bf16 a[140:143], v[240:243], a[212:215], a[140:143]
v_mfma_f32_16x16x32_bf16 a[132:135], v[240:243], a[208:211], a[132:135]
tr 240,23,53248
tr 242,23,59392
v_mfma_f32_16x16x32_bf16 a[160:163], v[252:255], a[208:211], a[160:163]
v_mfma_f32_16x16x32_bf16 a[168:171], v[252:255], a[212:215], a[168:171]
v_mfma_f32_16x16x32_bf16 a[176:179], v[252:255], a[216:219], a[176:179]
v_mfma_f32_16x16x32_bf16 a[184:187], v[252:255], a[220:223], a[184:187]
s_add_u32 m0, s16, 49152
s_add_u32 s22, s18, 147456
dn 11,48,22,0
v_mfma_f32_16x16x32_bf16 a[188:191], v[248:251], a[220:223], a[188:191]
ds_read_b128 a[220:223], v8 offset:47104
tr 252,22,54272
v_mfma_f32_16x16x32_bf16 a[180:183], v[248:251], a[216:219], a[180:183]
ds_read_b128 a[216:219], v8 offset:45056
tr 254,22,60416
v_mfma_f32_16x16x32_bf16 a[172:175], v[248:251], a[212:215], a[172:175]
ds_read_b128 a[212:215], v8 offset:43008
v_mfma_f32_16x16x32_bf16 a[164:167], v[248:251], a[208:211], a[164:167]
dn 11,52,22,1024
s_waitcnt lgkmcnt(5)
v_mfma_f32_16x16x32_bf16 v[120:123], v[216:219], a[192:195], v[120:123]
ds_read_b128 a[208:211], v8 offset:40960
tr 248,23,54272
v_mfma_f32_16x16x32_bf16 v[128:131], v[216:219], a[196:199], v[128:131]
tr 250,23,60416
v_mfma_f32_16x16x32_bf16 v[136:139], v[216:219], a[200:203], v[136:139]
v_mfma_f32_16x16x32_bf16 v[144:147], v[216:219], a[204:207], v[144:147]
dn 11,56,22,2048
v_mfma_f32_16x16x32_bf16 v[148:151], v[220:223], a[204:207], v[148:151]
tr 216,22,55296
tr 218,22,61440
v_mfma_f32_16x16x32_bf16 v[140:143], v[220:223], a[200:203], v[140:143]
v_add_u32 v22, 0xffff8000, v22
v_mfma_f32_16x16x32_bf16 v[132:135], v[220:223], a[196:199], v[132:135]
v_mfma_f32_16x16x32_bf16 v[124:127], v[220:223], a[192:195], v[124:127]
tr 220,23,55296
tr 222,23,61440
v_mfma_f32_16x16x32_bf16 v[152:155], v[224:227], a[192:195], v[152:155]
v_add_u32 v23, 0xffff8000, v23
v_mfma_f32_16x16x32_bf16 v[160:163], v[224:227], a[196:199], v[160:163]
v_mfma_f32_16x16x32_bf16 v[168:171], v[224:227], a[200:203], v[168:171]
v_mfma_f32_16x16x32_bf16 v[176:179], v[224:227], a[204:207], v[176:179]
v_mfma_f32_16x16x32_bf16 v[180:183], v[228:231], a[204:207], v[180:183]
s_waitcnt vmcnt(9) lgkmcnt(0)
s_barrier
v_mfma_f32_16x16x32_bf16 v[172:175], v[228:231], a[200:203], v[172:175]
rd 224,5,0
v_mfma_f32_16x16x32_bf16 v[164:167], v[228:231], a[196:199], v[164:167]
v_mfma_f32_16x16x32_bf16 v[156:159], v[228:231], a[192:195], v[156:159]
rd 228,6,0
v_mfma_f32_16x16x32_bf16 v[184:187], v[232:235], a[192:195], v[184:187]
v_mfma_f32_16x16x32_bf16 v[192:195], v[232:235], a[196:199], v[192:195]
v_mfma_f32_16x16x32_bf16 v[200:203], v[232:235], a[200:203], v[200:203]
v_mfma_f32_16x16x32_bf16 v[208:211], v[232:235], a[204:207], v[208:211]
s_add_u32 m0, s16, 52224
rd 232,5,1024
dn 12,48,22,0
v_mfma_f32_16x16x32_bf16 v[212:215], v[236:239], a[204:207], v[212:215]
v_mfma_f32_16x16x32_bf16 v[204:207], v[236:239], a[200:203], v[204:207]
v_mfma_f32_16x16x32_bf16 v[196:199], v[236:239], a[196:199], v[196:199]
v_mfma_f32_16x16x32_bf16 v[188:191], v[236:239], a[192:195], v[188:191]
dn 12,52,22,1024
v_mfma_f32_16x16x32_bf16 v[120:123], v[244:247], a[208:211], v[120:123]
rd 236,6,1024
v_mfma_f32_16x16x32_bf16 v[128:131], v[244:247], a[212:215], v[128:131]
v_mfma_f32_16x16x32_bf16 v[136:139], v[244:247], a[216:219], v[136:139]
v_mfma_f32_16x16x32_bf16 v[144:147], v[244:247], a[220:223], v[144:147]
dn 12,56,22,2048
v_mfma_f32_16x16x32_bf16 v[148:151], v[240:243], a[220:223], v[148:151]
rd 244,5,2048
v_mfma_f32_16x16x32_bf16 v[140:143], v[240:243], a[216:219], v[140:143]
v_mfma_f32_16x16x32_bf16 v[132:135], v[240:243], a[212:215], v[132:135]
v_mfma_f32_16x16x32_bf16 v[124:127], v[240:243], a[208:211], v[124:127]
rd 240,6,2048
v_mfma_f32_16x16x32_bf16 v[152:155], v[252:255], a[208:211], v[152:155]
v_mfma_f32_16x16x32_bf16 v[160:163], v[252:255], a[212:215], v[160:163]
v_mfma_f32_16x16x32_bf16 v[168:171], v[252:255], a[216:219], v[168:171]
v_mfma_f32_16x16x32_bf16 v[176:179], v[252:255], a[220:223], v[176:179]
rd 252,5,6144
v_mfma_f32_16x16x32_bf16 v[180:183], v[248:251], a[220:223], v[180:183]
v_mfma_f32_16x16x32_bf16 v[172:175], v[248:251], a[216:219], v[172:175]
v_mfma_f32_16x16x32_bf16 v[164:167], v[248:251], a[212:215], v[164:167]
v_mfma_f32_16x16x32_bf16 v[156:159], v[248:251], a[208:211], v[156:159]
rd 248,6,6144
v_mfma_f32_16x16x32_bf16 v[184:187], v[216:219], a[208:211], v[184:187]
v_mfma_f32_16x16x32_bf16 v[192:195], v[216:219], a[212:215], v[192:195]
v_mfma_f32_16x16x32_bf16 v[200:203], v[216:219], a[216:219], v[200:203]
v_mfma_f32_16x16x32_bf16 v[208:211], v[216:219], a[220:223], v[208:211]
s_add_u32 m0, s17, 147456
s_add_u32 s23, s21, 1152
dl 17,4,23,0
v_mfma_f32_16x16x32_bf16 v[212:215], v[220:223], a[220:223], v[212:215]
rd 216,5,7168
v_mfma_f32_16x16x32_bf16 v[204:207], v[220:223], a[216:219], v[204:207]
v_mfma_f32_16x16x32_bf16 v[196:199], v[220:223], a[212:215], v[196:199]
v_mfma_f32_16x16x32_bf16 v[188:191], v[220:223], a[208:211], v[188:191]
dl 18,4,23,1024
s_waitcnt lgkmcnt(1)
mvc 40,224,72,32
rd 220,6,7168
mv 40,228,80
mvc 48,228,84,36
rd 228,5,8192
mv 48,224,76
dl 17,8,23,2048
mv 48,232,92
rd 224,6,8192
mv 48,236,100
mv 40,236,96
rd 236,5,12288
mv 40,232,88
dl 18,8,23,3072
mv 40,244,104
rd 232,6,12288
mv 40,240,112
mv 48,240,116
rd 240,5,13312
mv 48,244,108
s_add_u32 s24, s19, 7078272
rd 244,6,13312
mvc 44,252,72,32
s_nop 1
v_max3_f32 v20, v40, v41, v42
mv 44,248,80
mvc 52,248,84,36
rd 248,5,14336
v_max3_f32 v20, v20, v43, v48
mv 52,252,76
rd 252,6,14336
v_max3_f32 v20, v20, v49, v50
s_waitcnt lgkmcnt(4)
mv 52,216,92
s_add_u32 m0, s16, 73728
s_nop 0
dn 13,60,24,0
mv 52,220,100
mv 44,220,96
rd 220,5,18432
mv 44,216,88
rd 216,6,18432
mv 44,228,104
dn 13,64,24,1024
mv 44,224,112
mv 52,224,116
rd 224,5,19456
mv 52,228,108
rd 228,6,19456
mvc 56,236,72,32
dn 13,68,24,2048
mv 56,232,80
s_nop 0
v_max3_f32 v20, v20, v51, v44
v_max3_f32 v20, v20, v45, v46
mvc 64,232,84,36
rd 232,5,20480
v_max3_f32 v20, v20, v47, v52
mv 64,236,76
rd 236,6,20480
v_max3_f32 v20, v20, v53, v54
s_waitcnt lgkmcnt(4)
mv 64,240,92
mv 64,244,100
mv 56,244,96
mv 56,240,88
s_waitcnt vmcnt(13) lgkmcnt(0)
s_barrier
mv 56,248,104
s_add_u32 m0, s16, 76800
tr 244,22,24576
dn 14,60,24,0
mv 56,252,112
tr 246,22,30720
tr 240,23,24576
mv 64,252,116
tr 242,23,30720
tr 252,22,25600
mv 64,248,108
tr 254,22,31744
tr 248,23,25600
mvc 60,220,72,32
dn 14,64,24,1024
mv 60,216,80
tr 250,23,31744
v_max3_f32 v20, v20, v55, v56
mvc 68,216,84,36
tr 216,22,26624
tr 218,22,32768
mv 68,220,76
tr 220,23,26624
tr 222,23,32768
mv 68,224,92
dn 14,68,24,2048
mv 68,228,100
v_max3_f32 v20, v20, v57, v58
v_max3_f32 v20, v20, v59, v64
mv 60,228,96
tr 228,22,36864
tr 230,22,43008
mv 60,224,88
s_add_u32 s25, s18, 172032
tr 224,23,36864
mv 60,232,104
tr 226,23,43008
v_max3_f32 v20, v20, v65, v66
mv 60,236,112
mv 68,236,116
tr 236,22,37888
tr 238,22,44032
mv 68,232,108
tr 232,23,37888
tr 234,23,44032
s_waitcnt lgkmcnt(8)
v_mfma_f32_16x16x32_bf16 a[96:99], v[244:247], a[192:195], a[96:99]
v_max3_f32 v20, v20, v67, v60
v_max3_f32 v20, v20, v61, v62
s_add_u32 m0, s16, 0
v_mfma_f32_16x16x32_bf16 a[104:107], v[244:247], a[196:199], a[104:107]
dn 9,36,25,0
v_mfma_f32_16x16x32_bf16 a[112:115], v[244:247], a[200:203], a[112:115]
v_max3_f32 v20, v20, v63, v68
v_max3_f32 v20, v20, v69, v70
v_mfma_f32_16x16x32_bf16 a[120:123], v[244:247], a[204:207], a[120:123]
tr 244,22,38912
tr 246,22,45056
v_mfma_f32_16x16x32_bf16 a[124:127], v[240:243], a[204:207], a[124:127]
v_max_f32 v20, v20, v71
v_cmp_lt_f32 vcc, s27, v20
s_cbranch_vccz Lc5
s_swappc_b64 s[30:31], s[28:29]
Lc5:
v_mfma_f32_16x16x32_bf16 a[116:119], v[240:243], a[200:203], a[116:119]
dn 9,40,25,1024
v_mfma_f32_16x16x32_bf16 a[108:111], v[240:243], a[196:199], a[108:111]
v_exp_f32 v40, v40
v_mfma_f32_16x16x32_bf16 a[100:103], v[240:243], a[192:195], a[100:103]
tr 240,23,38912
tr 242,23,45056
v_mfma_f32_16x16x32_bf16 a[128:131], v[252:255], a[192:195], a[128:131]
v_exp_f32 v41, v41
v_mfma_f32_16x16x32_bf16 a[136:139], v[252:255], a[196:199], a[136:139]
dn 9,44,25,2048
v_mfma_f32_16x16x32_bf16 a[144:147], v[252:255], a[200:203], a[144:147]
v_exp_f32 v42, v42
v_mfma_f32_16x16x32_bf16 a[152:155], v[252:255], a[204:207], a[152:155]
v_exp_f32 v43, v43
v_mfma_f32_16x16x32_bf16 a[156:159], v[248:251], a[204:207], a[156:159]
s_waitcnt vmcnt(13) lgkmcnt(0)
s_barrier
v_mfma_f32_16x16x32_bf16 a[148:151], v[248:251], a[200:203], a[148:151]
tr 252,1,40960
tr 254,1,47104
v_mfma_f32_16x16x32_bf16 a[140:143], v[248:251], a[196:199], a[140:143]
v_exp_f32 v44, v44
v_mfma_f32_16x16x32_bf16 a[132:135], v[248:251], a[192:195], a[132:135]
tr 248,2,40960
tr 250,2,47104
v_mfma_f32_16x16x32_bf16 a[160:163], v[216:219], a[192:195], a[160:163]
v_exp_f32 v45, v45
v_mfma_f32_16x16x32_bf16 a[168:171], v[216:219], a[196:199], a[168:171]
v_exp_f32 v46, v46
v_mfma_f32_16x16x32_bf16 a[176:179], v[216:219], a[200:203], a[176:179]
v_exp_f32 v47, v47
v_mfma_f32_16x16x32_bf16 a[184:187], v[216:219], a[204:207], a[184:187]
s_add_u32 m0, s16, 3072
cv 40,40,41
dn 10,36,25,0
v_mfma_f32_16x16x32_bf16 a[188:191], v[220:223], a[204:207], a[188:191]
cv 41,42,43
cv 42,44,45
v_mfma_f32_16x16x32_bf16 a[180:183], v[220:223], a[200:203], a[180:183]
cv 43,46,47
v_exp_f32 v48, v48
v_mfma_f32_16x16x32_bf16 a[172:175], v[220:223], a[196:199], a[172:175]
v_exp_f32 v49, v49
v_mfma_f32_16x16x32_bf16 a[164:167], v[220:223], a[192:195], a[164:167]
dn 10,40,25,1024
v_mfma_f32_16x16x32_bf16 a[96:99], v[228:231], a[208:211], a[96:99]
v_exp_f32 v50, v50
v_mfma_f32_16x16x32_bf16 a[104:107], v[228:231], a[212:215], a[104:107]
v_exp_f32 v51, v51
v_mfma_f32_16x16x32_bf16 a[112:115], v[228:231], a[216:219], a[112:115]
v_exp_f32 v52, v52
v_mfma_f32_16x16x32_bf16 a[120:123], v[228:231], a[220:223], a[120:123]
dn 10,44,25,2048
v_mfma_f32_16x16x32_bf16 a[124:127], v[224:227], a[220:223], a[124:127]
v_exp_f32 v53, v53
v_mfma_f32_16x16x32_bf16 a[116:119], v[224:227], a[216:219], a[116:119]
v_exp_f32 v54, v54
v_mfma_f32_16x16x32_bf16 a[108:111], v[224:227], a[212:215], a[108:111]
v_exp_f32 v55, v55
v_mfma_f32_16x16x32_bf16 a[100:103], v[224:227], a[208:211], a[100:103]
cv 48,48,49
tr 216,3,40960
v_mfma_f32_16x16x32_bf16 a[128:131], v[236:239], a[208:211], a[128:131]
tr 218,3,47104
cv 49,50,51
v_mfma_f32_16x16x32_bf16 a[136:139], v[236:239], a[212:215], a[136:139]
cv 50,52,53
cv 51,54,55
v_mfma_f32_16x16x32_bf16 a[144:147], v[236:239], a[216:219], a[144:147]
tr 220,4,40960
tr 222,4,47104
v_mfma_f32_16x16x32_bf16 a[152:155], v[236:239], a[220:223], a[152:155]
tr 228,1,41984
tr 230,1,48128
v_mfma_f32_16x16x32_bf16 a[156:159], v[232:235], a[220:223], a[156:159]
tr 224,2,41984
tr 226,2,48128
v_mfma_f32_16x16x32_bf16 a[148:151], v[232:235], a[216:219], a[148:151]
tr 236,3,41984
tr 238,3,48128
v_mfma_f32_16x16x32_bf16 a[140:143], v[232:235], a[212:215], a[140:143]
v_exp_f32 v56, v56
v_mfma_f32_16x16x32_bf16 a[132:135], v[232:235], a[208:211], a[132:135]
tr 232,4,41984
tr 234,4,48128
v_mfma_f32_16x16x32_bf16 a[160:163], v[244:247], a[208:211], a[160:163]
v_exp_f32 v57, v57
v_mfma_f32_16x16x32_bf16 a[168:171], v[244:247], a[212:215], a[168:171]
v_exp_f32 v58, v58
v_mfma_f32_16x16x32_bf16 a[176:179], v[244:247], a[216:219], a[176:179]
v_exp_f32 v59, v59
v_mfma_f32_16x16x32_bf16 a[184:187], v[244:247], a[220:223], a[184:187]
s_add_u32 m0, s16, 24576
s_add_u32 s22, s19, 7078656
dn 13,60,22,0
v_mfma_f32_16x16x32_bf16 a[188:191], v[240:243], a[220:223], a[188:191]
tr 244,1,43008
tr 246,1,49152
v_mfma_f32_16x16x32_bf16 a[180:183], v[240:243], a[216:219], a[180:183]
v_exp_f32 v60, v60
v_mfma_f32_16x16x32_bf16 a[172:175], v[240:243], a[212:215], a[172:175]
v_exp_f32 v61, v61
v_mfma_f32_16x16x32_bf16 a[164:167], v[240:243], a[208:211], a[164:167]
dn 13,64,22,1024
v_mfma_f32_16x16x32_bf16 a[244:247], a[240:243], v[40:43], a[244:247]
tr 240,2,43008
tr 242,2,49152
v_mfma_f32_16x16x32_bf16 a[248:251], a[240:243], v[48:51], a[248:251]
v_exp_f32 v62, v62
s_waitcnt lgkmcnt(4)
ma 0,252,40
v_exp_f32 v63, v63
ma 8,252,48
dn 13,68,22,2048
ma 4,248,40
tr 252,3,43008
tr 254,3,49152
ma 12,248,48
tr 248,4,43008
tr 250,4,49152
ma 16,216,40
cv 56,56,57
cv 57,58,59
ma 24,216,48
tr 216,1,53248
tr 218,1,59392
ma 20,220,40
cv 58,60,61
cv 59,62,63
ma 28,220,48
tr 220,2,53248
tr 222,2,59392
ma 32,228,40
v_exp_f32 v64, v64
ma 40,228,48
v_exp_f32 v65, v65
ma 36,224,40
v_exp_f32 v66, v66
ma 44,224,48
v_exp_f32 v67, v67
ma 48,236,40
v_exp_f32 v68, v68
ma 56,236,48
v_exp_f32 v69, v69
ma 52,232,40
v_exp_f32 v70, v70
ma 60,232,48
v_exp_f32 v71, v71
s_waitcnt lgkmcnt(0)
ma 64,244,40
cv 64,64,65
tr 228,3,53248
ma 72,244,48
tr 230,3,59392
cv 65,66,67
ma 68,240,40
cv 66,68,69
cv 67,70,71
ma 76,240,48
s_add_u32 m0, s16, 27648
tr 224,4,53248
dn 14,60,22,0
ma 80,252,40
tr 226,4,59392
tr 236,1,54272
ma 88,252,48
tr 238,1,60416
tr 232,2,54272
ma 84,248,40
tr 234,2,60416
tr 244,3,54272
ma 92,248,48
dn 14,64,22,1024
v_mfma_f32_16x16x32_bf16 a[244:247], a[240:243], v[56:59], a[244:247]
tr 246,3,60416
tr 240,4,54272
v_mfma_f32_16x16x32_bf16 a[248:251], a[240:243], v[64:67], a[248:251]
tr 242,4,60416
tr 252,1,55296
ma 0,216,56
tr 254,1,61440
tr 248,2,55296
ma 8,216,64
dn 14,68,22,2048
ma 4,220,56
tr 250,2,61440
tr 216,3,55296
ma 12,220,64
tr 218,3,61440
tr 220,4,55296
s_waitcnt lgkmcnt(10)
ma 16,228,56
tr 222,4,61440
v_add_u32 v22, 0x8000, v22
ma 24,228,64
v_add_u32 v23, 0x8000, v23
ma 20,224,56
ma 28,224,64
ma 32,236,56
ma 40,236,64
s_waitcnt vmcnt(12) lgkmcnt(0)
s_barrier
ma 36,232,56
ds_read_b128 a[192:195], v7 offset:57344
tr 228,22,40960
ma 44,232,64
tr 230,22,47104
ds_read_b128 a[196:199], v7 offset:59392
ma 48,244,56
ds_read_b128 a[200:203], v7 offset:61440
ds_read_b128 a[204:207], v7 offset:63488
ma 56,244,64
tr 224,23,40960
tr 226,23,47104
ma 52,240,56
tr 236,22,41984
tr 238,22,48128
ma 60,240,64
tr 232,23,41984
tr 234,23,48128
ma 64,252,56
tr 244,22,43008
tr 246,22,49152
ma 72,252,64
tr 240,23,43008
tr 242,23,49152
ma 68,248,56
ds_read_b128 a[208:211], v8 offset:57344
tr 252,22,53248
ma 76,248,64
s_add_u32 m0, s16, 49152
s_add_u32 s23, s18, 172032
dn 11,48,23,0
ma 80,216,56
tr 254,22,59392
ds_read_b128 a[212:215], v8 offset:59392
ma 88,216,64
ds_read_b128 a[216:219], v8 offset:61440
ds_read_b128 a[220:223], v8 offset:63488
ma 84,220,56
tr 248,23,53248
tr 250,23,59392
ma 92,220,64
dn 11,52,23,1024
s_waitcnt lgkmcnt(8)
v_mfma_f32_16x16x32_bf16 v[120:123], v[228:231], a[192:195], v[120:123]
tr 216,22,54272
tr 218,22,60416
v_mfma_f32_16x16x32_bf16 v[128:131], v[228:231], a[196:199], v[128:131]
tr 220,23,54272
tr 222,23,60416
v_mfma_f32_16x16x32_bf16 v[136:139], v[228:231], a[200:203], v[136:139]
v_mfma_f32_16x16x32_bf16 v[144:147], v[228:231], a[204:207], v[144:147]
dn 11,56,23,2048
v_mfma_f32_16x16x32_bf16 v[148:151], v[224:227], a[204:207], v[148:151]
tr 228,22,55296
tr 230,22,61440
v_mfma_f32_16x16x32_bf16 v[140:143], v[224:227], a[200:203], v[140:143]
v_add_u32 v22, 0xffff8000, v22
v_mfma_f32_16x16x32_bf16 v[132:135], v[224:227], a[196:199], v[132:135]
v_mfma_f32_16x16x32_bf16 v[124:127], v[224:227], a[192:195], v[124:127]
tr 224,23,55296
tr 226,23,61440
v_mfma_f32_16x16x32_bf16 v[152:155], v[236:239], a[192:195], v[152:155]
v_add_u32 v23, 0xffff8000, v23
v_mfma_f32_16x16x32_bf16 v[160:163], v[236:239], a[196:199], v[160:163]
v_mfma_f32_16x16x32_bf16 v[168:171], v[236:239], a[200:203], v[168:171]
v_mfma_f32_16x16x32_bf16 v[176:179], v[236:239], a[204:207], v[176:179]
v_mfma_f32_16x16x32_bf16 v[180:183], v[232:235], a[204:207], v[180:183]
s_waitcnt vmcnt(9) lgkmcnt(0)
s_barrier
v_mfma_f32_16x16x32_bf16 v[172:175], v[232:235], a[200:203], v[172:175]
rd 236,5,0
v_mfma_f32_16x16x32_bf16 v[164:167], v[232:235], a[196:199], v[164:167]
v_mfma_f32_16x16x32_bf16 v[156:159], v[232:235], a[192:195], v[156:159]
rd 232,6,0
v_mfma_f32_16x16x32_bf16 v[184:187], v[244:247], a[192:195], v[184:187]
v_mfma_f32_16x16x32_bf16 v[192:195], v[244:247], a[196:199], v[192:195]
v_mfma_f32_16x16x32_bf16 v[200:203], v[244:247], a[200:203], v[200:203]
v_mfma_f32_16x16x32_bf16 v[208:211], v[244:247], a[204:207], v[208:211]
s_add_u32 m0, s16, 52224
rd 244,5,1024
dn 12,48,23,0
v_mfma_f32_16x16x32_bf16 v[212:215], v[240:243], a[204:207], v[212:215]
v_mfma_f32_16x16x32_bf16 v[204:207], v[240:243], a[200:203], v[204:207]
v_mfma_f32_16x16x32_bf16 v[196:199], v[240:243], a[196:199], v[196:199]
v_mfma_f32_16x16x32_bf16 v[188:191], v[240:243], a[192:195], v[188:191]
dn 12,52,23,1024
v_mfma_f32_16x16x32_bf16 v[120:123], v[252:255], a[208:211], v[120:123]
rd 240,6,1024
v_mfma_f32_16x16x32_bf16 v[128:131], v[252:255], a[212:215], v[128:131]
v_mfma_f32_16x16x32_bf16 v[136:139], v[252:255], a[216:219], v[136:139]
v_mfma_f32_16x16x32_bf16 v[144:147], v[252:255], a[220:223], v[144:147]
dn 12,56,23,2048
v_mfma_f32_16x16x32_bf16 v[148:151], v[248:251], a[220:223], v[148:151]
rd 252,5,2048
s_nop 0
v_mul_f32 v120, s26, v120
v_mfma_f32_16x16x32_bf16 v[140:143], v[248:251], a[216:219], v[140:143]
v_mul_f32 v121, s26, v121
v_mul_f32 v128, s26, v128
v_mfma_f32_16x16x32_bf16 v[132:135], v[248:251], a[212:215], v[132:135]
v_mul_f32 v129, s26, v129
v_mul_f32 v136, s26, v136
v_mfma_f32_16x16x32_bf16 v[124:127], v[248:251], a[208:211], v[124:127]
rd 248,6,2048
v_mul_f32 v137, s26, v137
v_mfma_f32_16x16x32_bf16 v[152:155], v[216:219], a[208:211], v[152:155]
v_mul_f32 v144, s26, v144
v_mul_f32 v145, s26, v145
v_mfma_f32_16x16x32_bf16 v[160:163], v[216:219], a[212:215], v[160:163]
v_mul_f32 v122, s26, v122
v_mul_f32 v123, s26, v123
v_mfma_f32_16x16x32_bf16 v[168:171], v[216:219], a[216:219], v[168:171]
cv 120,120,121
v_mul_f32 v130, s26, v130
v_mfma_f32_16x16x32_bf16 v[176:179], v[216:219], a[220:223], v[176:179]
rd 216,5,6144
v_mul_f32 v152, s26, v152
v_mfma_f32_16x16x32_bf16 v[180:183], v[220:223], a[220:223], v[180:183]
v_mul_f32 v153, s26, v153
v_mul_f32 v160, s26, v160
v_mfma_f32_16x16x32_bf16 v[172:175], v[220:223], a[216:219], v[172:175]
v_mul_f32 v161, s26, v161
v_mul_f32 v168, s26, v168
v_mfma_f32_16x16x32_bf16 v[164:167], v[220:223], a[212:215], v[164:167]
v_mul_f32 v169, s26, v169
v_mul_f32 v176, s26, v176
v_mfma_f32_16x16x32_bf16 v[156:159], v[220:223], a[208:211], v[156:159]
rd 220,6,6144
v_mul_f32 v177, s26, v177
v_mfma_f32_16x16x32_bf16 v[184:187], v[228:231], a[208:211], v[184:187]
v_mul_f32 v131, s26, v131
cv 128,128,129
v_mfma_f32_16x16x32_bf16 v[192:195], v[228:231], a[212:215], v[192:195]
v_mul_f32 v138, s26, v138
v_mul_f32 v139, s26, v139
v_mfma_f32_16x16x32_bf16 v[200:203], v[228:231], a[216:219], v[200:203]
cv 136,136,137
v_mul_f32 v146, s26, v146
v_mfma_f32_16x16x32_bf16 v[208:211], v[228:231], a[220:223], v[208:211]
s_add_u32 m0, s16, 73728
s_add_u32 s24, s18, 196608
dn 9,36,24,0
v_mfma_f32_16x16x32_bf16 v[212:215], v[224:227], a[220:223], v[212:215]
rd 228,5,7168
v_mul_f32 v184, s26, v184
v_mfma_f32_16x16x32_bf16 v[204:207], v[224:227], a[216:219], v[204:207]
v_mul_f32 v185, s26, v185
v_mul_f32 v192, s26, v192
v_mfma_f32_16x16x32_bf16 v[196:199], v[224:227], a[212:215], v[196:199]
v_mul_f32 v193, s26, v193
v_mul_f32 v200, s26, v200
v_mfma_f32_16x16x32_bf16 v[188:191], v[224:227], a[208:211], v[188:191]
dn 9,40,24,1024
s_waitcnt lgkmcnt(1)
mvc 40,236,72,32
rd 224,6,7168
v_mul_f32 v201, s26, v201
mv 40,232,80
v_mul_f32 v208, s26, v208
v_mul_f32 v209, s26, v209
mvc 48,232,84,36
rd 232,5,8192
v_mul_f32 v147, s26, v147
mv 48,236,76
dn 9,44,24,2048
mv 48,244,92
rd 236,6,8192
cv 144,144,145
mv 48,240,100
v_mul_f32 v154, s26, v154
v_mul_f32 v155, s26, v155
mv 40,240,96
rd 240,5,12288
cv 152,152,153
mv 40,244,88
rd 244,6,12288
v_mul_f32 v162, s26, v162
mv 40,252,104
v_mul_f32 v163, s26, v163
cv 160,160,161
mv 40,248,112
v_mul_f32 v170, s26, v170
v_mul_f32 v171, s26, v171
mv 48,248,116
rd 248,5,13312
cv 168,168,169
mv 48,252,108
rd 252,6,13312
v_mul_f32 v178, s26, v178
mvc 44,216,72,32
v_max3_f32 v20, v40, v41, v42
v_mul_f32 v179, s26, v179
mv 44,220,80
cv 176,176,177
v_mul_f32 v186, s26, v186
mvc 52,220,84,36
rd 220,5,14336
v_max3_f32 v20, v20, v43, v48
mv 52,216,76
rd 216,6,14336
v_max3_f32 v20, v20, v49, v50
s_waitcnt lgkmcnt(4)
mv 52,228,92
v_mul_f32 v187, s26, v187
cv 184,184,185
mv 52,224,100
v_mul_f32 v194, s26, v194
v_mul_f32 v195, s26, v195
mv 44,224,96
rd 224,5,18432
cv 192,192,193
mv 44,228,88
s_add_u32 m0, s16, 76800
rd 228,6,18432
dn 10,36,24,0
mv 44,232,104
v_mul_f32 v202, s26, v202
v_mul_f32 v203, s26, v203
mv 44,236,112
cv 200,200,201
v_mul_f32 v210, s26, v210
mv 52,236,116
rd 236,5,19456
v_mul_f32 v211, s26, v211
mv 52,232,108
dn 10,40,24,1024
mvc 56,240,72,32
rd 232,6,19456
v_max3_f32 v20, v20, v51, v44
mv 56,244,80
v_max3_f32 v20, v20, v45, v46
cv 208,208,209
mvc 64,244,84,36
rd 244,5,20480
v_max3_f32 v20, v20, v47, v52
mv 64,240,76
dn 10,44,24,2048
s_waitcnt lgkmcnt(3)
mv 64,248,92
rd 240,6,20480
v_max3_f32 v20, v20, v53, v54
mv 64,252,100
v_mul_f32 v124, s26, v124
v_mul_f32 v125, s26, v125
mv 56,252,96
v_mul_f32 v126, s26, v126
v_mul_f32 v127, s26, v127
mv 56,248,88
cv 121,122,123
v_mul_f32 v132, s26, v132
mv 56,220,104
v_mul_f32 v133, s26, v133
v_mul_f32 v134, s26, v134
mv 56,216,112
s_waitcnt vmcnt(12) lgkmcnt(0)
s_barrier
mv 64,216,116
tr 252,22,24576
tr 254,22,30720
mv 64,220,108
tr 248,23,24576
tr 250,23,30720
mvc 60,224,72,32
tr 216,22,25600
tr 218,22,31744
mv 60,228,80
tr 220,23,25600
tr 222,23,31744
mvc 68,228,84,36
tr 228,22,26624
tr 230,22,32768
mv 68,224,76
tr 224,23,26624
tr 226,23,32768
mv 68,236,92
v_max3_f32 v20, v20, v55, v56
v_max3_f32 v20, v20, v57, v58
mv 68,232,100
v_max3_f32 v20, v20, v59, v64
v_max3_f32 v20, v20, v65, v66
mv 60,232,96
tr 232,22,36864
tr 234,22,43008
mv 60,236,88
s_add_u32 m0, s16, 0
s_add_u32 s25, s20, 0
dl 15,72,25,0
mv 60,244,104
tr 236,23,36864
tr 238,23,43008
mv 60,240,112
v_mul_f32 v135, s26, v135
cv 129,130,131
mv 68,240,116
tr 240,22,37888
tr 242,22,44032
mv 68,244,108
dl 15,76,25,1024
s_waitcnt lgkmcnt(6)
v_mfma_f32_16x16x32_bf16 a[96:99], v[252:255], a[192:195], a[96:99]
tr 244,23,37888
tr 246,23,44032
v_mfma_f32_16x16x32_bf16 a[104:107], v[252:255], a[196:199], a[104:107]
v_max3_f32 v20, v20, v67, v60
v_max3_f32 v20, v20, v61, v62
v_mfma_f32_16x16x32_bf16 a[112:115], v[252:255], a[200:203], a[112:115]
v_max3_f32 v20, v20, v63, v68
v_max3_f32 v20, v20, v69, v70
v_mfma_f32_16x16x32_bf16 a[120:123], v[252:255], a[204:207], a[120:123]
dl 15,80,25,2048
v_mfma_f32_16x16x32_bf16 a[124:127], v[248:251], a[204:207], a[124:127]
tr 252,22,38912
tr 254,22,45056
v_mfma_f32_16x16x32_bf16 a[116:119], v[248:251], a[200:203], a[116:119]
v_max_f32 v20, v20, v71
v_cmp_lt_f32 vcc, s27, v20
s_cbranch_vccz Lc6
s_swappc_b64 s[30:31], s[28:29]
Lc6:
v_mfma_f32_16x16x32_bf16 a[108:111], v[248:251], a[196:199], a[108:111]
v_exp_f32 v40, v40
v_mfma_f32_16x16x32_bf16 a[100:103], v[248:251], a[192:195], a[100:103]
tr 248,23,38912
tr 250,23,45056
v_mfma_f32_16x16x32_bf16 a[128:131], v[216:219], a[192:195], a[128:131]
v_exp_f32 v41, v41
v_mfma_f32_16x16x32_bf16 a[136:139], v[216:219], a[196:199], a[136:139]
v_exp_f32 v42, v42
v_mfma_f32_16x16x32_bf16 a[144:147], v[216:219], a[200:203], a[144:147]
v_exp_f32 v43, v43
v_mfma_f32_16x16x32_bf16 a[152:155], v[216:219], a[204:207], a[152:155]
v_exp_f32 v44, v44
v_mfma_f32_16x16x32_bf16 a[156:159], v[220:223], a[204:207], a[156:159]
s_waitcnt vmcnt(9) lgkmcnt(0)
s_barrier
v_mfma_f32_16x16x32_bf16 a[148:151], v[220:223], a[200:203], a[148:151]
tr 216,1,40960
tr 218,1,47104
v_mfma_f32_16x16x32_bf16 a[140:143], v[220:223], a[196:199], a[140:143]
v_exp_f32 v45, v45
v_mfma_f32_16x16x32_bf16 a[132:135], v[220:223], a[192:195], a[132:135]
tr 220,2,40960
tr 222,2,47104
v_mfma_f32_16x16x32_bf16 a[160:163], v[228:231], a[192:195], a[160:163]
v_exp_f32 v46, v46
v_mfma_f32_16x16x32_bf16 a[168:171], v[228:231], a[196:199], a[168:171]
v_exp_f32 v47, v47
v_mfma_f32_16x16x32_bf16 a[176:179], v[228:231], a[200:203], a[176:179]
cv 40,40,41
cv 41,42,43
v_mfma_f32_16x16x32_bf16 a[184:187], v[228:231], a[204:207], a[184:187]
s_add_u32 m0, s16, 3072
cv 42,44,45
dl 16,72,25,0
v_mfma_f32_16x16x32_bf16 a[188:191], v[224:227], a[204:207], a[188:191]
cv 43,46,47
v_exp_f32 v48, v48
v_mfma_f32_16x16x32_bf16 a[180:183], v[224:227], a[200:203], a[180:183]
v_exp_f32 v49, v49
v_mfma_f32_16x16x32_bf16 a[172:175], v[224:227], a[196:199], a[172:175]
v_exp_f32 v50, v50
v_mfma_f32_16x16x32_bf16 a[164:167], v[224:227], a[192:195], a[164:167]
dl 16,76,25,1024
v_mfma_f32_16x16x32_bf16 a[96:99], v[232:235], a[208:211], a[96:99]
v_exp_f32 v51, v51
v_mfma_f32_16x16x32_bf16 a[104:107], v[232:235], a[212:215], a[104:107]
v_exp_f32 v52, v52
v_mfma_f32_16x16x32_bf16 a[112:115], v[232:235], a[216:219], a[112:115]
v_exp_f32 v53, v53
v_mfma_f32_16x16x32_bf16 a[120:123], v[232:235], a[220:223], a[120:123]
dl 16,80,25,2048
v_mfma_f32_16x16x32_bf16 a[124:127], v[236:239], a[220:223], a[124:127]
v_exp_f32 v54, v54
v_mfma_f32_16x16x32_bf16 a[116:119], v[236:239], a[216:219], a[116:119]
v_exp_f32 v55, v55
v_mfma_f32_16x16x32_bf16 a[108:111], v[236:239], a[212:215], a[108:111]
cv 48,48,49
tr 228,3,40960
v_mfma_f32_16x16x32_bf16 a[100:103], v[236:239], a[208:211], a[100:103]
tr 230,3,47104
cv 49,50,51
v_mfma_f32_16x16x32_bf16 a[128:131], v[240:243], a[208:211], a[128:131]
cv 50,52,53
cv 51,54,55
v_mfma_f32_16x16x32_bf16 a[136:139], v[240:243], a[212:215], a[136:139]
tr 224,4,40960
tr 226,4,47104
v_mfma_f32_16x16x32_bf16 a[144:147], v[240:243], a[216:219], a[144:147]
tr 232,1,41984
tr 234,1,48128
v_mfma_f32_16x16x32_bf16 a[152:155], v[240:243], a[220:223], a[152:155]
tr 236,2,41984
tr 238,2,48128
v_mfma_f32_16x16x32_bf16 a[156:159], v[244:247], a[220:223], a[156:159]
tr 240,3,41984
tr 242,3,48128
v_mfma_f32_16x16x32_bf16 a[148:151], v[244:247], a[216:219], a[148:151]
v_exp_f32 v56, v56
v_mfma_f32_16x16x32_bf16 a[140:143], v[244:247], a[212:215], a[140:143]
v_exp_f32 v57, v57
v_mfma_f32_16x16x32_bf16 a[132:135], v[244:247], a[208:211], a[132:135]
tr 244,4,41984
tr 246,4,48128
v_mfma_f32_16x16x32_bf16 a[160:163], v[252:255], a[208:211], a[160:163]
v_exp_f32 v58, v58
v_mfma_f32_16x16x32_bf16 a[168:171], v[252:255], a[212:215], a[168:171]
v_exp_f32 v59, v59
v_mfma_f32_16x16x32_bf16 a[176:179], v[252:255], a[216:219], a[176:179]
v_exp_f32 v60, v60
v_mfma_f32_16x16x32_bf16 a[184:187], v[252:255], a[220:223], a[184:187]
s_add_u32 m0, s16, 24576
s_add_u32 s22, s18, 196608
dn 11,48,22,0
v_mfma_f32_16x16x32_bf16 a[188:191], v[248:251], a[220:223], a[188:191]
tr 252,1,43008
tr 254,1,49152
v_mfma_f32_16x16x32_bf16 a[180:183], v[248:251], a[216:219], a[180:183]
v_exp_f32 v61, v61
v_mfma_f32_16x16x32_bf16 a[172:175], v[248:251], a[212:215], a[172:175]
v_exp_f32 v62, v62
v_mfma_f32_16x16x32_bf16 a[164:167], v[248:251], a[208:211], a[164:167]
dn 11,52,22,1024
v_mfma_f32_16x16x32_bf16 a[244:247], a[240:243], v[40:43], a[244:247]
tr 248,2,43008
tr 250,2,49152
v_mfma_f32_16x16x32_bf16 a[248:251], a[240:243], v[48:51], a[248:251]
v_exp_f32 v63, v63
s_waitcnt lgkmcnt(4)
ma 0,216,40
cv 56,56,57
cv 57,58,59
ma 8,216,48
dn 11,56,22,2048
ma 4,220,40
tr 216,3,43008
tr 218,3,49152
ma 12,220,48
tr 220,4,43008
tr 222,4,49152
ma 16,228,40
cv 58,60,61
cv 59,62,63
ma 24,228,48
tr 228,1,53248
tr 230,1,59392
ma 20,224,40
v_exp_f32 v64, v64
ma 28,224,48
tr 224,2,53248
tr 226,2,59392
ma 32,232,40
v_exp_f32 v65, v65
ma 40,232,48
v_exp_f32 v66, v66
ma 36,236,40
v_exp_f32 v67, v67
ma 44,236,48
v_exp_f32 v68, v68
ma 48,240,40
v_exp_f32 v69, v69
ma 56,240,48
v_exp_f32 v70, v70
ma 52,244,40
v_exp_f32 v71, v71
ma 60,244,48
cv 64,64,65
tr 232,3,53248
s_waitcnt lgkmcnt(1)
ma 64,252,40
tr 234,3,59392
cv 65,66,67
ma 72,252,48
cv 66,68,69
cv 67,70,71
ma 68,248,40
tr 236,4,53248
tr 238,4,59392
ma 76,248,48
s_add_u32 m0, s16, 27648
tr 240,1,54272
dn 12,48,22,0
ma 80,216,40
tr 242,1,60416
tr 244,2,54272
ma 88,216,48
tr 246,2,60416
tr 252,3,54272
ma 84,220,40
tr 254,3,60416
tr 248,4,54272
ma 92,220,48
dn 12,52,22,1024
v_mfma_f32_16x16x32_bf16 a[244:247], a[240:243], v[56:59], a[244:247]
tr 250,4,60416
tr 216,1,55296
v_mfma_f32_16x16x32_bf16 a[248:251], a[240:243], v[64:67], a[248:251]
tr 218,1,61440
tr 220,2,55296
ma 0,228,56
tr 222,2,61440
v_mul_f32 v140, s26, v140
ma 8,228,64
dn 12,56,22,2048
ma 4,224,56
v_mul_f32 v141, s26, v141
v_mul_f32 v142, s26, v142
ma 12,224,64
v_mul_f32 v143, s26, v143
cv 137,138,139
s_waitcnt lgkmcnt(5)
ma 16,232,56
v_mul_f32 v148, s26, v148
v_mul_f32 v149, s26, v149
ma 24,232,64
v_mul_f32 v150, s26, v150
v_mul_f32 v151, s26, v151
ma 20,236,56
cv 145,146,147
v_mul_f32 v156, s26, v156
ma 28,236,64
v_mul_f32 v157, s26, v157
v_mul_f32 v158, s26, v158
ma 32,240,56
v_mul_f32 v159, s26, v159
cv 153,154,155
ma 40,240,64
v_mul_f32 v164, s26, v164
v_mul_f32 v165, s26, v165
ma 36,244,56
v_mul_f32 v166, s26, v166
v_mul_f32 v167, s26, v167
ma 44,244,64
cv 161,162,163
v_mul_f32 v172, s26, v172
ma 48,252,56
v_mul_f32 v173, s26, v173
v_mul_f32 v174, s26, v174
v_mul_f32 v175, s26, v175
cv 169,170,171
v_mul_f32 v180, s26, v180
v_mul_f32 v181, s26, v181
v_mul_f32 v182, s26, v182
v_mul_f32 v183, s26, v183
cv 177,178,179
v_mul_f32 v188, s26, v188
v_mul_f32 v189, s26, v189
v_mul_f32 v190, s26, v190
v_mul_f32 v191, s26, v191
cv 185,186,187
v_mul_f32 v196, s26, v196
v_mul_f32 v197, s26, v197
v_mul_f32 v198, s26, v198
v_mul_f32 v199, s26, v199
cv 193,194,195
v_mul_f32 v204, s26, v204
v_mul_f32 v205, s26, v205
v_mul_f32 v206, s26, v206
v_mul_f32 v207, s26, v207
cv 201,202,203
v_mul_f32 v212, s26, v212
v_mul_f32 v213, s26, v213
v_mul_f32 v214, s26, v214
v_mul_f32 v215, s26, v215
cv 209,210,211
cv 122,124,125
cv 123,126,127
cv 130,132,133
cv 131,134,135
cv 138,140,141
cv 139,142,143
cv 146,148,149
cv 147,150,151
cv 154,156,157
cv 155,158,159
cv 162,164,165
cv 163,166,167
cv 170,172,173
cv 171,174,175
cv 178,180,181
cv 179,182,183
cv 186,188,189
cv 187,190,191
cv 194,196,197
cv 195,198,199
cv 202,204,205
cv 203,206,207
cv 210,212,213
cv 211,214,215
v_permlane32_swap_b32 v120, v122
v_permlane32_swap_b32 v121, v123
v_permlane32_swap_b32 v128, v130
v_permlane32_swap_b32 v129, v131
v_permlane32_swap_b32 v136, v138
v_permlane32_swap_b32 v137, v139
v_permlane32_swap_b32 v144, v146
v_permlane32_swap_b32 v145, v147
v_permlane32_swap_b32 v152, v154
v_permlane32_swap_b32 v153, v155
v_permlane32_swap_b32 v160, v162
v_permlane32_swap_b32 v161, v163
v_permlane32_swap_b32 v168, v170
v_permlane32_swap_b32 v169, v171
v_permlane32_swap_b32 v176, v178
v_permlane32_swap_b32 v177, v179
v_permlane32_swap_b32 v184, v186
v_permlane32_swap_b32 v185, v187
v_permlane32_swap_b32 v192, v194
v_permlane32_swap_b32 v193, v195
v_permlane32_swap_b32 v200, v202
v_permlane32_swap_b32 v201, v203
v_permlane32_swap_b32 v208, v210
v_permlane32_swap_b32 v209, v211
ma 56,252,64
v_add_u32 v124, s89, v19
v_permlane16_swap_b32 v120, v122
v_permlane16_swap_b32 v121, v123
v_add_u32 v132, s89, v19
v_permlane16_swap_b32 v128, v130
v_permlane16_swap_b32 v129, v131
v_add_u32 v140, s89, v19
v_permlane16_swap_b32 v136, v138
v_permlane16_swap_b32 v137, v139
v_add_u32 v148, s89, v19
v_permlane16_swap_b32 v144, v146
v_permlane16_swap_b32 v145, v147
v_add_u32 v156, s89, v19
v_permlane16_swap_b32 v152, v154
v_permlane16_swap_b32 v153, v155
v_add_u32 v164, s89, v19
v_permlane16_swap_b32 v160, v162
v_permlane16_swap_b32 v161, v163
v_add_u32 v172, s89, v19
v_permlane16_swap_b32 v168, v170
v_permlane16_swap_b32 v169, v171
v_add_u32 v180, s89, v19
v_permlane16_swap_b32 v176, v178
v_permlane16_swap_b32 v177, v179
v_add_u32 v188, s89, v19
v_permlane16_swap_b32 v184, v186
v_permlane16_swap_b32 v185, v187
v_add_u32 v196, s89, v19
v_permlane16_swap_b32 v192, v194
v_permlane16_swap_b32 v193, v195
v_add_u32 v204, s89, v19
v_permlane16_swap_b32 v200, v202
v_permlane16_swap_b32 v201, v203
v_add_u32 v212, s89, v19
v_permlane16_swap_b32 v208, v210
v_permlane16_swap_b32 v209, v211
ds_write_b128 v124, v[120:123] offset:0
ds_write_b128 v132, v[128:131] offset:4096
ds_write_b128 v140, v[136:139] offset:1024
ds_write_b128 v148, v[144:147] offset:5120
ds_write_b128 v156, v[152:155] offset:16384
ds_write_b128 v164, v[160:163] offset:20480
ds_write_b128 v172, v[168:171] offset:17408
ds_write_b128 v180, v[176:179] offset:21504
ds_write_b128 v188, v[184:187] offset:32768
ds_write_b128 v196, v[192:195] offset:36864
ds_write_b128 v204, v[200:203] offset:33792
ds_write_b128 v212, v[208:211] offset:37888
tr 228,3,55296
tr 230,3,61440
tr 224,4,55296
tr 226,4,61440
s_waitcnt lgkmcnt(15)
ma 52,248,56
v_add_u32 v5, 0x8000, v5
v_add_u32 v6, 0x8000, v6
ma 60,248,64
v_add_u32 v1, 0xffffe000, v1
v_add_u32 v2, 0xffffe000, v2
ma 64,216,56
v_add_u32 v3, 0xffffe000, v3
ar 208,244
ma 72,216,64
v_rcp_f32 v208, v208
s_waitcnt vmcnt(12) lgkmcnt(0)
s_barrier
rd 72,19,0
rd 232,5,40960
ma 68,220,56
rd 80,19,8192
rd 236,6,40960
ma 76,220,64
rd 84,19,12288
rd 76,19,4096
ma 80,228,56
rd 92,19,20480
rd 240,5,41984
ma 88,228,64
rd 100,19,28672
rd 244,6,41984
ma 84,224,56
rd 96,19,24576
rd 88,19,16384
ma 92,224,64
rd 104,19,32768
rd 252,5,43008
rd 112,19,40960
rd 248,6,43008
rd 116,19,45056
rd 108,19,36864
rd 216,5,47104
s_waitcnt lgkmcnt(15)
mvz 40,232,72
rd 220,6,47104
rd 228,5,48128
mv 40,236,80
rd 224,6,48128
ar 200,0
s_waitcnt lgkmcnt(14)
mvz 48,236,84
rd 236,5,49152
ar 201,1
mv 48,232,76
rd 232,6,49152
ar 202,2
mv 48,240,92
ar 203,3
ar 204,4
s_waitcnt lgkmcnt(7)
mv 48,244,100
ar 205,5
ar 206,6
mv 40,244,96
rd 244,5,53248
ar 207,7
mv 40,240,88
rd 240,6,53248
v_mul_f32 v200, v208, v200
mv 40,252,104
v_mul_f32 v201, v208, v201
v_mul_f32 v202, v208, v202
mv 40,248,112
v_mul_f32 v203, v208, v203
v_mul_f32 v204, v208, v204
mv 48,248,116
rd 248,5,54272
v_mul_f32 v205, v208, v205
s_waitcnt lgkmcnt(3)
mv 48,252,108
rd 252,6,54272
v_mul_f32 v206, v208, v206
mvz 44,216,72
v_mul_f32 v207, v208, v207
ar 209,248
mv 44,220,80
cv 120,200,201
cv 121,202,203
mvz 52,220,84
rd 220,5,55296
cv 122,204,205
mv 52,216,76
rd 216,6,55296
cv 123,206,207
mv 52,228,92
v_rcp_f32 v209, v209
mv 52,224,100
v_add_u32 v4, 0xffffe000, v4
ar 24,8
mv 44,224,96
rd 224,5,59392
ar 25,9
mv 44,228,88
rd 228,6,59392
ar 26,10
mv 44,236,104
ar 27,11
ar 28,12
mv 44,232,112
ar 29,13
ar 30,14
mv 52,232,116
rd 232,5,60416
ar 31,15
mv 52,236,108
rd 236,6,60416
v_mul_f32 v24, v209, v24
s_waitcnt lgkmcnt(4)
mvz 56,244,72
v_mul_f32 v25, v209, v25
v_mul_f32 v26, v209, v26
mv 56,240,80
v_mul_f32 v27, v209, v27
v_mul_f32 v28, v209, v28
mvz 64,240,84
rd 240,5,61440
v_mul_f32 v29, v209, v29
mv 64,244,76
rd 244,6,61440
v_mul_f32 v30, v209, v30
mv 64,248,92
v_mul_f32 v31, v209, v31
cv 124,24,25
mv 64,252,100
cv 125,26,27
cv 126,28,29
mv 56,252,96
cv 127,30,31
ar 200,16
mv 56,248,88
ar 201,17
ar 202,18
mv 56,220,104
s_waitcnt vmcnt(6) lgkmcnt(0)
s_barrier
mv 56,216,112
tr 252,1,0
tr 254,1,6144
mv 64,216,116
tr 248,2,0
tr 250,2,6144
mv 64,220,108
tr 216,3,0
)"
R"(tr 218,3,6144
mvz 60,224,72
tr 220,4,0
tr 222,4,6144
mv 60,228,80
ar 203,19
ar 204,20
mvz 68,228,84
tr 228,1,12288
tr 230,1,18432
mv 68,224,76
tr 224,2,12288
tr 226,2,18432
mv 68,232,92
ar 205,21
ar 206,22
mv 68,236,100
ar 207,23
v_mul_f32 v200, v208, v200
mv 60,236,96
tr 236,3,12288
tr 238,3,18432
mv 60,232,88
s_add_u32 m0, s16, 49152
s_add_u32 s23, s18, 221184
dn 9,36,23,0
mv 60,240,104
v_mul_f32 v201, v208, v201
v_mul_f32 v202, v208, v202
mv 60,244,112
v_mul_f32 v203, v208, v203
v_mul_f32 v204, v208, v204
mv 68,244,116
v_mul_f32 v205, v208, v205
v_mul_f32 v206, v208, v206
mv 68,240,108
dn 9,40,23,1024
s_waitcnt lgkmcnt(2)
mvz 168,252,120
v_mul_f32 v207, v208, v207
cv 128,200,201
mvz 176,252,124
cv 129,202,203
cv 130,204,205
mvz 172,248,120
cv 131,206,207
ar 24,24
mvz 180,248,124
dn 9,44,23,2048
mvz 184,216,120
ar 25,25
ar 26,26
mvz 192,216,124
ar 27,27
ar 28,28
mvz 188,220,120
ar 29,29
ar 30,30
mvz 196,220,124
ar 31,31
tr 232,4,12288
tr 234,4,18432
mv 168,228,128
v_mul_f32 v24, v209, v24
v_mul_f32 v25, v209, v25
v_mul_f32 v26, v209, v26
v_mul_f32 v27, v209, v27
v_mul_f32 v28, v209, v28
v_mul_f32 v29, v209, v29
v_mul_f32 v30, v209, v30
v_mul_f32 v31, v209, v31
cv 132,24,25
cv 133,26,27
cv 134,28,29
cv 135,30,31
tr 244,1,1024
tr 246,1,7168
mv 176,228,132
tr 240,2,1024
tr 242,2,7168
mv 172,224,128
tr 252,3,1024
tr 254,3,7168
mv 180,224,132
ar 200,32
ar 201,33
s_waitcnt lgkmcnt(6)
mv 184,236,128
ar 202,34
ar 203,35
mv 192,236,132
ar 204,36
ar 205,37
mv 188,232,128
ar 206,38
ar 207,39
mv 196,232,132
v_mul_f32 v200, v208, v200
v_mul_f32 v201, v208, v201
v_mul_f32 v202, v208, v202
v_mul_f32 v203, v208, v203
v_mul_f32 v204, v208, v204
v_mul_f32 v205, v208, v205
v_mul_f32 v206, v208, v206
v_mul_f32 v207, v208, v207
cv 136,200,201
cv 137,202,203
cv 138,204,205
cv 139,206,207
ar 24,40
ar 25,41
ar 26,42
ar 27,43
ar 28,44
ar 29,45
ar 30,46
ar 31,47
tr 248,4,1024
tr 250,4,7168
s_waitcnt lgkmcnt(2)
mv 168,244,136
v_mul_f32 v24, v209, v24
v_mul_f32 v25, v209, v25
v_mul_f32 v26, v209, v26
v_mul_f32 v27, v209, v27
v_mul_f32 v28, v209, v28
v_mul_f32 v29, v209, v29
v_mul_f32 v30, v209, v30
v_mul_f32 v31, v209, v31
cv 140,24,25
cv 141,26,27
cv 142,28,29
cv 143,30,31
tr 216,1,13312
tr 218,1,19456
mv 176,244,140
tr 220,2,13312
tr 222,2,19456
mv 172,240,136
tr 228,3,13312
tr 230,3,19456
mv 180,240,140
s_add_u32 m0, s16, 52224
ar 200,48
dn 10,36,23,0
mv 184,252,136
ar 201,49
ar 202,50
mv 192,252,140
ar 203,51
ar 204,52
s_waitcnt lgkmcnt(6)
mv 188,248,136
ar 205,53
ar 206,54
ar 207,55
dn 10,40,23,1024
mv 196,248,140
v_mul_f32 v200, v208, v200
v_mul_f32 v201, v208, v201
v_mul_f32 v202, v208, v202
v_mul_f32 v203, v208, v203
v_mul_f32 v204, v208, v204
v_mul_f32 v205, v208, v205
v_mul_f32 v206, v208, v206
v_mul_f32 v207, v208, v207
cv 144,200,201
cv 145,202,203
cv 146,204,205
cv 147,206,207
dn 10,44,23,2048
ar 24,56
ar 25,57
ar 26,58
ar 27,59
ar 28,60
ar 29,61
ar 30,62
ar 31,63
tr 224,4,13312
tr 226,4,19456
s_waitcnt lgkmcnt(2)
mv 168,216,144
v_mul_f32 v24, v209, v24
v_mul_f32 v25, v209, v25
v_mul_f32 v26, v209, v26
v_mul_f32 v27, v209, v27
v_mul_f32 v28, v209, v28
v_mul_f32 v29, v209, v29
v_mul_f32 v30, v209, v30
v_mul_f32 v31, v209, v31
cv 148,24,25
cv 149,26,27
cv 150,28,29
cv 151,30,31
tr 236,1,2048
tr 238,1,8192
mv 176,216,148
tr 232,2,2048
tr 234,2,8192
mv 172,220,144
tr 244,3,2048
tr 246,3,8192
mv 180,220,148
ar 200,64
ar 201,65
mv 184,228,144
ar 202,66
ar 203,67
mv 192,228,148
ar 204,68
ar 205,69
s_waitcnt lgkmcnt(6)
mv 188,224,144
ar 206,70
ar 207,71
mv 196,224,148
v_mul_f32 v200, v208, v200
v_mul_f32 v201, v208, v201
v_mul_f32 v202, v208, v202
v_mul_f32 v203, v208, v203
v_mul_f32 v204, v208, v204
v_mul_f32 v205, v208, v205
v_mul_f32 v206, v208, v206
v_mul_f32 v207, v208, v207
cv 152,200,201
cv 153,202,203
cv 154,204,205
cv 155,206,207
ar 24,72
ar 25,73
ar 26,74
ar 27,75
ar 28,76
ar 29,77
ar 30,78
ar 31,79
tr 240,4,2048
tr 242,4,8192
s_waitcnt lgkmcnt(2)
mv 168,236,152
v_mul_f32 v24, v209, v24
v_mul_f32 v25, v209, v25
v_mul_f32 v26, v209, v26
v_mul_f32 v27, v209, v27
v_mul_f32 v28, v209, v28
v_mul_f32 v29, v209, v29
v_mul_f32 v30, v209, v30
v_mul_f32 v31, v209, v31
cv 156,24,25
cv 157,26,27
cv 158,28,29
cv 159,30,31
tr 252,1,14336
tr 254,1,20480
mv 176,236,156
tr 248,2,14336
tr 250,2,20480
tr 216,3,14336
tr 218,3,20480
tr 220,4,14336
tr 222,4,20480
mv 172,232,152
ar 200,80
ar 201,81
mv 180,232,156
ar 202,82
ar 203,83
mv 184,244,152
ar 204,84
ar 205,85
mv 192,244,156
ar 206,86
ar 207,87
s_waitcnt lgkmcnt(6)
mv 188,240,152
s_waitcnt vmcnt(6) lgkmcnt(0)
s_barrier
mv 196,240,156
v_mul_f32 v200, v208, v200
v_mul_f32 v201, v208, v201
v_mul_f32 v202, v208, v202
v_mul_f32 v203, v208, v203
v_mul_f32 v204, v208, v204
v_mul_f32 v205, v208, v205
v_mul_f32 v206, v208, v206
v_mul_f32 v207, v208, v207
cv 160,200,201
cv 161,202,203
cv 162,204,205
cv 163,206,207
ar 24,88
ar 25,89
ar 26,90
ar 27,91
ar 28,92
ar 29,93
ar 30,94
ar 31,95
mv 168,252,160
v_mul_f32 v24, v209, v24
v_mul_f32 v25, v209, v25
v_mul_f32 v26, v209, v26
v_mul_f32 v27, v209, v27
v_mul_f32 v28, v209, v28
v_mul_f32 v29, v209, v29
v_mul_f32 v30, v209, v30
v_mul_f32 v31, v209, v31
cv 164,24,25
cv 165,26,27
cv 166,28,29
cv 167,30,31
v_max3_f32 v20, v40, v41, v42
v_max3_f32 v20, v20, v43, v44
mv 176,252,164
v_max3_f32 v20, v20, v45, v46
v_max3_f32 v20, v20, v47, v56
mv 172,248,160
v_max3_f32 v20, v20, v57, v58
v_max3_f32 v21, v48, v49, v50
mv 180,248,164
s_add_u32 m0, s16, 0
s_add_u32 s24, s20, 128
dl 15,72,24,0
v_max3_f32 v20, v20, v59, v60
v_max3_f32 v21, v21, v51, v52
v_max3_f32 v20, v20, v61, v62
v_max3_f32 v21, v21, v53, v54
v_max_f32 v20, v20, v63
tr 228,1,24576
tr 230,1,30720
mv 184,216,160
v_max3_f32 v21, v21, v55, v64
v_mov_b32 v32, v20
v_max3_f32 v21, v21, v65, v66
v_max3_f32 v21, v21, v67, v68
v_max3_f32 v21, v21, v69, v70
dl 15,76,24,1024
v_permlane32_swap_b32 v20, v32
v_max_f32 v21, v21, v71
v_max_f32 v20, v20, v32
mv 192,216,164
v_mov_b32 v32, v20
v_mov_b32 v36, v21
tr 224,2,24576
v_permlane16_swap_b32 v20, v32
v_permlane32_swap_b32 v21, v36
v_max_f32 v20, v20, v32
v_max_f32 v21, v21, v36
tr 226,2,30720
mv 188,220,160
dl 15,80,24,2048
v_mov_b32 v36, v21
v_sub_f32 v40, v40, v20
v_sub_f32 v41, v41, v20
v_sub_f32 v42, v42, v20
v_sub_f32 v43, v43, v20
v_exp_f32 v40, v40
v_exp_f32 v41, v41
v_sub_f32 v44, v44, v20
v_sub_f32 v45, v45, v20
v_sub_f32 v46, v46, v20
v_sub_f32 v47, v47, v20
v_exp_f32 v42, v42
v_exp_f32 v43, v43
v_permlane16_swap_b32 v21, v36
v_exp_f32 v44, v44
v_exp_f32 v45, v45
v_exp_f32 v46, v46
v_exp_f32 v47, v47
v_max_f32 v21, v21, v36
cv 40,40,41
mv 196,220,164
v_sub_f32 v48, v48, v21
v_sub_f32 v49, v49, v21
cv 41,42,43
v_sub_f32 v50, v50, v21
v_sub_f32 v51, v51, v21
cv 42,44,45
cv 43,46,47
v_exp_f32 v48, v48
v_exp_f32 v49, v49
v_sub_f32 v52, v52, v21
v_sub_f32 v53, v53, v21
v_sub_f32 v54, v54, v21
v_sub_f32 v55, v55, v21
v_exp_f32 v50, v50
v_exp_f32 v51, v51
v_exp_f32 v52, v52
v_exp_f32 v53, v53
v_exp_f32 v54, v54
v_exp_f32 v55, v55
cv 48,48,49
tr 236,3,24576
tr 238,3,30720
v_mfma_f32_16x16x32_bf16 a[244:247], a[240:243], v[40:43], 0
cv 49,50,51
cv 50,52,53
cv 51,54,55
tr 232,4,24576
tr 234,4,30720
v_mfma_f32_16x16x32_bf16 a[248:251], a[240:243], v[48:51], 0
tr 244,1,25600
tr 246,1,31744
s_waitcnt lgkmcnt(6)
maz 0,228,40
tr 240,2,25600
tr 242,2,31744
maz 8,228,48
tr 252,3,25600
tr 254,3,31744
maz 4,224,40
tr 248,4,25600
tr 250,4,31744
maz 12,224,48
tr 216,1,26624
tr 218,1,32768
tr 220,2,26624
s_waitcnt lgkmcnt(13)
maz 16,236,40
tr 222,2,32768
tr 228,3,26624
maz 24,236,48
tr 230,3,32768
tr 224,4,26624
s_waitcnt lgkmcnt(13)
maz 20,232,40
tr 226,4,32768
tr 236,1,36864
maz 28,232,48
tr 238,1,43008
tr 232,2,36864
maz 32,244,40
tr 234,2,43008
v_sub_f32 v56, v56, v20
maz 40,244,48
v_sub_f32 v57, v57, v20
v_sub_f32 v58, v58, v20
s_waitcnt lgkmcnt(9)
maz 36,240,40
v_sub_f32 v59, v59, v20
v_exp_f32 v56, v56
maz 44,240,48
v_exp_f32 v57, v57
maz 48,252,40
v_sub_f32 v60, v60, v20
v_sub_f32 v61, v61, v20
maz 56,252,48
v_sub_f32 v62, v62, v20
v_sub_f32 v63, v63, v20
maz 52,248,40
v_exp_f32 v58, v58
maz 60,248,48
v_exp_f32 v59, v59
maz 64,216,40
v_exp_f32 v60, v60
maz 72,216,48
v_exp_f32 v61, v61
s_waitcnt lgkmcnt(0)
maz 68,220,40
v_exp_f32 v62, v62
maz 76,220,48
s_add_u32 m0, s16, 3072
v_exp_f32 v63, v63
dl 16,72,24,0
maz 80,228,40
cv 56,56,57
v_sub_f32 v64, v64, v21
maz 88,228,48
v_sub_f32 v65, v65, v21
cv 57,58,59
maz 84,224,40
v_sub_f32 v66, v66, v21
v_sub_f32 v67, v67, v21
maz 92,224,48
dl 16,76,24,1024
cv 58,60,61
cv 59,62,63
v_exp_f32 v64, v64
v_exp_f32 v65, v65
v_sub_f32 v68, v68, v21
v_sub_f32 v69, v69, v21
v_sub_f32 v70, v70, v21
v_sub_f32 v71, v71, v21
v_exp_f32 v66, v66
v_exp_f32 v67, v67
dl 16,80,24,2048
v_exp_f32 v68, v68
v_exp_f32 v69, v69
v_exp_f32 v70, v70
v_exp_f32 v71, v71
cv 64,64,65
tr 244,3,36864
tr 246,3,43008
v_mfma_f32_16x16x32_bf16 a[244:247], a[240:243], v[56:59], a[244:247]
cv 65,66,67
cv 66,68,69
cv 67,70,71
tr 240,4,36864
tr 242,4,43008
v_mfma_f32_16x16x32_bf16 a[248:251], a[240:243], v[64:67], a[248:251]
tr 252,1,37888
tr 254,1,44032
ma 0,236,56
tr 248,2,37888
tr 250,2,44032
ma 8,236,64
tr 216,3,37888
tr 218,3,44032
ma 4,232,56
tr 220,4,37888
tr 222,4,44032
ma 12,232,64
tr 228,1,38912
tr 230,1,45056
tr 224,2,38912
tr 226,2,45056
s_waitcnt lgkmcnt(14)
ma 16,244,56
tr 236,3,38912
tr 238,3,45056
ma 24,244,64
tr 232,4,38912
tr 234,4,45056
s_waitcnt lgkmcnt(13)
ma 20,240,56
v_sub_f32 v32, 0, v20
v_sub_f32 v33, 0, v20
ma 28,240,64
v_sub_f32 v34, 0, v20
v_sub_f32 v35, 0, v20
ma 32,252,56
v_sub_f32 v36, 0, v21
v_sub_f32 v37, 0, v21
ma 40,252,64
v_sub_f32 v38, 0, v21
v_sub_f32 v39, 0, v21
s_waitcnt lgkmcnt(4)
ma 36,248,56
s_waitcnt vmcnt(6) lgkmcnt(0)
s_barrier
ma 44,248,64
rd 244,5,16384
rd 240,6,16384
ma 48,216,56
rd 252,5,17408
rd 248,6,17408
ma 56,216,64
rd 216,5,18432
cv 168,168,169
ma 52,220,56
cv 169,170,171
cv 170,172,173
ma 60,220,64
rd 220,6,18432
cv 171,174,175
ma 64,228,56
aw 192,168
aw 193,169
ma 72,228,64
rd 228,5,22528
aw 194,170
ma 68,224,56
aw 195,171
cv 176,176,177
ma 76,224,64
s_add_u32 m0, s16, 24576
s_add_u32 s25, s18, 221184
dn 11,48,25,0
ma 80,236,56
rd 224,6,22528
cv 177,178,179
ma 88,236,64
rd 236,5,23552
cv 178,180,181
ma 84,232,56
cv 179,182,183
aw 196,176
ma 92,232,64
dn 11,52,25,1024
s_waitcnt lgkmcnt(2)
mvc 40,244,72,32
rd 232,6,23552
aw 197,177
mv 40,240,80
aw 198,178
aw 199,179
mvc 48,240,84,36
rd 240,5,24576
cv 184,184,185
mv 48,244,76
dn 11,56,25,2048
mv 48,252,92
rd 244,6,24576
cv 185,186,187
mv 48,248,100
cv 186,188,189
cv 187,190,191
mv 40,248,96
rd 248,5,28672
aw 200,184
mv 40,252,88
rd 252,6,28672
aw 201,185
mv 40,216,104
aw 202,186
aw 203,187
mv 40,220,112
cv 192,192,193
cv 193,194,195
mv 48,220,116
rd 220,5,29696
cv 194,196,197
mv 48,216,108
rd 216,6,29696
cv 195,198,199
mvc 44,228,72,32
aw 204,192
aw 205,193
s_waitcnt lgkmcnt(3)
mv 44,224,80
aw 206,194
aw 207,195
mvc 52,224,84,36
rd 224,5,30720
v_max3_f32 v20, v40, v41, v42
mv 52,228,76
rd 228,6,30720
v_max3_f32 v20, v20, v43, v48
mv 52,236,92
v_max3_f32 v20, v20, v49, v50
ar 24,96
mv 52,232,100
ar 25,97
ar 26,98
mv 44,232,96
rd 232,5,34816
ar 27,99
mv 44,236,88
s_add_u32 m0, s16, 27648
rd 236,6,34816
dn 12,48,25,0
mv 44,240,104
ar 28,100
ar 29,101
mv 44,244,112
ar 30,102
ar 31,103
mv 52,244,116
rd 244,5,35840
v_mul_f32 v24, s26, v24
mv 52,240,108
dn 12,52,25,1024
mvc 56,248,72,32
rd 240,6,35840
v_max3_f32 v20, v20, v51, v44
s_waitcnt lgkmcnt(3)
mv 56,252,80
v_max3_f32 v20, v20, v45, v46
s_nop 0
v_max3_f32 v20, v20, v47, v52
mvc 64,252,84,36
rd 252,5,36864
v_max3_f32 v20, v20, v53, v54
mv 64,248,76
dn 12,56,25,2048
mv 64,220,92
rd 248,6,36864
v_mul_f32 v25, s26, v25
mv 64,216,100
v_mul_f32 v26, s26, v26
v_mul_f32 v27, s26, v27
mv 56,216,96
cv 24,24,25
v_mul_f32 v28, s26, v28
mv 56,220,88
v_mul_f32 v29, s26, v29
v_mul_f32 v30, s26, v30
mv 56,224,104
v_mul_f32 v31, s26, v31
cv 25,26,27
mv 56,228,112
s_waitcnt vmcnt(6) lgkmcnt(0)
s_barrier
mv 64,228,116
tr 216,1,0
tr 218,1,6144
mv 64,224,108
tr 220,1,12288
tr 222,1,18432
mvc 60,232,72,32
tr 228,1,1024
tr 230,1,7168
mv 60,236,80
tr 224,1,13312
tr 226,1,19456
mvc 68,236,84,36
tr 236,1,2048
tr 238,1,8192
mv 68,232,76
tr 232,1,14336
tr 234,1,20480
mv 68,244,92
v_max3_f32 v20, v20, v55, v56
v_max3_f32 v20, v20, v57, v58
mv 68,240,100
v_max3_f32 v20, v20, v59, v64
v_max3_f32 v20, v20, v65, v66
mv 60,240,96
tr 240,2,0
tr 242,2,6144
mv 60,244,88
s_add_u32 m0, s16, 49152
s_add_u32 s22, s18, 245760
dn 9,36,22,0
mv 60,252,104
tr 244,2,12288
tr 246,2,18432
mv 60,248,112
cv 26,28,29
cv 27,30,31
mv 68,248,116
tr 248,2,1024
tr 250,2,7168
mv 68,252,108
dn 9,40,22,1024
s_waitcnt lgkmcnt(6)
mvz 168,216,120
tr 252,2,13312
tr 254,2,19456
mv 168,220,128
v_max3_f32 v20, v20, v67, v60
v_max3_f32 v20, v20, v61, v62
mvz 176,220,132
tr 220,2,2048
tr 222,2,8192
mv 176,216,124
dn 9,44,22,2048
mv 176,228,140
tr 216,2,14336
tr 218,2,20480
mv 176,224,148
v_max3_f32 v20, v20, v63, v68
v_max3_f32 v20, v20, v69, v70
mv 168,224,144
tr 224,3,0
tr 226,3,6144
mv 168,228,136
tr 228,3,12288
tr 230,3,18432
mv 168,236,152
v_max_f32 v20, v20, v71
v_cmp_lt_f32 vcc, s27, v20
s_cbranch_vccz Lc7
s_swappc_b64 s[30:31], s[28:29]
Lc7:
mv 168,232,160
v_exp_f32 v40, v40
mv 176,232,164
tr 232,3,1024
tr 234,3,7168
mv 176,236,156
tr 236,3,13312
tr 238,3,19456
s_waitcnt lgkmcnt(8)
mvz 172,240,120
v_exp_f32 v41, v41
mv 172,244,128
v_exp_f32 v42, v42
mvz 180,244,132
tr 244,3,2048
tr 246,3,8192
mv 180,240,124
tr 240,3,14336
tr 242,3,20480
mv 180,248,140
v_exp_f32 v43, v43
mv 180,252,148
v_exp_f32 v44, v44
mv 172,252,144
tr 252,4,0
tr 254,4,6144
mv 172,248,136
s_add_u32 m0, s16, 52224
tr 248,4,12288
dn 10,36,22,0
mv 172,220,152
tr 250,4,18432
v_exp_f32 v45, v45
mv 172,216,160
v_exp_f32 v46, v46
mv 180,216,164
tr 216,4,1024
tr 218,4,7168
mv 180,220,156
dn 10,40,22,1024
s_waitcnt lgkmcnt(5)
mvz 184,224,120
tr 220,4,13312
tr 222,4,19456
mv 184,228,128
v_exp_f32 v47, v47
mvz 192,228,132
tr 228,4,2048
tr 230,4,8192
mv 192,224,124
dn 10,44,22,2048
mv 192,232,140
tr 224,4,14336
tr 226,4,20480
mv 192,236,148
cv 40,40,41
cv 41,42,43
mv 184,236,144
cv 42,44,45
cv 43,46,47
mv 184,232,136
v_exp_f32 v48, v48
mv 184,244,152
v_exp_f32 v49, v49
mv 184,240,160
s_waitcnt vmcnt(6) lgkmcnt(0)
s_barrier
mv 192,240,164
tr 236,1,24576
tr 238,1,30720
mv 192,244,156
tr 232,2,24576
tr 234,2,30720
mvz 188,252,120
v_exp_f32 v50, v50
mv 188,248,128
v_exp_f32 v51, v51
mvz 196,248,132
v_exp_f32 v52, v52
mv 196,252,124
v_exp_f32 v53, v53
mv 196,216,140
v_exp_f32 v54, v54
mv 196,220,148
v_exp_f32 v55, v55
mv 188,220,144
cv 48,48,49
tr 240,3,24576
mv 188,216,136
s_add_u32 m0, s16, 0
s_add_u32 s23, s20, 256
dl 15,72,23,0
mv 188,228,152
tr 242,3,30720
cv 49,50,51
mv 188,224,160
cv 50,52,53
cv 51,54,55
mv 196,224,164
tr 244,4,24576
tr 246,4,30720
mv 196,228,156
dl 15,76,23,1024
v_mfma_f32_16x16x32_bf16 a[244:247], a[240:243], v[40:43], a[244:247]
tr 248,1,25600
tr 250,1,31744
v_mfma_f32_16x16x32_bf16 a[248:251], a[240:243], v[48:51], a[248:251]
tr 252,2,25600
tr 254,2,31744
s_waitcnt lgkmcnt(7)
ma 0,236,40
tr 220,3,25600
tr 222,3,31744
ma 8,236,48
dl 15,80,23,2048
ma 4,232,40
tr 216,4,25600
tr 218,4,31744
ma 12,232,48
tr 224,1,26624
tr 226,1,32768
s_waitcnt lgkmcnt(10)
ma 16,240,40
tr 228,2,26624
tr 230,2,32768
ma 24,240,48
tr 236,3,26624
tr 238,3,32768
ma 20,244,40
tr 232,4,26624
tr 234,4,32768
ma 28,244,48
tr 240,1,36864
tr 242,1,43008
s_waitcnt lgkmcnt(12)
ma 32,248,40
tr 244,2,36864
tr 246,2,43008
ma 40,248,48
v_exp_f32 v56, v56
ma 36,252,40
v_exp_f32 v57, v57
ma 44,252,48
v_exp_f32 v58, v58
ma 48,220,40
v_exp_f32 v59, v59
ma 56,220,48
v_exp_f32 v60, v60
s_waitcnt lgkmcnt(2)
ma 52,216,40
v_exp_f32 v61, v61
ma 60,216,48
v_exp_f32 v62, v62
ma 64,224,40
v_exp_f32 v63, v63
ma 72,224,48
cv 56,56,57
cv 57,58,59
ma 68,228,40
cv 58,60,61
cv 59,62,63
ma 76,228,48
s_add_u32 m0, s16, 3072
v_exp_f32 v64, v64
dl 16,72,23,0
ma 80,236,40
v_exp_f32 v65, v65
ma 88,236,48
v_exp_f32 v66, v66
ma 84,232,40
v_exp_f32 v67, v67
ma 92,232,48
dl 16,76,23,1024
v_exp_f32 v68, v68
v_exp_f32 v69, v69
v_exp_f32 v70, v70
v_exp_f32 v71, v71
cv 64,64,65
tr 248,3,36864
tr 250,3,43008
v_mfma_f32_16x16x32_bf16 a[244:247], a[240:243], v[56:59], a[244:247]
cv 65,66,67
dl 16,80,23,2048
cv 66,68,69
cv 67,70,71
tr 252,4,36864
tr 254,4,43008
v_mfma_f32_16x16x32_bf16 a[248:251], a[240:243], v[64:67], a[248:251]
tr 220,1,37888
tr 222,1,44032
ma 0,240,56
tr 216,2,37888
tr 218,2,44032
ma 8,240,64
tr 224,3,37888
tr 226,3,44032
s_waitcnt lgkmcnt(10)
ma 4,244,56
tr 228,4,37888
tr 230,4,44032
ma 12,244,64
tr 236,1,38912
tr 238,1,45056
s_waitcnt lgkmcnt(12)
ma 16,248,56
tr 232,2,38912
tr 234,2,45056
ma 24,248,64
tr 240,3,38912
tr 242,3,45056
s_waitcnt lgkmcnt(12)
ma 20,252,56
tr 244,4,38912
tr 246,4,45056
ma 28,252,64
cv 168,168,169
cv 169,170,171
ma 32,220,56
cv 170,172,173
cv 171,174,175
ma 40,220,64
aw 208,168
aw 209,169
s_waitcnt lgkmcnt(6)
ma 36,216,56
aw 210,170
aw 211,171
ma 44,216,64
s_waitcnt vmcnt(6) lgkmcnt(0)
s_barrier
ma 48,224,56
rd 248,5,16384
rd 252,6,16384
ma 56,224,64
rd 220,5,17408
rd 216,6,17408
ma 52,228,56
rd 224,5,18432
cv 176,176,177
ma 60,228,64
rd 228,6,18432
cv 177,178,179
ma 64,236,56
cv 178,180,181
cv 179,182,183
ma 72,236,64
rd 236,5,22528
aw 212,176
ma 68,232,56
aw 213,177
aw 214,178
ma 76,232,64
s_add_u32 m0, s16, 24576
s_add_u32 s24, s18, 245760
dn 11,48,24,0
ma 80,240,56
rd 232,6,22528
aw 215,179
ma 88,240,64
rd 240,5,23552
cv 184,184,185
ma 84,244,56
cv 185,186,187
cv 186,188,189
ma 92,244,64
dn 11,52,24,1024
s_waitcnt lgkmcnt(2)
mvc 40,248,72,32
rd 244,6,23552
cv 187,190,191
mv 40,252,80
aw 216,184
aw 217,185
mvc 48,252,84,36
rd 252,5,24576
aw 218,186
mv 48,248,76
dn 11,56,24,2048
mv 48,220,92
rd 248,6,24576
aw 219,187
mv 48,216,100
cv 192,192,193
cv 193,194,195
mv 40,216,96
rd 216,5,28672
cv 194,196,197
mv 40,220,88
rd 220,6,28672
cv 195,198,199
mv 40,224,104
aw 220,192
aw 221,193
mv 40,228,112
aw 222,194
aw 223,195
mv 48,228,116
rd 228,5,29696
v_permlane32_swap_b32 v24, v26
mv 48,224,108
rd 224,6,29696
v_permlane32_swap_b32 v25, v27
mvc 44,236,72,32
v_max3_f32 v20, v40, v41, v42
v_add_u32 v28, s89, v19
s_waitcnt lgkmcnt(3)
mv 44,232,80
v_permlane16_swap_b32 v24, v26
v_max3_f32 v20, v20, v43, v48
mvc 52,232,84,36
rd 232,5,30720
v_max3_f32 v20, v20, v49, v50
mv 52,236,76
rd 236,6,30720
v_permlane16_swap_b32 v25, v27
mv 52,240,92
ds_write_b128 v28, v[24:27] offset:0
ar 24,104
mv 52,244,100
ar 25,105
ar 26,106
mv 44,244,96
rd 244,5,34816
ar 27,107
mv 44,240,88
s_add_u32 m0, s16, 27648
rd 240,6,34816
dn 12,48,24,0
mv 44,252,104
ar 28,108
ar 29,109
mv 44,248,112
ar 30,110
ar 31,111
mv 52,248,116
rd 248,5,35840
v_mul_f32 v24, s26, v24
mv 52,252,108
dn 12,52,24,1024
mvc 56,216,72,32
rd 252,6,35840
v_max3_f32 v20, v20, v51, v44
s_waitcnt lgkmcnt(3)
mv 56,220,80
v_max3_f32 v20, v20, v45, v46
s_nop 0
v_max3_f32 v20, v20, v47, v52
mvc 64,220,84,36
rd 220,5,36864
v_max3_f32 v20, v20, v53, v54
mv 64,216,76
dn 12,56,24,2048
mv 64,228,92
rd 216,6,36864
v_add_u32 v5, 0xffff8000, v5
mv 64,224,100
v_add_u32 v6, 0xffff8000, v6
v_mul_f32 v25, s26, v25
mv 56,224,96
v_mul_f32 v26, s26, v26
v_mul_f32 v27, s26, v27
mv 56,228,88
cv 24,24,25
v_mul_f32 v28, s26, v28
mv 56,232,104
v_mul_f32 v29, s26, v29
v_mul_f32 v30, s26, v30
mv 56,236,112
s_waitcnt vmcnt(6) lgkmcnt(0)
s_barrier
mv 64,236,116
tr 224,1,0
tr 226,1,6144
mv 64,232,108
tr 228,1,12288
tr 230,1,18432
mvc 60,244,72,32
tr 236,1,1024
tr 238,1,7168
mv 60,240,80
tr 232,1,13312
tr 234,1,19456
mvc 68,240,84,36
tr 240,1,2048
tr 242,1,8192
mv 68,244,76
tr 244,1,14336
tr 246,1,20480
mv 68,248,92
v_max3_f32 v20, v20, v55, v56
v_max3_f32 v20, v20, v57, v58
mv 68,252,100
v_max3_f32 v20, v20, v59, v64
v_max3_f32 v20, v20, v65, v66
mv 60,252,96
tr 252,2,0
tr 254,2,6144
mv 60,248,88
s_add_u32 m0, s16, 49152
s_add_u32 s25, s20, 384
dl 15,72,25,0
mv 60,220,104
tr 248,2,12288
tr 250,2,18432
mv 60,216,112
v_mul_f32 v31, s26, v31
cv 25,26,27
mv 68,216,116
tr 216,2,1024
tr 218,2,7168
mv 68,220,108
dl 15,76,25,1024
s_waitcnt lgkmcnt(6)
mvz 168,224,120
tr 220,2,13312
tr 222,2,19456
mv 168,228,128
v_max3_f32 v20, v20, v67, v60
v_max3_f32 v20, v20, v61, v62
mvz 176,228,132
tr 228,2,2048
tr 230,2,8192
mv 176,224,124
dl 15,80,25,2048
mv 176,236,140
tr 224,2,14336
tr 226,2,20480
mv 176,232,148
v_max3_f32 v20, v20, v63, v68
v_max3_f32 v20, v20, v69, v70
mv 168,232,144
tr 232,3,0
tr 234,3,6144
mv 168,236,136
tr 236,3,12288
tr 238,3,18432
mv 168,240,152
v_max_f32 v20, v20, v71
v_cmp_lt_f32 vcc, s27, v20
s_cbranch_vccz Lc8
s_swappc_b64 s[30:31], s[28:29]
Lc8:
mv 168,244,160
v_exp_f32 v40, v40
mv 176,244,164
tr 244,3,1024
tr 246,3,7168
mv 176,240,156
tr 240,3,13312
tr 242,3,19456
s_waitcnt lgkmcnt(8)
mvz 172,252,120
v_exp_f32 v41, v41
mv 172,248,128
v_exp_f32 v42, v42
mvz 180,248,132
tr 248,3,2048
tr 250,3,8192
mv 180,252,124
tr 252,3,14336
tr 254,3,20480
mv 180,216,140
v_exp_f32 v43, v43
mv 180,220,148
v_exp_f32 v44, v44
mv 172,220,144
tr 220,4,0
tr 222,4,6144
mv 172,216,136
s_add_u32 m0, s16, 52224
tr 216,4,12288
dl 16,72,25,0
mv 172,228,152
tr 218,4,18432
v_exp_f32 v45, v45
mv 172,224,160
v_exp_f32 v46, v46
mv 180,224,164
tr 224,4,1024
tr 226,4,7168
mv 180,228,156
dl 16,76,25,1024
s_waitcnt lgkmcnt(5)
mvz 184,232,120
tr 228,4,13312
tr 230,4,19456
mv 184,236,128
v_exp_f32 v47, v47
mvz 192,236,132
tr 236,4,2048
tr 238,4,8192
mv 192,232,124
dl 16,80,25,2048
mv 192,244,140
tr 232,4,14336
tr 234,4,20480
mv 192,240,148
cv 40,40,41
cv 41,42,43
mv 184,240,144
cv 42,44,45
cv 43,46,47
mv 184,244,136
v_exp_f32 v48, v48
mv 184,248,152
v_exp_f32 v49, v49
mv 184,252,160
s_waitcnt vmcnt(6) lgkmcnt(0)
s_barrier
mv 192,252,164
tr 240,1,24576
tr 242,1,30720
mv 192,248,156
tr 244,2,24576
tr 246,2,30720
mvz 188,220,120
v_exp_f32 v50, v50
mv 188,216,128
v_exp_f32 v51, v51
mvz 196,216,132
v_exp_f32 v52, v52
mv 196,220,124
v_exp_f32 v53, v53
mv 196,224,140
v_exp_f32 v54, v54
mv 196,228,148
v_exp_f32 v55, v55
mv 188,228,144
cv 48,48,49
tr 252,3,24576
mv 188,224,136
s_add_u32 m0, s16, 0
s_add_u32 s22, s18, 270336
dn 9,36,22,0
mv 188,236,152
tr 254,3,30720
cv 49,50,51
mv 188,232,160
cv 50,52,53
cv 51,54,55
mv 196,232,164
tr 248,4,24576
tr 250,4,30720
mv 196,236,156
dn 9,40,22,1024
v_mfma_f32_16x16x32_bf16 a[244:247], a[240:243], v[40:43], a[244:247]
tr 216,1,25600
tr 218,1,31744
v_mfma_f32_16x16x32_bf16 a[248:251], a[240:243], v[48:51], a[248:251]
tr 220,2,25600
tr 222,2,31744
s_waitcnt lgkmcnt(7)
ma 0,240,40
tr 228,3,25600
tr 230,3,31744
ma 8,240,48
dn 9,44,22,2048
ma 4,244,40
tr 224,4,25600
tr 226,4,31744
ma 12,244,48
tr 232,1,26624
tr 234,1,32768
s_waitcnt lgkmcnt(10)
ma 16,252,40
tr 236,2,26624
tr 238,2,32768
ma 24,252,48
tr 240,3,26624
tr 242,3,32768
ma 20,248,40
tr 244,4,26624
tr 246,4,32768
ma 28,248,48
tr 252,1,36864
tr 254,1,43008
s_waitcnt lgkmcnt(12)
ma 32,216,40
tr 248,2,36864
tr 250,2,43008
ma 40,216,48
v_exp_f32 v56, v56
ma 36,220,40
v_exp_f32 v57, v57
ma 44,220,48
v_exp_f32 v58, v58
ma 48,228,40
v_exp_f32 v59, v59
ma 56,228,48
v_exp_f32 v60, v60
s_waitcnt lgkmcnt(2)
ma 52,224,40
v_exp_f32 v61, v61
ma 60,224,48
v_exp_f32 v62, v62
ma 64,232,40
v_exp_f32 v63, v63
ma 72,232,48
cv 56,56,57
cv 57,58,59
ma 68,236,40
cv 58,60,61
cv 59,62,63
ma 76,236,48
s_add_u32 m0, s16, 3072
v_exp_f32 v64, v64
dn 10,36,22,0
ma 80,240,40
v_exp_f32 v65, v65
ma 88,240,48
v_exp_f32 v66, v66
ma 84,244,40
v_exp_f32 v67, v67
ma 92,244,48
dn 10,40,22,1024
v_exp_f32 v68, v68
v_exp_f32 v69, v69
v_exp_f32 v70, v70
v_exp_f32 v71, v71
cv 64,64,65
tr 216,3,36864
tr 218,3,43008
v_mfma_f32_16x16x32_bf16 a[244:247], a[240:243], v[56:59], a[244:247]
cv 65,66,67
dn 10,44,22,2048
cv 66,68,69
cv 67,70,71
tr 220,4,36864
tr 222,4,43008
v_mfma_f32_16x16x32_bf16 a[248:251], a[240:243], v[64:67], a[248:251]
tr 228,1,37888
tr 230,1,44032
ma 0,252,56
tr 224,2,37888
tr 226,2,44032
ma 8,252,64
tr 232,3,37888
tr 234,3,44032
s_waitcnt lgkmcnt(10)
ma 4,248,56
tr 236,4,37888
tr 238,4,44032
ma 12,248,64
tr 240,1,38912
tr 242,1,45056
s_waitcnt lgkmcnt(12)
ma 16,216,56
tr 244,2,38912
tr 246,2,45056
ma 24,216,64
tr 252,3,38912
tr 254,3,45056
s_waitcnt lgkmcnt(12)
ma 20,220,56
tr 248,4,38912
tr 250,4,45056
ma 28,220,64
v_add_u32 v1, 0x2000, v1
cv 168,168,169
ma 32,228,56
cv 169,170,171
cv 170,172,173
ma 40,228,64
cv 171,174,175
aw 224,168
s_waitcnt lgkmcnt(6)
ma 36,224,56
aw 225,169
aw 226,170
ma 44,224,64
s_waitcnt vmcnt(6) lgkmcnt(0)
s_barrier
ma 48,232,56
tr 216,1,40960
tr 218,1,47104
ma 56,232,64
tr 220,1,53248
tr 222,1,59392
ma 52,236,56
tr 228,1,41984
tr 230,1,48128
ma 60,236,64
tr 224,1,54272
tr 226,1,60416
ma 64,240,56
aw 227,171
cv 176,176,177
ma 72,240,64
cv 177,178,179
cv 178,180,181
ma 68,244,56
cv 179,182,183
aw 228,176
ma 76,244,64
s_add_u32 m0, s16, 24576
s_add_u32 s23, s20, 512
dl 15,72,23,0
ma 80,252,56
aw 229,177
aw 230,178
ma 88,252,64
aw 231,179
tr 232,1,43008
ma 84,248,56
tr 234,1,49152
tr 236,1,55296
ma 92,248,64
dl 15,76,23,1024
s_waitcnt lgkmcnt(3)
mvz 168,216,120
tr 238,1,61440
v_add_u32 v2, 0x2000, v2
mv 168,220,128
tr 240,2,40960
tr 242,2,47104
mvz 176,220,132
tr 244,2,53248
tr 246,2,59392
mv 176,216,124
dl 15,80,23,2048
mv 176,228,140
tr 252,2,41984
tr 254,2,48128
mv 176,224,148
tr 248,2,54272
tr 250,2,60416
mv 168,224,144
tr 220,2,43008
tr 222,2,49152
mv 168,228,136
tr 216,2,55296
tr 218,2,61440
s_waitcnt lgkmcnt(10)
mv 168,232,152
v_add_u32 v3, 0x2000, v3
tr 224,3,40960
mv 168,236,160
tr 226,3,47104
tr 228,3,53248
mv 176,236,164
tr 230,3,59392
tr 236,3,41984
mv 176,232,156
tr 238,3,48128
)"
R"(tr 232,3,54272
mvz 172,240,120
tr 234,3,60416
cv 184,184,185
s_waitcnt lgkmcnt(10)
mv 172,244,128
cv 185,186,187
cv 186,188,189
mvz 180,244,132
cv 187,190,191
aw 232,184
mv 180,240,124
aw 233,185
aw 234,186
mv 180,252,140
aw 235,187
cv 192,192,193
mv 180,248,148
cv 193,194,195
cv 194,196,197
mv 172,248,144
cv 195,198,199
aw 236,192
mv 172,252,136
s_add_u32 m0, s16, 27648
aw 237,193
dl 16,72,23,0
mv 172,220,152
aw 238,194
aw 239,195
s_waitcnt lgkmcnt(0)
mv 172,216,160
tr 244,3,43008
tr 246,3,49152
mv 180,216,164
tr 240,3,55296
tr 242,3,61440
mv 180,220,156
dl 16,76,23,1024
mvz 184,224,120
v_add_u32 v4, 0x2000, v4
tr 248,4,40960
mv 184,228,128
tr 250,4,47104
tr 252,4,53248
mvz 192,228,132
tr 254,4,59392
tr 216,4,41984
mv 192,224,124
dl 16,80,23,2048
mv 192,236,140
tr 218,4,48128
tr 220,4,54272
mv 192,232,148
tr 222,4,60416
tr 228,4,43008
mv 184,232,144
tr 230,4,49152
tr 224,4,55296
mv 184,236,136
tr 226,4,61440
cv 168,168,169
s_waitcnt lgkmcnt(9)
mv 184,244,152
cv 169,170,171
cv 170,172,173
mv 184,240,160
cv 171,174,175
wr 168,49152
mv 192,240,164
cv 176,176,177
cv 177,178,179
mv 192,244,156
cv 178,180,181
s_waitcnt vmcnt(6) lgkmcnt(0)
s_barrier
mvz 188,248,120
rd 232,5,0
rd 236,6,0
mv 188,252,128
rd 240,5,1024
rd 244,6,1024
mvz 196,252,132
rd 252,5,2048
cv 179,182,183
mv 196,248,124
rd 248,6,2048
wr 176,53248
mv 196,216,140
cv 184,184,185
cv 185,186,187
mv 196,220,148
cv 192,192,193
cv 193,194,195
mv 188,220,144
rd 220,5,6144
cv 26,28,29
mv 188,216,136
s_add_u32 m0, s16, 49152
s_add_u32 s24, s18, 270336
dn 11,48,24,0
mv 188,228,152
rd 216,6,6144
cv 27,30,31
mv 188,224,160
v_permlane32_swap_b32 v24, v26
v_permlane32_swap_b32 v25, v27
mv 196,224,164
rd 224,5,7168
v_add_u32 v28, s89, v19
mv 196,228,156
dn 11,52,24,1024
s_waitcnt lgkmcnt(3)
mvc 40,232,72,32
rd 228,6,7168
cv 186,188,189
mv 40,236,80
cv 187,190,191
wr 184,57344
mvc 48,236,84,36
rd 236,5,8192
cv 194,196,197
mv 48,232,76
dn 11,56,24,2048
mv 48,240,92
rd 232,6,8192
cv 195,198,199
mv 48,244,100
wr 192,61440
v_permlane16_swap_b32 v24, v26
mv 40,244,96
rd 244,5,12288
v_permlane16_swap_b32 v25, v27
mv 40,240,88
rd 240,6,12288
ds_write_b128 v28, v[24:27] offset:4096
mv 40,252,104
ar 24,112
ar 25,113
mv 40,248,112
ar 26,114
ar 27,115
mv 48,248,116
rd 248,5,13312
ar 28,116
mv 48,252,108
rd 252,6,13312
ar 29,117
s_waitcnt lgkmcnt(5)
mvc 44,220,72,32
v_max3_f32 v20, v40, v41, v42
ar 30,118
mv 44,216,80
ar 31,119
v_max3_f32 v20, v20, v43, v48
mvc 52,216,84,36
rd 216,5,14336
v_max3_f32 v20, v20, v49, v50
mv 52,220,76
rd 220,6,14336
v_mul_f32 v24, s26, v24
mv 52,224,92
v_mul_f32 v25, s26, v25
v_mul_f32 v26, s26, v26
mv 52,228,100
v_mul_f32 v27, s26, v27
cv 24,24,25
mv 44,228,96
rd 228,5,18432
v_mul_f32 v28, s26, v28
mv 44,224,88
s_add_u32 m0, s16, 52224
rd 224,6,18432
dn 12,48,24,0
mv 44,236,104
v_mul_f32 v29, s26, v29
v_mul_f32 v30, s26, v30
mv 44,232,112
v_mul_f32 v31, s26, v31
cv 25,26,27
mv 52,232,116
rd 232,5,19456
cv 26,28,29
mv 52,236,108
dn 12,52,24,1024
s_waitcnt lgkmcnt(3)
mvc 56,244,72,32
rd 236,6,19456
v_max3_f32 v20, v20, v51, v44
mv 56,240,80
v_max3_f32 v20, v20, v45, v46
s_nop 0
v_max3_f32 v20, v20, v47, v52
mvc 64,240,84,36
rd 240,5,20480
v_max3_f32 v20, v20, v53, v54
mv 64,244,76
dn 12,56,24,2048
mv 64,248,92
rd 244,6,20480
cv 27,30,31
mv 64,252,100
v_permlane32_swap_b32 v24, v26
v_permlane32_swap_b32 v25, v27
mv 56,252,96
v_add_u32 v28, s89, v19
v_permlane16_swap_b32 v24, v26
mv 56,248,88
v_permlane16_swap_b32 v25, v27
ds_write_b128 v28, v[24:27] offset:1024
mv 56,216,104
ar 24,120
ar 25,121
mv 56,220,112
s_waitcnt vmcnt(6) lgkmcnt(0)
s_barrier
mv 64,220,116
tr 252,1,16384
tr 254,1,22528
mv 64,216,108
tr 248,1,28672
tr 250,1,34816
mvc 60,228,72,32
tr 220,1,17408
tr 222,1,23552
mv 60,224,80
tr 216,1,29696
tr 218,1,35840
mvc 68,224,84,36
tr 224,1,18432
tr 226,1,24576
mv 68,228,76
tr 228,1,30720
tr 230,1,36864
mv 68,232,92
v_max3_f32 v20, v20, v55, v56
v_max3_f32 v20, v20, v57, v58
mv 68,236,100
v_max3_f32 v20, v20, v59, v64
v_max3_f32 v20, v20, v65, v66
mv 60,236,96
tr 236,2,16384
tr 238,2,22528
mv 60,232,88
s_add_u32 m0, s16, 0
s_add_u32 s25, s18, 294912
dn 9,36,25,0
mv 60,240,104
tr 232,2,28672
tr 234,2,34816
mv 60,244,112
ar 26,122
ar 27,123
mv 68,244,116
tr 244,2,17408
tr 246,2,23552
mv 68,240,108
dn 9,40,25,1024
s_waitcnt lgkmcnt(6)
mvz 168,252,120
tr 240,2,29696
tr 242,2,35840
mv 168,248,128
v_max3_f32 v20, v20, v67, v60
v_max3_f32 v20, v20, v61, v62
mvz 176,248,132
tr 248,2,18432
tr 250,2,24576
mv 176,252,124
dn 9,44,25,2048
mv 176,220,140
tr 252,2,30720
tr 254,2,36864
mv 176,216,148
v_max3_f32 v20, v20, v63, v68
v_max3_f32 v20, v20, v69, v70
mv 168,216,144
tr 216,3,16384
tr 218,3,22528
mv 168,220,136
tr 220,3,28672
tr 222,3,34816
mv 168,224,152
v_max_f32 v20, v20, v71
v_cmp_lt_f32 vcc, s27, v20
s_cbranch_vccz Lc9
s_swappc_b64 s[30:31], s[28:29]
Lc9:
mv 168,228,160
v_exp_f32 v40, v40
mv 176,228,164
tr 228,3,17408
tr 230,3,23552
mv 176,224,156
tr 224,3,29696
tr 226,3,35840
s_waitcnt lgkmcnt(8)
mvz 172,236,120
v_exp_f32 v41, v41
mv 172,232,128
v_exp_f32 v42, v42
mvz 180,232,132
tr 232,3,18432
tr 234,3,24576
mv 180,236,124
tr 236,3,30720
tr 238,3,36864
mv 180,244,140
v_exp_f32 v43, v43
mv 180,240,148
v_exp_f32 v44, v44
mv 172,240,144
tr 240,4,16384
tr 242,4,22528
mv 172,244,136
s_add_u32 m0, s16, 3072
tr 244,4,28672
dn 10,36,25,0
mv 172,248,152
tr 246,4,34816
v_exp_f32 v45, v45
mv 172,252,160
v_exp_f32 v46, v46
mv 180,252,164
tr 252,4,17408
tr 254,4,23552
mv 180,248,156
dn 10,40,25,1024
s_waitcnt lgkmcnt(5)
mvz 184,216,120
tr 248,4,29696
tr 250,4,35840
mv 184,220,128
v_exp_f32 v47, v47
mvz 192,220,132
tr 220,4,18432
tr 222,4,24576
mv 192,216,124
dn 10,44,25,2048
mv 192,228,140
tr 216,4,30720
tr 218,4,36864
mv 192,224,148
cv 40,40,41
cv 41,42,43
mv 184,224,144
cv 42,44,45
cv 43,46,47
mv 184,228,136
v_exp_f32 v48, v48
mv 184,232,152
v_exp_f32 v49, v49
mv 184,236,160
s_waitcnt vmcnt(6) lgkmcnt(0)
s_barrier
mv 192,236,164
tr 224,1,40960
tr 226,1,47104
mv 192,232,156
tr 228,2,40960
tr 230,2,47104
mvz 188,240,120
v_exp_f32 v50, v50
mv 188,244,128
v_exp_f32 v51, v51
mvz 196,244,132
v_exp_f32 v52, v52
mv 196,240,124
v_exp_f32 v53, v53
mv 196,252,140
v_exp_f32 v54, v54
mv 196,248,148
v_exp_f32 v55, v55
mv 188,248,144
cv 48,48,49
tr 236,3,40960
mv 188,252,136
s_add_u32 m0, s16, 24576
s_add_u32 s22, s20, 640
dl 15,72,22,0
mv 188,220,152
tr 238,3,47104
cv 49,50,51
mv 188,216,160
cv 50,52,53
cv 51,54,55
mv 196,216,164
tr 232,4,40960
tr 234,4,47104
mv 196,220,156
dl 15,76,22,1024
v_mfma_f32_16x16x32_bf16 a[244:247], a[240:243], v[40:43], a[244:247]
tr 244,1,41984
tr 246,1,48128
v_mfma_f32_16x16x32_bf16 a[248:251], a[240:243], v[48:51], a[248:251]
tr 240,2,41984
tr 242,2,48128
s_waitcnt lgkmcnt(7)
ma 0,224,40
tr 248,3,41984
tr 250,3,48128
ma 8,224,48
dl 15,80,22,2048
ma 4,228,40
tr 252,4,41984
tr 254,4,48128
ma 12,228,48
tr 216,1,43008
tr 218,1,49152
s_waitcnt lgkmcnt(10)
ma 16,236,40
tr 220,2,43008
tr 222,2,49152
ma 24,236,48
tr 224,3,43008
tr 226,3,49152
ma 20,232,40
tr 228,4,43008
tr 230,4,49152
ma 28,232,48
tr 236,1,53248
tr 238,1,59392
s_waitcnt lgkmcnt(12)
ma 32,244,40
tr 232,2,53248
tr 234,2,59392
ma 40,244,48
v_exp_f32 v56, v56
ma 36,240,40
v_exp_f32 v57, v57
ma 44,240,48
v_exp_f32 v58, v58
ma 48,248,40
v_exp_f32 v59, v59
ma 56,248,48
v_exp_f32 v60, v60
s_waitcnt lgkmcnt(2)
ma 52,252,40
v_exp_f32 v61, v61
ma 60,252,48
v_exp_f32 v62, v62
ma 64,216,40
v_exp_f32 v63, v63
ma 72,216,48
cv 56,56,57
cv 57,58,59
ma 68,220,40
cv 58,60,61
cv 59,62,63
ma 76,220,48
s_add_u32 m0, s16, 27648
v_exp_f32 v64, v64
dl 16,72,22,0
ma 80,224,40
v_exp_f32 v65, v65
ma 88,224,48
v_exp_f32 v66, v66
ma 84,228,40
v_exp_f32 v67, v67
ma 92,228,48
dl 16,76,22,1024
v_exp_f32 v68, v68
v_exp_f32 v69, v69
v_exp_f32 v70, v70
v_exp_f32 v71, v71
cv 64,64,65
tr 244,3,53248
tr 246,3,59392
v_mfma_f32_16x16x32_bf16 a[244:247], a[240:243], v[56:59], a[244:247]
cv 65,66,67
dl 16,80,22,2048
cv 66,68,69
cv 67,70,71
tr 240,4,53248
tr 242,4,59392
v_mfma_f32_16x16x32_bf16 a[248:251], a[240:243], v[64:67], a[248:251]
tr 248,1,54272
tr 250,1,60416
ma 0,236,56
tr 252,2,54272
tr 254,2,60416
ma 8,236,64
tr 216,3,54272
tr 218,3,60416
s_waitcnt lgkmcnt(10)
ma 4,232,56
tr 220,4,54272
tr 222,4,60416
ma 12,232,64
tr 224,1,55296
tr 226,1,61440
s_waitcnt lgkmcnt(12)
ma 16,244,56
tr 228,2,55296
tr 230,2,61440
ma 24,244,64
tr 236,3,55296
tr 238,3,61440
s_waitcnt lgkmcnt(12)
ma 20,240,56
tr 232,4,55296
tr 234,4,61440
ma 28,240,64
cv 168,168,169
cv 169,170,171
ma 32,248,56
cv 170,172,173
cv 171,174,175
ma 40,248,64
aw 96,168
aw 97,169
s_waitcnt lgkmcnt(6)
ma 36,252,56
aw 98,170
aw 99,171
ma 44,252,64
s_waitcnt vmcnt(6) lgkmcnt(0)
s_barrier
ma 48,216,56
rd 244,5,0
rd 240,6,0
ma 56,216,64
rd 248,5,1024
rd 252,6,1024
ma 52,220,56
rd 216,5,2048
cv 176,176,177
ma 60,220,64
rd 220,6,2048
cv 177,178,179
ma 64,224,56
cv 178,180,181
cv 179,182,183
ma 72,224,64
rd 224,5,6144
aw 100,176
ma 68,228,56
aw 101,177
aw 102,178
ma 76,228,64
s_add_u32 m0, s16, 49152
s_add_u32 s23, s18, 294912
dn 11,48,23,0
ma 80,236,56
rd 228,6,6144
aw 103,179
ma 88,236,64
rd 236,5,7168
cv 184,184,185
ma 84,232,56
cv 185,186,187
cv 186,188,189
ma 92,232,64
dn 11,52,23,1024
s_waitcnt lgkmcnt(2)
mvc 40,244,72,32
rd 232,6,7168
cv 187,190,191
mv 40,240,80
aw 104,184
aw 105,185
mvc 48,240,84,36
rd 240,5,8192
aw 106,186
mv 48,244,76
dn 11,56,23,2048
mv 48,248,92
rd 244,6,8192
aw 107,187
mv 48,252,100
cv 192,192,193
cv 193,194,195
mv 40,252,96
rd 252,5,12288
cv 194,196,197
mv 40,248,88
rd 248,6,12288
cv 195,198,199
mv 40,216,104
aw 108,192
aw 109,193
mv 40,220,112
aw 110,194
aw 111,195
mv 48,220,116
rd 220,5,13312
ar 28,124
mv 48,216,108
rd 216,6,13312
ar 29,125
mvc 44,224,72,32
v_max3_f32 v20, v40, v41, v42
ar 30,126
s_waitcnt lgkmcnt(3)
mv 44,228,80
ar 31,127
v_max3_f32 v20, v20, v43, v48
mvc 52,228,84,36
rd 228,5,14336
v_max3_f32 v20, v20, v49, v50
mv 52,224,76
rd 224,6,14336
v_mul_f32 v24, s26, v24
mv 52,236,92
v_mul_f32 v25, s26, v25
v_mul_f32 v26, s26, v26
mv 52,232,100
v_mul_f32 v27, s26, v27
cv 24,24,25
mv 44,232,96
rd 232,5,18432
v_mul_f32 v28, s26, v28
mv 44,236,88
s_add_u32 m0, s16, 52224
rd 236,6,18432
dn 12,48,23,0
mv 44,240,104
v_mul_f32 v29, s26, v29
v_mul_f32 v30, s26, v30
mv 44,244,112
v_mul_f32 v31, s26, v31
cv 25,26,27
mv 52,244,116
rd 244,5,19456
cv 26,28,29
mv 52,240,108
dn 12,52,23,1024
mvc 56,252,72,32
rd 240,6,19456
v_max3_f32 v20, v20, v51, v44
s_waitcnt lgkmcnt(3)
mv 56,248,80
v_max3_f32 v20, v20, v45, v46
s_nop 0
v_max3_f32 v20, v20, v47, v52
mvc 64,248,84,36
rd 248,5,20480
v_max3_f32 v20, v20, v53, v54
mv 64,252,76
dn 12,56,23,2048
mv 64,220,92
rd 252,6,20480
cv 27,30,31
mv 64,216,100
v_permlane32_swap_b32 v24, v26
v_permlane32_swap_b32 v25, v27
mv 56,216,96
v_add_u32 v28, s89, v19
v_permlane16_swap_b32 v24, v26
mv 56,220,88
v_permlane16_swap_b32 v25, v27
ds_write_b128 v28, v[24:27] offset:5120
mv 56,228,104
ar 24,128
ar 25,129
mv 56,224,112
s_waitcnt vmcnt(6) lgkmcnt(0)
s_barrier
mv 64,224,116
tr 216,1,16384
tr 218,1,22528
mv 64,228,108
tr 220,1,28672
tr 222,1,34816
mvc 60,232,72,32
tr 224,1,17408
tr 226,1,23552
mv 60,236,80
tr 228,1,29696
tr 230,1,35840
mvc 68,236,84,36
tr 236,1,18432
tr 238,1,24576
mv 68,232,76
tr 232,1,30720
tr 234,1,36864
mv 68,244,92
v_max3_f32 v20, v20, v55, v56
v_max3_f32 v20, v20, v57, v58
mv 68,240,100
v_max3_f32 v20, v20, v59, v64
v_max3_f32 v20, v20, v65, v66
mv 60,240,96
tr 240,2,16384
tr 242,2,22528
mv 60,244,88
s_add_u32 m0, s16, 0
s_add_u32 s24, s18, 319488
dn 9,36,24,0
mv 60,248,104
tr 244,2,28672
tr 246,2,34816
mv 60,252,112
ar 26,130
ar 27,131
mv 68,252,116
tr 252,2,17408
tr 254,2,23552
mv 68,248,108
dn 9,40,24,1024
s_waitcnt lgkmcnt(6)
mvz 168,216,120
tr 248,2,29696
tr 250,2,35840
mv 168,220,128
v_max3_f32 v20, v20, v67, v60
v_max3_f32 v20, v20, v61, v62
mvz 176,220,132
tr 220,2,18432
tr 222,2,24576
mv 176,216,124
dn 9,44,24,2048
mv 176,224,140
tr 216,2,30720
tr 218,2,36864
mv 176,228,148
v_max3_f32 v20, v20, v63, v68
v_max3_f32 v20, v20, v69, v70
mv 168,228,144
tr 228,3,16384
tr 230,3,22528
mv 168,224,136
tr 224,3,28672
tr 226,3,34816
mv 168,236,152
v_max_f32 v20, v20, v71
v_cmp_lt_f32 vcc, s27, v20
s_cbranch_vccz Lc10
s_swappc_b64 s[30:31], s[28:29]
Lc10:
mv 168,232,160
v_exp_f32 v40, v40
mv 176,232,164
tr 232,3,17408
tr 234,3,23552
mv 176,236,156
tr 236,3,29696
tr 238,3,35840
s_waitcnt lgkmcnt(8)
mvz 172,240,120
v_exp_f32 v41, v41
mv 172,244,128
v_exp_f32 v42, v42
mvz 180,244,132
tr 244,3,18432
tr 246,3,24576
mv 180,240,124
tr 240,3,30720
tr 242,3,36864
mv 180,252,140
v_exp_f32 v43, v43
mv 180,248,148
v_exp_f32 v44, v44
mv 172,248,144
tr 248,4,16384
tr 250,4,22528
mv 172,252,136
s_add_u32 m0, s16, 3072
tr 252,4,28672
dn 10,36,24,0
mv 172,220,152
tr 254,4,34816
v_exp_f32 v45, v45
mv 172,216,160
v_exp_f32 v46, v46
mv 180,216,164
tr 216,4,17408
tr 218,4,23552
mv 180,220,156
dn 10,40,24,1024
s_waitcnt lgkmcnt(5)
mvz 184,228,120
tr 220,4,29696
tr 222,4,35840
mv 184,224,128
v_exp_f32 v47, v47
mvz 192,224,132
tr 224,4,18432
tr 226,4,24576
mv 192,228,124
dn 10,44,24,2048
mv 192,232,140
tr 228,4,30720
tr 230,4,36864
mv 192,236,148
cv 40,40,41
cv 41,42,43
mv 184,236,144
cv 42,44,45
cv 43,46,47
mv 184,232,136
v_exp_f32 v48, v48
mv 184,244,152
v_exp_f32 v49, v49
mv 184,240,160
s_waitcnt vmcnt(6) lgkmcnt(0)
s_barrier
mv 192,240,164
tr 236,1,40960
tr 238,1,47104
mv 192,244,156
tr 232,2,40960
tr 234,2,47104
mvz 188,248,120
v_exp_f32 v50, v50
mv 188,252,128
v_exp_f32 v51, v51
mvz 196,252,132
v_exp_f32 v52, v52
mv 196,248,124
v_exp_f32 v53, v53
mv 196,216,140
v_exp_f32 v54, v54
mv 196,220,148
v_exp_f32 v55, v55
mv 188,220,144
cv 48,48,49
tr 240,3,40960
mv 188,216,136
s_add_u32 m0, s16, 24576
s_add_u32 s25, s20, 768
dl 15,72,25,0
mv 188,224,152
tr 242,3,47104
cv 49,50,51
mv 188,228,160
cv 50,52,53
cv 51,54,55
mv 196,228,164
tr 244,4,40960
tr 246,4,47104
mv 196,224,156
dl 15,76,25,1024
v_mfma_f32_16x16x32_bf16 a[244:247], a[240:243], v[40:43], a[244:247]
tr 252,1,41984
tr 254,1,48128
v_mfma_f32_16x16x32_bf16 a[248:251], a[240:243], v[48:51], a[248:251]
tr 248,2,41984
tr 250,2,48128
s_waitcnt lgkmcnt(7)
ma 0,236,40
tr 220,3,41984
tr 222,3,48128
ma 8,236,48
dl 15,80,25,2048
ma 4,232,40
tr 216,4,41984
tr 218,4,48128
ma 12,232,48
tr 228,1,43008
tr 230,1,49152
s_waitcnt lgkmcnt(10)
ma 16,240,40
tr 224,2,43008
tr 226,2,49152
ma 24,240,48
tr 236,3,43008
tr 238,3,49152
ma 20,244,40
tr 232,4,43008
tr 234,4,49152
ma 28,244,48
tr 240,1,53248
tr 242,1,59392
s_waitcnt lgkmcnt(12)
ma 32,252,40
tr 244,2,53248
tr 246,2,59392
ma 40,252,48
v_exp_f32 v56, v56
ma 36,248,40
v_exp_f32 v57, v57
ma 44,248,48
v_exp_f32 v58, v58
ma 48,220,40
v_exp_f32 v59, v59
ma 56,220,48
v_exp_f32 v60, v60
s_waitcnt lgkmcnt(2)
ma 52,216,40
v_exp_f32 v61, v61
ma 60,216,48
v_exp_f32 v62, v62
ma 64,228,40
v_exp_f32 v63, v63
ma 72,228,48
cv 56,56,57
cv 57,58,59
ma 68,224,40
cv 58,60,61
cv 59,62,63
ma 76,224,48
s_add_u32 m0, s16, 27648
v_exp_f32 v64, v64
dl 16,72,25,0
ma 80,236,40
v_exp_f32 v65, v65
ma 88,236,48
v_exp_f32 v66, v66
ma 84,232,40
v_exp_f32 v67, v67
ma 92,232,48
dl 16,76,25,1024
v_exp_f32 v68, v68
v_exp_f32 v69, v69
v_exp_f32 v70, v70
v_exp_f32 v71, v71
cv 64,64,65
tr 252,3,53248
tr 254,3,59392
v_mfma_f32_16x16x32_bf16 a[244:247], a[240:243], v[56:59], a[244:247]
cv 65,66,67
dl 16,80,25,2048
cv 66,68,69
cv 67,70,71
tr 248,4,53248
tr 250,4,59392
v_mfma_f32_16x16x32_bf16 a[248:251], a[240:243], v[64:67], a[248:251]
tr 220,1,54272
tr 222,1,60416
ma 0,240,56
tr 216,2,54272
tr 218,2,60416
ma 8,240,64
tr 228,3,54272
tr 230,3,60416
s_waitcnt lgkmcnt(10)
ma 4,244,56
tr 224,4,54272
tr 226,4,60416
ma 12,244,64
tr 236,1,55296
tr 238,1,61440
s_waitcnt lgkmcnt(12)
ma 16,252,56
tr 232,2,55296
tr 234,2,61440
ma 24,252,64
tr 240,3,55296
tr 242,3,61440
s_waitcnt lgkmcnt(12)
ma 20,248,56
tr 244,4,55296
tr 246,4,61440
ma 28,248,64
cv 168,168,169
cv 169,170,171
ma 32,220,56
cv 170,172,173
cv 171,174,175
ma 40,220,64
aw 112,168
aw 113,169
s_waitcnt lgkmcnt(6)
ma 36,216,56
aw 114,170
aw 115,171
ma 44,216,64
s_waitcnt vmcnt(6) lgkmcnt(0)
s_barrier
ma 48,228,56
rd 252,5,0
rd 248,6,0
ma 56,228,64
rd 220,5,1024
rd 216,6,1024
ma 52,224,56
rd 228,5,2048
cv 176,176,177
ma 60,224,64
rd 224,6,2048
cv 177,178,179
ma 64,236,56
cv 178,180,181
cv 179,182,183
ma 72,236,64
rd 236,5,6144
aw 116,176
ma 68,232,56
aw 117,177
aw 118,178
ma 76,232,64
s_add_u32 m0, s16, 49152
s_add_u32 s22, s18, 319488
dn 11,48,22,0
ma 80,240,56
rd 232,6,6144
aw 119,179
ma 88,240,64
rd 240,5,7168
cv 184,184,185
ma 84,244,56
cv 185,186,187
cv 186,188,189
ma 92,244,64
dn 11,52,22,1024
s_waitcnt lgkmcnt(2)
mvc 40,252,72,32
rd 244,6,7168
cv 187,190,191
mv 40,248,80
aw 120,184
aw 121,185
mvc 48,248,84,36
rd 248,5,8192
aw 122,186
mv 48,252,76
dn 11,56,22,2048
mv 48,220,92
rd 252,6,8192
aw 123,187
mv 48,216,100
cv 192,192,193
cv 193,194,195
mv 40,216,96
rd 216,5,12288
cv 194,196,197
mv 40,220,88
rd 220,6,12288
cv 195,198,199
mv 40,228,104
aw 124,192
aw 125,193
mv 40,224,112
aw 126,194
aw 127,195
mv 48,224,116
rd 224,5,13312
ar 28,132
mv 48,228,108
rd 228,6,13312
ar 29,133
mvc 44,236,72,32
v_max3_f32 v20, v40, v41, v42
ar 30,134
s_waitcnt lgkmcnt(3)
mv 44,232,80
ar 31,135
v_max3_f32 v20, v20, v43, v48
mvc 52,232,84,36
rd 232,5,14336
v_max3_f32 v20, v20, v49, v50
mv 52,236,76
rd 236,6,14336
v_mul_f32 v24, s26, v24
mv 52,240,92
v_mul_f32 v25, s26, v25
v_mul_f32 v26, s26, v26
mv 52,244,100
v_mul_f32 v27, s26, v27
cv 24,24,25
mv 44,244,96
rd 244,5,18432
v_mul_f32 v28, s26, v28
mv 44,240,88
s_add_u32 m0, s16, 52224
rd 240,6,18432
dn 12,48,22,0
mv 44,248,104
v_mul_f32 v29, s26, v29
v_mul_f32 v30, s26, v30
mv 44,252,112
v_mul_f32 v31, s26, v31
cv 25,26,27
mv 52,252,116
rd 252,5,19456
cv 26,28,29
mv 52,248,108
dn 12,52,22,1024
mvc 56,216,72,32
rd 248,6,19456
v_max3_f32 v20, v20, v51, v44
s_waitcnt lgkmcnt(3)
mv 56,220,80
v_max3_f32 v20, v20, v45, v46
s_nop 0
v_max3_f32 v20, v20, v47, v52
mvc 64,220,84,36
rd 220,5,20480
v_max3_f32 v20, v20, v53, v54
mv 64,216,76
dn 12,56,22,2048
mv 64,224,92
rd 216,6,20480
cv 27,30,31
mv 64,228,100
v_permlane32_swap_b32 v24, v26
v_permlane32_swap_b32 v25, v27
mv 56,228,96
v_add_u32 v28, s89, v19
v_permlane16_swap_b32 v24, v26
mv 56,224,88
v_permlane16_swap_b32 v25, v27
ds_write_b128 v28, v[24:27] offset:16384
mv 56,232,104
ar 24,136
ar 25,137
mv 56,236,112
s_waitcnt vmcnt(6) lgkmcnt(0)
s_barrier
mv 64,236,116
tr 228,1,16384
tr 230,1,22528
mv 64,232,108
tr 224,1,28672
tr 226,1,34816
mvc 60,244,72,32
tr 236,1,17408
tr 238,1,23552
mv 60,240,80
tr 232,1,29696
tr 234,1,35840
mvc 68,240,84,36
tr 240,1,18432
tr 242,1,24576
mv 68,244,76
tr 244,1,30720
tr 246,1,36864
mv 68,252,92
v_max3_f32 v20, v20, v55, v56
v_max3_f32 v20, v20, v57, v58
mv 68,248,100
v_max3_f32 v20, v20, v59, v64
v_max3_f32 v20, v20, v65, v66
mv 60,248,96
tr 248,2,16384
tr 250,2,22528
mv 60,252,88
s_add_u32 m0, s16, 0
s_add_u32 s23, s20, 896
dl 15,72,23,0
mv 60,220,104
tr 252,2,28672
tr 254,2,34816
mv 60,216,112
ar 26,138
ar 27,139
mv 68,216,116
tr 216,2,17408
tr 218,2,23552
mv 68,220,108
dl 15,76,23,1024
s_waitcnt lgkmcnt(6)
mvz 168,228,120
tr 220,2,29696
tr 222,2,35840
mv 168,224,128
v_max3_f32 v20, v20, v67, v60
v_max3_f32 v20, v20, v61, v62
mvz 176,224,132
tr 224,2,18432
tr 226,2,24576
mv 176,228,124
dl 15,80,23,2048
mv 176,236,140
tr 228,2,30720
tr 230,2,36864
mv 176,232,148
v_max3_f32 v20, v20, v63, v68
v_max3_f32 v20, v20, v69, v70
mv 168,232,144
tr 232,3,16384
tr 234,3,22528
mv 168,236,136
tr 236,3,28672
tr 238,3,34816
mv 168,240,152
v_max_f32 v20, v20, v71
v_cmp_lt_f32 vcc, s27, v20
s_cbranch_vccz Lc11
s_swappc_b64 s[30:31], s[28:29]
Lc11:
mv 168,244,160
v_exp_f32 v40, v40
mv 176,244,164
tr 244,3,17408
tr 246,3,23552
mv 176,240,156
tr 240,3,29696
tr 242,3,35840
s_waitcnt lgkmcnt(8)
mvz 172,248,120
v_exp_f32 v41, v41
mv 172,252,128
v_exp_f32 v42, v42
mvz 180,252,132
tr 252,3,18432
tr 254,3,24576
mv 180,248,124
tr 248,3,30720
tr 250,3,36864
mv 180,216,140
v_exp_f32 v43, v43
mv 180,220,148
v_exp_f32 v44, v44
mv 172,220,144
tr 220,4,16384
tr 222,4,22528
mv 172,216,136
s_add_u32 m0, s16, 3072
tr 216,4,28672
dl 16,72,23,0
mv 172,224,152
tr 218,4,34816
v_exp_f32 v45, v45
mv 172,228,160
v_exp_f32 v46, v46
mv 180,228,164
tr 228,4,17408
tr 230,4,23552
mv 180,224,156
dl 16,76,23,1024
s_waitcnt lgkmcnt(5)
mvz 184,232,120
tr 224,4,29696
tr 226,4,35840
mv 184,236,128
v_exp_f32 v47, v47
mvz 192,236,132
tr 236,4,18432
tr 238,4,24576
mv 192,232,124
dl 16,80,23,2048
mv 192,244,140
tr 232,4,30720
tr 234,4,36864
mv 192,240,148
cv 40,40,41
cv 41,42,43
mv 184,240,144
cv 42,44,45
cv 43,46,47
mv 184,244,136
v_exp_f32 v48, v48
mv 184,252,152
v_exp_f32 v49, v49
mv 184,248,160
s_waitcnt vmcnt(6) lgkmcnt(0)
s_barrier
mv 192,248,164
tr 240,1,40960
tr 242,1,47104
mv 192,252,156
tr 244,2,40960
tr 246,2,47104
mvz 188,220,120
v_exp_f32 v50, v50
mv 188,216,128
v_exp_f32 v51, v51
mvz 196,216,132
v_exp_f32 v52, v52
mv 196,220,124
v_exp_f32 v53, v53
mv 196,228,140
v_exp_f32 v54, v54
mv 196,224,148
v_exp_f32 v55, v55
mv 188,224,144
cv 48,48,49
tr 248,3,40960
mv 188,228,136
s_add_u32 m0, s16, 24576
s_add_u32 s24, s18, 344064
dn 9,36,24,0
mv 188,236,152
tr 250,3,47104
cv 49,50,51
mv 188,232,160
cv 50,52,53
cv 51,54,55
mv 196,232,164
tr 252,4,40960
tr 254,4,47104
mv 196,236,156
dn 9,40,24,1024
v_mfma_f32_16x16x32_bf16 a[244:247], a[240:243], v[40:43], a[244:247]
tr 216,1,41984
tr 218,1,48128
v_mfma_f32_16x16x32_bf16 a[248:251], a[240:243], v[48:51], a[248:251]
tr 220,2,41984
tr 222,2,48128
s_waitcnt lgkmcnt(7)
ma 0,240,40
tr 224,3,41984
tr 226,3,48128
ma 8,240,48
dn 9,44,24,2048
ma 4,244,40
tr 228,4,41984
tr 230,4,48128
ma 12,244,48
tr 232,1,43008
tr 234,1,49152
s_waitcnt lgkmcnt(10)
ma 16,248,40
tr 236,2,43008
tr 238,2,49152
ma 24,248,48
tr 240,3,43008
tr 242,3,49152
ma 20,252,40
tr 244,4,43008
tr 246,4,49152
ma 28,252,48
tr 248,1,53248
tr 250,1,59392
s_waitcnt lgkmcnt(12)
ma 32,216,40
tr 252,2,53248
tr 254,2,59392
ma 40,216,48
v_exp_f32 v56, v56
ma 36,220,40
v_exp_f32 v57, v57
ma 44,220,48
v_exp_f32 v58, v58
ma 48,224,40
v_exp_f32 v59, v59
ma 56,224,48
v_exp_f32 v60, v60
s_waitcnt lgkmcnt(2)
ma 52,228,40
v_exp_f32 v61, v61
ma 60,228,48
v_exp_f32 v62, v62
ma 64,232,40
v_exp_f32 v63, v63
ma 72,232,48
cv 56,56,57
cv 57,58,59
ma 68,236,40
cv 58,60,61
cv 59,62,63
ma 76,236,48
s_add_u32 m0, s16, 27648
v_exp_f32 v64, v64
dn 10,36,24,0
ma 80,240,40
v_exp_f32 v65, v65
ma 88,240,48
v_exp_f32 v66, v66
ma 84,244,40
v_exp_f32 v67, v67
ma 92,244,48
dn 10,40,24,1024
v_exp_f32 v68, v68
v_exp_f32 v69, v69
v_exp_f32 v70, v70
v_exp_f32 v71, v71
cv 64,64,65
tr 216,3,53248
tr 218,3,59392
v_mfma_f32_16x16x32_bf16 a[244:247], a[240:243], v[56:59], a[244:247]
cv 65,66,67
dn 10,44,24,2048
cv 66,68,69
cv 67,70,71
tr 220,4,53248
tr 222,4,59392
v_mfma_f32_16x16x32_bf16 a[248:251], a[240:243], v[64:67], a[248:251]
tr 224,1,54272
tr 226,1,60416
ma 0,248,56
tr 228,2,54272
tr 230,2,60416
ma 8,248,64
tr 232,3,54272
tr 234,3,60416
s_waitcnt lgkmcnt(10)
ma 4,252,56
tr 236,4,54272
tr 238,4,60416
ma 12,252,64
tr 240,1,55296
tr 242,1,61440
s_waitcnt lgkmcnt(12)
ma 16,216,56
tr 244,2,55296
tr 246,2,61440
ma 24,216,64
tr 248,3,55296
tr 250,3,61440
s_waitcnt lgkmcnt(12)
ma 20,220,56
tr 252,4,55296
tr 254,4,61440
ma 28,220,64
v_add_u32 v1, 0xffffe000, v1
cv 168,168,169
ma 32,224,56
cv 169,170,171
cv 170,172,173
ma 40,224,64
cv 171,174,175
aw 128,168
s_waitcnt lgkmcnt(6)
ma 36,228,56
aw 129,169
aw 130,170
ma 44,228,64
s_waitcnt vmcnt(6) lgkmcnt(0)
s_barrier
ma 48,232,56
tr 216,1,0
tr 218,1,6144
ma 56,232,64
tr 220,1,12288
tr 222,1,18432
ma 52,236,56
tr 224,1,1024
tr 226,1,7168
ma 60,236,64
tr 228,1,13312
tr 230,1,19456
ma 64,240,56
aw 131,171
cv 176,176,177
ma 72,240,64
cv 177,178,179
cv 178,180,181
ma 68,244,56
cv 179,182,183
aw 132,176
ma 76,244,64
s_add_u32 m0, s16, 49152
s_add_u32 s25, s20, 1024
dl 15,72,25,0
ma 80,248,56
aw 133,177
aw 134,178
ma 88,248,64
aw 135,179
tr 232,1,2048
ma 84,252,56
tr 234,1,8192
tr 236,1,14336
ma 92,252,64
dl 15,76,25,1024
s_waitcnt lgkmcnt(3)
mvz 168,216,120
tr 238,1,20480
v_add_u32 v2, 0xffffe000, v2
mv 168,220,128
tr 240,2,0
tr 242,2,6144
mvz 176,220,132
tr 244,2,12288
tr 246,2,18432
mv 176,216,124
dl 15,80,25,2048
mv 176,224,140
tr 248,2,1024
tr 250,2,7168
mv 176,228,148
tr 252,2,13312
tr 254,2,19456
mv 168,228,144
tr 220,2,2048
tr 222,2,8192
mv 168,224,136
tr 216,2,14336
tr 218,2,20480
s_waitcnt lgkmcnt(10)
mv 168,232,152
v_add_u32 v3, 0xffffe000, v3
tr 228,3,0
mv 168,236,160
tr 230,3,6144
tr 224,3,12288
mv 176,236,164
tr 226,3,18432
tr 236,3,1024
mv 176,232,156
tr 238,3,7168
tr 232,3,13312
mvz 172,240,120
tr 234,3,19456
cv 184,184,185
s_waitcnt lgkmcnt(10)
mv 172,244,128
cv 185,186,187
cv 186,188,189
mvz 180,244,132
cv 187,190,191
aw 136,184
mv 180,240,124
aw 137,185
aw 138,186
mv 180,248,140
aw 139,187
cv 192,192,193
mv 180,252,148
cv 193,194,195
ar 28,140
mv 172,252,144
ar 29,141
ar 30,142
mv 172,248,136
s_add_u32 m0, s16, 52224
ar 31,143
)"
R"(dl 16,72,25,0
mv 172,220,152
cv 194,196,197
cv 195,198,199
s_waitcnt lgkmcnt(0)
mv 172,216,160
aw 140,192
aw 141,193
mv 180,216,164
aw 142,194
aw 143,195
mv 180,220,156
dl 16,76,25,1024
mvz 184,228,120
tr 244,3,2048
tr 246,3,8192
mv 184,224,128
tr 240,3,14336
tr 242,3,20480
mvz 192,224,132
v_add_u32 v4, 0xffffe000, v4
tr 252,4,0
mv 192,228,124
dl 16,80,25,2048
mv 192,236,140
tr 254,4,6144
tr 248,4,12288
mv 192,232,148
tr 250,4,18432
tr 216,4,1024
mv 184,232,144
tr 218,4,7168
tr 220,4,13312
mv 184,236,136
tr 222,4,19456
tr 224,4,2048
tr 226,4,8192
s_waitcnt lgkmcnt(10)
mv 184,244,152
tr 228,4,14336
tr 230,4,20480
mv 184,240,160
v_add_u32 v1, 0x2000, v1
v_mul_f32 v24, s26, v24
mv 192,240,164
v_mul_f32 v25, s26, v25
v_mul_f32 v26, s26, v26
mv 192,244,156
v_mul_f32 v27, s26, v27
cv 24,24,25
s_waitcnt lgkmcnt(7)
mvz 188,252,120
v_mul_f32 v28, s26, v28
v_mul_f32 v29, s26, v29
s_waitcnt vmcnt(6) lgkmcnt(0)
s_barrier
mv 188,248,128
rd 232,5,24576
rd 236,6,24576
mvz 196,248,132
rd 240,5,25600
rd 244,6,25600
mv 196,252,124
rd 248,5,26624
rd 252,6,26624
mv 196,216,140
v_mul_f32 v30, s26, v30
v_mul_f32 v31, s26, v31
mv 196,220,148
cv 25,26,27
cv 26,28,29
mv 188,220,144
rd 220,5,30720
cv 27,30,31
mv 188,216,136
s_add_u32 m0, s16, 0
s_add_u32 s22, s18, 344064
dn 11,48,22,0
mv 188,224,152
rd 216,6,30720
v_permlane32_swap_b32 v24, v26
mv 188,228,160
v_permlane32_swap_b32 v25, v27
v_add_u32 v28, s89, v19
mv 196,228,164
rd 228,5,31744
v_permlane16_swap_b32 v24, v26
mv 196,224,156
dn 11,52,22,1024
s_waitcnt lgkmcnt(3)
mvc 40,232,72,32
rd 224,6,31744
v_permlane16_swap_b32 v25, v27
mv 40,236,80
cv 168,168,169
ds_write_b128 v28, v[24:27] offset:20480
mvc 48,236,84,36
rd 236,5,32768
cv 169,170,171
mv 48,232,76
dn 11,56,22,2048
mv 48,240,92
rd 232,6,32768
ar 24,144
mv 48,244,100
ar 25,145
ar 26,146
mv 40,244,96
rd 244,5,36864
ar 27,147
mv 40,240,88
rd 240,6,36864
cv 170,172,173
mv 40,248,104
cv 171,174,175
aw 144,168
mv 40,252,112
aw 145,169
aw 146,170
mv 48,252,116
rd 252,5,37888
aw 147,171
mv 48,248,108
rd 248,6,37888
cv 176,176,177
s_waitcnt lgkmcnt(4)
mvc 44,220,72,32
cv 177,178,179
ar 28,148
mv 44,216,80
ar 29,149
ar 30,150
mvc 52,216,84,36
rd 216,5,38912
ar 31,151
mv 52,220,76
rd 220,6,38912
cv 178,180,181
mv 52,228,92
cv 179,182,183
aw 148,176
mv 52,224,100
aw 149,177
aw 150,178
mv 44,224,96
rd 224,5,43008
aw 151,179
mv 44,228,88
s_add_u32 m0, s16, 3072
rd 228,6,43008
dn 12,48,22,0
mv 44,236,104
v_add_u32 v2, 0x2000, v2
v_add_u32 v3, 0x2000, v3
mv 44,232,112
v_mul_f32 v24, s26, v24
v_mul_f32 v25, s26, v25
mv 52,232,116
rd 232,5,44032
v_mul_f32 v26, s26, v26
mv 52,236,108
dn 12,52,22,1024
s_waitcnt lgkmcnt(3)
mvc 56,244,72,32
rd 236,6,44032
v_mul_f32 v27, s26, v27
mv 56,240,80
cv 24,24,25
v_mul_f32 v28, s26, v28
mvc 64,240,84,36
rd 240,5,45056
v_mul_f32 v29, s26, v29
mv 64,244,76
dn 12,56,22,2048
mv 64,252,92
rd 244,6,45056
v_mul_f32 v30, s26, v30
mv 64,248,100
v_mul_f32 v31, s26, v31
cv 25,26,27
mv 56,248,96
cv 26,28,29
cv 27,30,31
mv 56,252,88
v_permlane32_swap_b32 v24, v26
v_permlane32_swap_b32 v25, v27
mv 56,216,104
v_add_u32 v28, s89, v19
v_permlane16_swap_b32 v24, v26
mv 56,220,112
s_waitcnt vmcnt(6) lgkmcnt(0)
s_barrier
mv 64,220,116
tr 248,1,40960
tr 250,1,47104
mv 64,216,108
tr 252,1,53248
tr 254,1,59392
mvc 60,224,72,32
tr 220,1,41984
tr 222,1,48128
mv 60,228,80
tr 216,1,54272
tr 218,1,60416
mvc 68,228,84,36
tr 228,1,43008
tr 230,1,49152
mv 68,224,76
tr 224,1,55296
tr 226,1,61440
mv 68,232,92
v_permlane16_swap_b32 v25, v27
cv 184,184,185
mv 68,236,100
ds_write_b128 v28, v[24:27] offset:17408
cv 185,186,187
mv 60,236,96
tr 236,2,40960
tr 238,2,47104
mv 60,232,88
s_add_u32 m0, s16, 24576
s_add_u32 s23, s18, 368640
dn 9,36,23,0
mv 60,240,104
tr 232,2,53248
tr 234,2,59392
mv 60,244,112
ar 24,152
ar 25,153
mv 68,244,116
tr 244,2,41984
tr 246,2,48128
mv 68,240,108
dn 9,40,23,1024
s_waitcnt lgkmcnt(6)
mvz 168,248,120
tr 240,2,54272
tr 242,2,60416
mv 168,252,128
ar 26,154
ar 27,155
mvz 176,252,132
tr 252,2,43008
tr 254,2,49152
mv 176,248,124
dn 9,44,23,2048
mv 176,220,140
tr 248,2,55296
tr 250,2,61440
mv 176,216,148
cv 186,188,189
cv 187,190,191
mv 168,216,144
tr 216,3,40960
tr 218,3,47104
mv 168,220,136
tr 220,3,53248
tr 222,3,59392
mv 168,228,152
aw 152,184
aw 153,185
mv 168,224,160
aw 154,186
aw 155,187
mv 176,224,164
tr 224,3,41984
tr 226,3,48128
mv 176,228,156
tr 228,3,54272
tr 230,3,60416
s_waitcnt lgkmcnt(8)
mvz 172,236,120
cv 192,192,193
cv 193,194,195
mv 172,232,128
ar 28,156
ar 29,157
mvz 180,232,132
ar 30,158
ar 31,159
mv 180,236,124
cv 194,196,197
cv 195,198,199
mv 180,244,140
aw 156,192
aw 157,193
mv 180,240,148
aw 158,194
aw 159,195
mv 172,240,144
tr 232,3,43008
tr 234,3,49152
mv 172,244,136
s_add_u32 m0, s16, 27648
tr 236,3,55296
dn 10,36,23,0
mv 172,252,152
tr 238,3,61440
v_add_u32 v4, 0x2000, v4
mv 172,248,160
tr 240,4,40960
tr 242,4,47104
mv 180,248,164
tr 244,4,53248
tr 246,4,59392
mv 180,252,156
dn 10,40,23,1024
s_waitcnt lgkmcnt(8)
mvz 184,216,120
tr 248,4,41984
tr 250,4,48128
mv 184,220,128
tr 252,4,54272
tr 254,4,60416
mvz 192,220,132
tr 220,4,43008
tr 222,4,49152
mv 192,216,124
dn 10,44,23,2048
mv 192,224,140
tr 216,4,55296
tr 218,4,61440
mv 192,228,148
v_max3_f32 v20, v40, v41, v42
v_max3_f32 v20, v20, v43, v48
mv 184,228,144
v_max3_f32 v20, v20, v49, v50
v_max3_f32 v20, v20, v51, v44
mv 184,224,136
v_max3_f32 v20, v20, v45, v46
v_max3_f32 v20, v20, v47, v52
s_waitcnt lgkmcnt(4)
mv 184,232,152
v_add_u32 v1, 0xffffe000, v1
v_max3_f32 v20, v20, v53, v54
mv 184,236,160
s_waitcnt vmcnt(6) lgkmcnt(0)
s_barrier
mv 192,236,164
tr 228,1,0
tr 230,1,6144
mv 192,232,156
v_max3_f32 v20, v20, v55, v56
v_max3_f32 v20, v20, v57, v58
mvz 188,240,120
v_max3_f32 v20, v20, v59, v64
v_max3_f32 v20, v20, v65, v66
mv 188,244,128
v_max3_f32 v20, v20, v67, v60
v_max3_f32 v20, v20, v61, v62
mvz 196,244,132
v_max3_f32 v20, v20, v63, v68
v_max3_f32 v20, v20, v69, v70
mv 196,240,124
v_max_f32 v20, v20, v71
v_add_u32 v2, 0xffffe000, v2
mv 196,248,140
v_cmp_lt_f32 vcc, s27, v20
s_cbranch_vccz Lc12
s_swappc_b64 s[30:31], s[28:29]
Lc12:
mv 196,252,148
tr 224,2,0
tr 226,2,6144
mv 188,252,144
v_exp_f32 v40, v40
mv 188,248,136
s_add_u32 m0, s16, 49152
s_add_u32 s24, s20, 1152
dl 15,72,24,0
mv 188,220,152
v_exp_f32 v41, v41
mv 188,216,160
v_exp_f32 v42, v42
mv 196,216,164
v_exp_f32 v43, v43
v_exp_f32 v44, v44
dl 15,76,24,1024
v_exp_f32 v45, v45
v_exp_f32 v46, v46
v_exp_f32 v47, v47
cv 40,40,41
mv 196,220,156
cv 41,42,43
cv 42,44,45
cv 43,46,47
v_exp_f32 v48, v48
dl 15,80,24,2048
v_exp_f32 v49, v49
v_exp_f32 v50, v50
v_exp_f32 v51, v51
v_exp_f32 v52, v52
v_exp_f32 v53, v53
v_exp_f32 v54, v54
v_exp_f32 v55, v55
v_add_u32 v3, 0xffffe000, v3
cv 48,48,49
tr 236,3,0
tr 238,3,6144
v_mfma_f32_16x16x32_bf16 a[244:247], a[240:243], v[40:43], a[244:247]
cv 49,50,51
cv 50,52,53
cv 51,54,55
v_add_u32 v4, 0xffffe000, v4
tr 232,4,0
v_mfma_f32_16x16x32_bf16 a[248:251], a[240:243], v[48:51], a[248:251]
tr 234,4,6144
tr 244,1,1024
s_waitcnt lgkmcnt(5)
ma 0,228,40
tr 246,1,7168
tr 240,2,1024
ma 8,228,48
tr 242,2,7168
tr 252,3,1024
ma 4,224,40
tr 254,3,7168
tr 248,4,1024
ma 12,224,48
tr 250,4,7168
tr 216,1,2048
tr 218,1,8192
s_waitcnt lgkmcnt(12)
ma 16,236,40
tr 220,2,2048
tr 222,2,8192
ma 24,236,48
tr 228,3,2048
tr 230,3,8192
tr 224,4,2048
s_waitcnt lgkmcnt(13)
ma 20,232,40
tr 226,4,8192
v_mul_f32 v24, s26, v24
ma 28,232,48
v_mul_f32 v25, s26, v25
v_mul_f32 v26, s26, v26
ma 32,244,40
v_mul_f32 v27, s26, v27
cv 24,24,25
ma 40,244,48
v_mul_f32 v28, s26, v28
v_mul_f32 v29, s26, v29
s_waitcnt lgkmcnt(6)
ma 36,240,40
v_mul_f32 v30, s26, v30
v_mul_f32 v31, s26, v31
ma 44,240,48
cv 25,26,27
cv 26,28,29
ma 48,252,40
cv 27,30,31
v_permlane32_swap_b32 v24, v26
ma 56,252,48
v_permlane32_swap_b32 v25, v27
v_add_u32 v28, s89, v19
ma 52,248,40
v_permlane16_swap_b32 v24, v26
v_permlane16_swap_b32 v25, v27
ma 60,248,48
ds_write_b128 v28, v[24:27] offset:21504
ar 24,160
ma 64,216,40
ar 25,161
ar 26,162
ma 72,216,48
ar 27,163
v_mul_f32 v24, s26, v24
s_waitcnt lgkmcnt(1)
ma 68,220,40
v_mul_f32 v25, s26, v25
ar 28,164
ar 29,165
ar 30,166
ar 31,167
v_mul_f32 v26, s26, v26
v_mul_f32 v27, s26, v27
cv 24,24,25
ma 76,220,48
s_add_u32 m0, s16, 52224
v_mul_f32 v28, s26, v28
dl 16,72,24,0
v_mul_f32 v29, s26, v29
v_mul_f32 v30, s26, v30
v_mul_f32 v31, s26, v31
cv 25,26,27
cv 26,28,29
cv 27,30,31
tr 236,1,12288
v_permlane32_swap_b32 v24, v26
v_permlane32_swap_b32 v25, v27
tr 238,1,18432
ma 80,228,40
v_add_u32 v28, s89, v19
v_permlane16_swap_b32 v24, v26
dl 16,76,24,1024
v_permlane16_swap_b32 v25, v27
ds_write_b128 v28, v[24:27] offset:32768
ar 24,168
ar 25,169
ma 88,228,48
ar 26,170
ar 27,171
v_mul_f32 v24, s26, v24
v_mul_f32 v25, s26, v25
ar 28,172
ar 29,173
ar 30,174
ar 31,175
dl 16,80,24,2048
v_mul_f32 v26, s26, v26
v_mul_f32 v27, s26, v27
cv 24,24,25
v_mul_f32 v28, s26, v28
v_mul_f32 v29, s26, v29
v_mul_f32 v30, s26, v30
v_mul_f32 v31, s26, v31
cv 25,26,27
cv 26,28,29
cv 27,30,31
tr 232,2,12288
tr 234,2,18432
ma 84,224,40
v_permlane32_swap_b32 v24, v26
v_permlane32_swap_b32 v25, v27
v_exp_f32 v56, v56
v_exp_f32 v57, v57
v_exp_f32 v58, v58
v_exp_f32 v59, v59
v_exp_f32 v60, v60
v_exp_f32 v61, v61
v_exp_f32 v62, v62
v_exp_f32 v63, v63
v_add_u32 v28, s89, v19
v_permlane16_swap_b32 v24, v26
v_permlane16_swap_b32 v25, v27
cv 56,56,57
ma 92,224,48
ds_write_b128 v28, v[24:27] offset:36864
cv 57,58,59
ar 24,176
ar 25,177
cv 58,60,61
cv 59,62,63
v_exp_f32 v64, v64
v_exp_f32 v65, v65
ar 26,178
ar 27,179
v_mul_f32 v24, s26, v24
v_mul_f32 v25, s26, v25
v_exp_f32 v66, v66
v_exp_f32 v67, v67
ar 28,180
ar 29,181
ar 30,182
ar 31,183
v_mul_f32 v26, s26, v26
v_mul_f32 v27, s26, v27
cv 24,24,25
v_exp_f32 v68, v68
v_exp_f32 v69, v69
v_exp_f32 v70, v70
v_exp_f32 v71, v71
v_mul_f32 v28, s26, v28
v_mul_f32 v29, s26, v29
v_mul_f32 v30, s26, v30
v_mul_f32 v31, s26, v31
cv 25,26,27
cv 64,64,65
tr 244,3,12288
tr 246,3,18432
v_mfma_f32_16x16x32_bf16 a[244:247], a[240:243], v[56:59], a[244:247]
cv 26,28,29
cv 27,30,31
cv 65,66,67
cv 66,68,69
cv 67,70,71
v_permlane32_swap_b32 v24, v26
v_permlane32_swap_b32 v25, v27
v_mfma_f32_16x16x32_bf16 a[248:251], a[240:243], v[64:67], a[248:251]
v_add_u32 v28, s89, v19
v_permlane16_swap_b32 v24, v26
v_permlane16_swap_b32 v25, v27
ds_write_b128 v28, v[24:27] offset:33792
ar 24,184
ar 25,185
ar 26,186
ar 27,187
v_mul_f32 v24, s26, v24
v_mul_f32 v25, s26, v25
tr 240,4,12288
tr 242,4,18432
s_waitcnt lgkmcnt(4)
ma 0,236,56
ar 28,188
ar 29,189
ar 30,190
ar 31,191
v_mul_f32 v26, s26, v26
v_mul_f32 v27, s26, v27
cv 24,24,25
v_mul_f32 v28, s26, v28
v_mul_f32 v29, s26, v29
v_mul_f32 v30, s26, v30
v_mul_f32 v31, s26, v31
cv 25,26,27
cv 26,28,29
cv 27,30,31
ma 8,236,64
v_permlane32_swap_b32 v24, v26
v_permlane32_swap_b32 v25, v27
v_add_u32 v28, s89, v19
v_permlane16_swap_b32 v24, v26
v_permlane16_swap_b32 v25, v27
ds_write_b128 v28, v[24:27] offset:37888
tr 252,1,13312
tr 254,1,19456
tr 248,2,13312
tr 250,2,19456
tr 216,3,13312
tr 218,3,19456
tr 220,4,13312
tr 222,4,19456
tr 228,1,14336
tr 230,1,20480
tr 224,2,14336
tr 226,2,20480
tr 236,3,14336
tr 238,3,20480
ma 4,232,56
v_add_u32 v1, 0x2000, v1
cv 168,168,169
ma 12,232,64
tr 232,4,14336
tr 234,4,20480
s_waitcnt lgkmcnt(15)
ma 16,244,56
cv 169,170,171
cv 170,172,173
ma 24,244,64
cv 171,174,175
aw 160,168
ma 20,240,56
aw 161,169
aw 162,170
ma 28,240,64
aw 163,171
cv 176,176,177
s_waitcnt vmcnt(6) lgkmcnt(0)
s_barrier
ma 32,252,56
rd 244,5,24576
rd 240,6,24576
ma 40,252,64
rd 252,5,25600
cv 177,178,179
ma 36,248,56
cv 178,180,181
cv 179,182,183
ma 44,248,64
rd 248,6,25600
aw 164,176
ma 48,216,56
aw 165,177
aw 166,178
ma 56,216,64
rd 216,5,26624
aw 167,179
ma 52,220,56
v_add_u32 v2, 0x2000, v2
v_add_u32 v3, 0x2000, v3
ma 60,220,64
rd 220,6,26624
cv 184,184,185
ma 64,228,56
cv 185,186,187
cv 186,188,189
ma 72,228,64
rd 228,5,30720
cv 187,190,191
ma 68,224,56
aw 168,184
aw 169,185
ma 76,224,64
s_add_u32 m0, s16, 0
s_add_u32 s25, s18, 368640
dn 11,48,25,0
ma 80,236,56
rd 224,6,30720
aw 170,186
ma 88,236,64
rd 236,5,31744
aw 171,187
ma 84,232,56
cv 192,192,193
cv 193,194,195
ma 92,232,64
dn 11,52,25,1024
s_waitcnt lgkmcnt(2)
mvc 40,244,72,32
rd 232,6,31744
cv 194,196,197
mv 40,240,80
cv 195,198,199
aw 172,192
mvc 48,240,84,36
rd 240,5,32768
aw 173,193
mv 48,244,76
dn 11,56,25,2048
mv 48,252,92
rd 244,6,32768
aw 174,194
mv 48,248,100
aw 175,195
v_add_u32 v4, 0x2000, v4
mv 40,248,96
rd 248,5,36864
rd 212,19,61440
mv 40,252,88
rd 252,6,36864
mv 40,216,104
mv 40,220,112
mv 48,220,116
rd 220,5,37888
mv 48,216,108
rd 216,6,37888
mvc 44,228,72,32
s_add_u32 m0, s16, 3072
s_nop 1
v_max3_f32 v20, v40, v41, v42
dn 12,48,25,0
s_waitcnt lgkmcnt(3)
mv 44,224,80
v_max3_f32 v20, v20, v43, v48
v_max3_f32 v20, v20, v49, v50
mvc 52,224,84,36
rd 224,5,38912
mv 52,228,76
rd 228,6,38912
mv 52,236,92
dn 12,52,25,1024
mv 52,232,100
mv 44,232,96
rd 232,5,43008
mv 44,236,88
s_add_u32 s22, s18, 393216
rd 236,6,43008
mv 44,240,104
dn 12,56,25,2048
mv 44,244,112
mv 52,244,116
rd 244,5,44032
mv 52,240,108
rd 240,6,44032
mvc 56,248,72,32
s_nop 2
v_max3_f32 v20, v20, v51, v44
v_max3_f32 v20, v20, v45, v46
s_waitcnt lgkmcnt(3)
mv 56,252,80
v_max3_f32 v20, v20, v47, v52
mvc 64,252,84,36
rd 252,5,45056
v_max3_f32 v20, v20, v53, v54
mv 64,248,76
rd 248,6,45056
v_add_u32 v5, 0x8000, v5
mv 64,220,92
v_add_u32 v6, 0x8000, v6
s_add_u32 m0, s16, 73728
mv 64,216,100
dn 9,36,22,0
mv 56,216,96
mv 56,220,88
s_waitcnt vmcnt(7) lgkmcnt(0)
s_barrier
mv 56,224,104
tr 216,1,40960
tr 218,1,47104
dn 9,40,22,1024
mv 56,228,112
tr 220,1,53248
tr 222,1,59392
mv 64,228,116
tr 228,1,41984
tr 230,1,48128
mv 64,224,108
tr 224,1,54272
tr 226,1,60416
mvc 60,232,72,32
dn 9,44,22,2048
mv 60,236,80
v_max3_f32 v20, v20, v55, v56
v_max3_f32 v20, v20, v57, v58
mvc 68,236,84,36
tr 236,1,43008
tr 238,1,49152
mv 68,232,76
tr 232,1,55296
tr 234,1,61440
mv 68,244,92
v_add_u32 v1, 0xffffe000, v1
v_max3_f32 v20, v20, v59, v64
mv 68,240,100
v_max3_f32 v20, v20, v65, v66
rd 72,19,0
mv 60,240,96
tr 240,2,40960
tr 242,2,47104
mv 60,244,88
s_add_u32 s23, s20, 245760
tr 244,2,53248
mv 60,252,104
tr 246,2,59392
rd 76,19,4096
mv 60,248,112
s_add_u32 m0, s16, 76800
rd 80,19,8192
dn 10,36,22,0
mv 68,248,116
tr 248,2,41984
tr 250,2,48128
mv 68,252,108
tr 252,2,54272
tr 254,2,60416
s_waitcnt lgkmcnt(10)
mvz 168,216,120
v_max3_f32 v20, v20, v67, v60
v_max3_f32 v20, v20, v61, v62
mv 168,220,128
dn 10,40,22,1024
mvz 176,220,132
tr 220,2,43008
tr 222,2,49152
mv 176,216,124
tr 216,2,55296
tr 218,2,61440
mv 176,228,140
v_max3_f32 v20, v20, v63, v68
v_max3_f32 v20, v20, v69, v70
mv 176,224,148
dn 10,44,22,2048
mv 168,224,144
tr 224,3,40960
tr 226,3,47104
mv 168,228,136
tr 228,3,53248
tr 230,3,59392
mv 168,236,152
v_max_f32 v20, v20, v71
v_add_u32 v2, 0xffffe000, v2
mv 168,232,160
v_cmp_lt_f32 vcc, s27, v20
s_cbranch_vccz Lc13
s_swappc_b64 s[30:31], s[28:29]
Lc13:
mv 176,232,164
tr 232,3,41984
tr 234,3,48128
mv 176,236,156
tr 236,3,54272
tr 238,3,60416
s_waitcnt lgkmcnt(8)
mvz 172,240,120
v_exp_f32 v40, v40
mv 172,244,128
v_exp_f32 v41, v41
s_add_u32 m0, s16, 24576
mvz 180,244,132
dl 15,72,23,0
mv 180,240,124
tr 244,3,43008
tr 246,3,49152
mv 180,248,140
tr 240,3,55296
tr 242,3,61440
mv 180,252,148
v_exp_f32 v42, v42
mv 172,252,144
dl 15,76,23,1024
mv 172,248,136
tr 252,4,40960
tr 254,4,47104
mv 172,220,152
tr 248,4,53248
tr 250,4,59392
mv 172,216,160
v_exp_f32 v43, v43
mv 180,216,164
dl 15,80,23,2048
mv 180,220,156
tr 216,4,41984
tr 218,4,48128
s_waitcnt lgkmcnt(6)
mvz 184,224,120
tr 220,4,54272
tr 222,4,60416
mv 184,228,128
v_exp_f32 v44, v44
mvz 192,228,132
tr 228,4,43008
tr 230,4,49152
mv 192,224,124
tr 224,4,55296
tr 226,4,61440
mv 192,232,140
v_exp_f32 v45, v45
mv 192,236,148
v_exp_f32 v46, v46
mv 184,236,144
v_exp_f32 v47, v47
s_add_u32 m0, s16, 27648
mv 184,232,136
dl 16,72,23,0
mv 184,244,152
s_waitcnt vmcnt(10) lgkmcnt(0)
s_barrier
mv 184,240,160
tr 236,1,0
tr 238,1,6144
mv 192,240,164
tr 232,2,0
tr 234,2,6144
mv 192,244,156
dl 16,76,23,1024
mvz 188,252,120
cv 40,40,41
cv 41,42,43
mv 188,248,128
cv 42,44,45
cv 43,46,47
mvz 196,248,132
v_exp_f32 v48, v48
mv 196,252,124
dl 16,80,23,2048
mv 196,216,140
v_exp_f32 v49, v49
mv 196,220,148
v_exp_f32 v50, v50
mv 188,220,144
v_exp_f32 v51, v51
mv 188,216,136
s_add_u32 s24, s18, 393216
v_exp_f32 v52, v52
mv 188,228,152
v_exp_f32 v53, v53
mv 188,224,160
v_exp_f32 v54, v54
mv 196,224,164
v_exp_f32 v55, v55
mv 196,228,156
v_add_u32 v3, 0xffffe000, v3
cv 48,48,49
s_add_u32 m0, s16, 49152
tr 240,3,0
dn 11,48,24,0
tr 242,3,6144
v_mfma_f32_16x16x32_bf16 a[244:247], a[240:243], v[40:43], a[244:247]
cv 49,50,51
cv 50,52,53
cv 51,54,55
v_add_u32 v4, 0xffffe000, v4
tr 244,4,0
v_mfma_f32_16x16x32_bf16 a[248:251], a[240:243], v[48:51], a[248:251]
tr 246,4,6144
tr 248,1,1024
s_waitcnt lgkmcnt(5)
ma 0,236,40
dn 11,52,24,1024
ma 8,236,48
tr 250,1,7168
tr 252,2,1024
ma 4,232,40
tr 254,2,7168
tr 220,3,1024
ma 12,232,48
tr 222,3,7168
tr 216,4,1024
tr 218,4,7168
s_waitcnt lgkmcnt(10)
ma 16,240,40
dn 11,56,24,2048
ma 24,240,48
tr 224,1,2048
tr 226,1,8192
tr 228,2,2048
s_waitcnt lgkmcnt(10)
ma 20,244,40
tr 230,2,8192
tr 236,3,2048
ma 28,244,48
tr 238,3,8192
tr 232,4,2048
s_waitcnt lgkmcnt(10)
ma 32,248,40
tr 234,4,8192
tr 240,1,12288
ma 40,248,48
tr 242,1,18432
tr 244,2,12288
ma 36,252,40
tr 246,2,18432
v_exp_f32 v56, v56
ma 44,252,48
v_exp_f32 v57, v57
s_waitcnt lgkmcnt(9)
ma 48,220,40
v_exp_f32 v58, v58
ma 56,220,48
v_exp_f32 v59, v59
ma 52,216,40
v_exp_f32 v60, v60
ma 60,216,48
v_exp_f32 v61, v61
ma 64,224,40
v_exp_f32 v62, v62
ma 72,224,48
v_exp_f32 v63, v63
s_waitcnt lgkmcnt(0)
ma 68,228,40
cv 56,56,57
cv 57,58,59
ma 76,228,48
s_add_u32 m0, s16, 52224
cv 58,60,61
dn 12,48,24,0
ma 80,236,40
cv 59,62,63
v_exp_f32 v64, v64
ma 88,236,48
v_exp_f32 v65, v65
ma 84,232,40
v_exp_f32 v66, v66
ma 92,232,48
dn 12,52,24,1024
v_exp_f32 v67, v67
v_exp_f32 v68, v68
v_exp_f32 v69, v69
v_exp_f32 v70, v70
v_exp_f32 v71, v71
cv 64,64,65
tr 248,3,12288
tr 250,3,18432
v_mfma_f32_16x16x32_bf16 a[244:247], a[240:243], v[56:59], a[244:247]
dn 12,56,24,2048
cv 65,66,67
cv 66,68,69
cv 67,70,71
tr 252,4,12288
tr 254,4,18432
v_mfma_f32_16x16x32_bf16 a[248:251], a[240:243], v[64:67], a[248:251]
tr 220,1,13312
tr 222,1,19456
ma 0,240,56
tr 216,2,13312
tr 218,2,19456
ma 8,240,64
tr 224,3,13312
tr 226,3,19456
ma 4,244,56
tr 228,4,13312
tr 230,4,19456
ma 12,244,64
rd 84,19,12288
rd 88,19,16384
s_waitcnt lgkmcnt(12)
ma 16,248,56
rd 92,19,20480
rd 96,19,24576
ma 24,248,64
rd 100,19,28672
rd 104,19,32768
s_waitcnt lgkmcnt(12)
ma 20,252,56
rd 108,19,36864
rd 112,19,40960
ma 28,252,64
rd 116,19,45056
tr 236,1,14336
ma 32,220,56
tr 238,1,20480
tr 232,2,14336
ma 40,220,64
tr 234,2,20480
tr 240,3,14336
tr 242,3,20480
tr 244,4,14336
tr 246,4,20480
s_waitcnt lgkmcnt(13)
ma 36,216,56
cv 176,176,177
cv 177,178,179
ma 44,216,64
cv 178,180,181
cv 179,182,183
ma 48,224,56
aw 180,176
aw 181,177
ma 56,224,64
aw 182,178
aw 183,179
s_waitcnt vmcnt(12) lgkmcnt(0)
s_barrier
ma 52,228,56
rd 248,5,40960
rd 252,6,40960
ma 60,228,64
rd 220,5,41984
rd 216,6,41984
ma 64,236,56
rd 224,5,43008
rd 228,6,43008
ma 72,236,64
rd 236,5,47104
cv 168,168,169
ma 68,232,56
cv 169,170,171
ar 208,244
ma 76,232,64
s_add_u32 m0, s16, 0
s_add_u32 s25, s20, 245888
dl 15,72,25,0
ma 80,240,56
rd 232,6,47104
cv 170,172,173
ma 88,240,64
rd 240,5,48128
cv 171,174,175
ma 84,244,56
v_rcp_f32 v208, v208
ma 92,244,64
dl 15,76,25,1024
s_waitcnt lgkmcnt(2)
mvz 40,248,72
rd 244,6,48128
aw 176,168
mv 40,252,80
aw 177,169
aw 178,170
mvz 48,252,84
rd 252,5,49152
aw 179,171
mv 48,248,76
dl 15,80,25,2048
mv 48,220,92
rd 248,6,49152
ar 200,0
mv 48,216,100
ar 201,1
ar 202,2
mv 40,216,96
rd 216,5,53248
ar 203,3
mv 40,220,88
rd 220,6,53248
ar 204,4
mv 40,224,104
ar 205,5
ar 206,6
mv 40,228,112
ar 207,7
v_mul_f32 v200, v208, v200
mv 48,228,116
rd 228,5,54272
v_mul_f32 v201, v208, v201
mv 48,224,108
rd 224,6,54272
v_mul_f32 v202, v208, v202
mvz 44,236,72
v_mul_f32 v203, v208, v203
v_mul_f32 v204, v208, v204
s_waitcnt lgkmcnt(3)
mv 44,232,80
v_mul_f32 v205, v208, v205
v_mul_f32 v206, v208, v206
mvz 52,232,84
rd 232,5,55296
v_mul_f32 v207, v208, v207
mv 52,236,76
rd 236,6,55296
ar 209,248
mv 52,240,92
cv 120,200,201
cv 121,202,203
mv 52,244,100
cv 122,204,205
cv 123,206,207
mv 44,244,96
rd 244,5,59392
v_rcp_f32 v209, v209
mv 44,240,88
s_add_u32 m0, s16, 3072
rd 240,6,59392
dl 16,72,25,0
mv 44,252,104
ar 24,8
ar 25,9
mv 44,248,112
ar 26,10
ar 27,11
mv 52,248,116
rd 248,5,60416
ar 28,12
mv 52,252,108
dl 16,76,25,1024
mvz 56,216,72
rd 252,6,60416
ar 29,13
s_waitcnt lgkmcnt(3)
mv 56,220,80
ar 30,14
ar 31,15
mvz 64,220,84
rd 220,5,61440
v_mul_f32 v24, v209, v24
mv 64,216,76
dl 16,80,25,2048
mv 64,228,92
rd 216,6,61440
v_mul_f32 v25, v209, v25
mv 64,224,100
v_mul_f32 v26, v209, v26
v_mul_f32 v27, v209, v27
mv 56,224,96
v_mul_f32 v28, v209, v28
v_mul_f32 v29, v209, v29
mv 56,228,88
v_mul_f32 v30, v209, v30
v_mul_f32 v31, v209, v31
mv 56,232,104
cv 124,24,25
cv 125,26,27
mv 56,236,112
s_waitcnt vmcnt(12) lgkmcnt(0)
s_barrier
mv 64,236,116
tr 224,1,24576
tr 226,1,30720
mv 64,232,108
tr 228,2,24576
tr 230,2,30720
mvz 60,244,72
cv 126,28,29
cv 127,30,31
mv 60,240,80
tr 236,3,24576
tr 238,3,30720
mvz 68,240,84
ar 242,252
ar 243,253
mv 68,244,76
v_mov_b32 v244, 0
v_mov_b32 v245, 0
mv 68,248,92
v_mov_b32 v240, 0
v_mov_b32 v241, 0
mv 68,252,100
cv 192,192,193
cv 193,194,195
mv 60,252,96
cv 194,196,197
cv 195,198,199
mv 60,248,88
s_add_u32 m0, s16, 73728
s_add_u32 s22, s20, 246016
dl 15,72,22,0
mv 60,220,104
aw 188,192
aw 189,193
mv 60,216,112
aw 190,194
aw 191,195
mv 68,216,116
tr 232,4,24576
tr 234,4,30720
mv 68,220,108
dl 15,76,22,1024
v_mfma_f32_16x16x32_bf16 v[176:179], v[242:245], a[196:199], 0
cv 184,184,185
cv 185,186,187
v_mfma_f32_16x16x32_bf16 v[168:171], v[242:245], a[192:195], 0
cv 186,188,189
cv 187,190,191
s_waitcnt lgkmcnt(2)
mv 168,224,120
aw 184,184
aw 185,185
mv 176,224,124
dl 15,80,22,2048
v_mfma_f32_16x16x32_bf16 v[180:183], v[240:243], a[196:199], 0
aw 186,186
aw 187,187
v_mfma_f32_16x16x32_bf16 v[172:175], v[240:243], a[192:195], 0
tr 252,1,36864
tr 254,1,43008
mv 172,228,120
tr 248,2,36864
tr 250,2,43008
mv 180,228,124
tr 216,3,36864
tr 218,3,43008
v_mfma_f32_16x16x32_bf16 v[192:195], v[242:245], a[204:207], 0
ar 200,16
ar 201,17
v_mfma_f32_16x16x32_bf16 v[184:187], v[242:245], a[200:203], 0
ar 202,18
ar 203,19
mv 184,236,120
ar 204,20
ar 205,21
mv 192,236,124
ar 206,22
ar 207,23
v_mfma_f32_16x16x32_bf16 v[196:199], v[240:243], a[204:207], 0
v_mul_f32 v200, v208, v200
v_mul_f32 v201, v208, v201
v_mfma_f32_16x16x32_bf16 v[188:191], v[240:243], a[200:203], 0
v_mul_f32 v202, v208, v202
v_mul_f32 v203, v208, v203
s_waitcnt lgkmcnt(0)
mv 188,232,120
v_mul_f32 v204, v208, v204
v_mul_f32 v205, v208, v205
mv 196,232,124
v_mul_f32 v206, v208, v206
v_mul_f32 v207, v208, v207
cv 128,200,201
cv 129,202,203
cv 130,204,205
cv 131,206,207
ar 24,24
ar 25,25
ar 26,26
ar 27,27
ar 28,28
ar 29,29
ar 30,30
ar 31,31
tr 220,4,36864
tr 222,4,43008
mv 168,252,128
v_mul_f32 v24, v209, v24
v_mul_f32 v25, v209, v25
v_mul_f32 v26, v209, v26
v_mul_f32 v27, v209, v27
v_mul_f32 v28, v209, v28
v_mul_f32 v29, v209, v29
v_mul_f32 v30, v209, v30
v_mul_f32 v31, v209, v31
cv 132,24,25
cv 133,26,27
cv 134,28,29
cv 135,30,31
tr 224,1,25600
tr 226,1,31744
mv 176,252,132
tr 228,2,25600
tr 230,2,31744
mv 172,248,128
tr 236,3,25600
tr 238,3,31744
mv 180,248,132
ar 200,32
ar 201,33
mv 184,216,128
ar 202,34
ar 203,35
mv 192,216,132
ar 204,36
ar 205,37
s_waitcnt lgkmcnt(6)
mv 188,220,128
ar 206,38
ar 207,39
mv 196,220,132
s_add_u32 m0, s16, 76800
v_mul_f32 v200, v208, v200
dl 16,72,22,0
v_mul_f32 v201, v208, v201
v_mul_f32 v202, v208, v202
v_mul_f32 v203, v208, v203
v_mul_f32 v204, v208, v204
v_mul_f32 v205, v208, v205
v_mul_f32 v206, v208, v206
v_mul_f32 v207, v208, v207
cv 136,200,201
cv 137,202,203
cv 138,204,205
cv 139,206,207
ar 24,40
ar 25,41
ar 26,42
dl 16,76,22,1024
ar 27,43
ar 28,44
ar 29,45
ar 30,46
ar 31,47
tr 232,4,25600
tr 234,4,31744
s_waitcnt lgkmcnt(2)
mv 168,224,136
v_mul_f32 v24, v209, v24
v_mul_f32 v25, v209, v25
v_mul_f32 v26, v209, v26
v_mul_f32 v27, v209, v27
dl 16,80,22,2048
v_mul_f32 v28, v209, v28
v_mul_f32 v29, v209, v29
v_mul_f32 v30, v209, v30
v_mul_f32 v31, v209, v31
cv 140,24,25
cv 141,26,27
cv 142,28,29
cv 143,30,31
tr 252,1,37888
tr 254,1,44032
mv 176,224,140
tr 248,2,37888
tr 250,2,44032
mv 172,228,136
tr 216,3,37888
tr 218,3,44032
mv 180,228,140
ar 200,48
ar 201,49
mv 184,236,136
ar 202,50
ar 203,51
mv 192,236,140
ar 204,52
ar 205,53
s_waitcnt lgkmcnt(6)
mv 188,232,136
ar 206,54
ar 207,55
mv 196,232,140
v_mul_f32 v200, v208, v200
v_mul_f32 v201, v208, v201
v_mul_f32 v202, v208, v202
v_mul_f32 v203, v208, v203
v_mul_f32 v204, v208, v204
v_mul_f32 v205, v208, v205
v_mul_f32 v206, v208, v206
v_mul_f32 v207, v208, v207
cv 144,200,201
cv 145,202,203
cv 146,204,205
cv 147,206,207
ar 24,56
ar 25,57
ar 26,58
ar 27,59
ar 28,60
ar 29,61
ar 30,62
ar 31,63
tr 220,4,37888
tr 222,4,44032
s_waitcnt lgkmcnt(2)
mv 168,252,144
v_mul_f32 v24, v209, v24
v_mul_f32 v25, v209, v25
v_mul_f32 v26, v209, v26
v_mul_f32 v27, v209, v27
v_mul_f32 v28, v209, v28
v_mul_f32 v29, v209, v29
v_mul_f32 v30, v209, v30
v_mul_f32 v31, v209, v31
cv 148,24,25
cv 149,26,27
cv 150,28,29
cv 151,30,31
tr 224,1,26624
tr 226,1,32768
mv 176,252,148
tr 228,2,26624
tr 230,2,32768
mv 172,248,144
tr 236,3,26624
tr 238,3,32768
mv 180,248,148
ar 200,64
ar 201,65
mv 184,216,144
ar 202,66
ar 203,67
mv 192,216,148
ar 204,68
ar 205,69
s_waitcnt lgkmcnt(6)
mv 188,220,144
ar 206,70
ar 207,71
mv 196,220,148
v_mul_f32 v200, v208, v200
v_mul_f32 v201, v208, v201
v_mul_f32 v202, v208, v202
v_mul_f32 v203, v208, v203
)"
R"(v_mul_f32 v204, v208, v204
v_mul_f32 v205, v208, v205
v_mul_f32 v206, v208, v206
v_mul_f32 v207, v208, v207
cv 152,200,201
cv 153,202,203
cv 154,204,205
cv 155,206,207
ar 24,72
ar 25,73
ar 26,74
ar 27,75
ar 28,76
ar 29,77
ar 30,78
ar 31,79
tr 232,4,26624
tr 234,4,32768
s_waitcnt lgkmcnt(2)
mv 168,224,152
v_mul_f32 v24, v209, v24
v_mul_f32 v25, v209, v25
v_mul_f32 v26, v209, v26
v_mul_f32 v27, v209, v27
v_mul_f32 v28, v209, v28
v_mul_f32 v29, v209, v29
v_mul_f32 v30, v209, v30
v_mul_f32 v31, v209, v31
cv 156,24,25
cv 157,26,27
cv 158,28,29
cv 159,30,31
tr 252,1,38912
tr 254,1,45056
mv 176,224,156
tr 248,2,38912
tr 250,2,45056
tr 216,3,38912
tr 218,3,45056
tr 220,4,38912
tr 222,4,45056
mv 172,228,152
ar 200,80
ar 201,81
mv 180,228,156
ar 202,82
ar 203,83
mv 184,236,152
ar 204,84
ar 205,85
mv 192,236,156
ar 206,86
ar 207,87
s_waitcnt lgkmcnt(6)
mv 188,232,152
s_waitcnt vmcnt(12) lgkmcnt(0)
s_barrier
mv 196,232,156
v_mul_f32 v200, v208, v200
v_mul_f32 v201, v208, v201
v_mul_f32 v202, v208, v202
v_mul_f32 v203, v208, v203
v_mul_f32 v204, v208, v204
v_mul_f32 v205, v208, v205
v_mul_f32 v206, v208, v206
v_mul_f32 v207, v208, v207
cv 160,200,201
cv 161,202,203
cv 162,204,205
cv 163,206,207
ar 24,88
ar 25,89
ar 26,90
ar 27,91
ar 28,92
ar 29,93
ar 30,94
ar 31,95
mv 168,252,160
v_mul_f32 v24, v209, v24
v_mul_f32 v25, v209, v25
v_mul_f32 v26, v209, v26
v_mul_f32 v27, v209, v27
v_mul_f32 v28, v209, v28
v_mul_f32 v29, v209, v29
v_mul_f32 v30, v209, v30
v_mul_f32 v31, v209, v31
cv 164,24,25
cv 165,26,27
cv 166,28,29
cv 167,30,31
v_max3_f32 v20, v40, v41, v42
v_max3_f32 v20, v20, v43, v44
mv 176,252,164
v_max3_f32 v20, v20, v45, v46
v_max3_f32 v20, v20, v47, v56
mv 172,248,160
v_max3_f32 v20, v20, v57, v58
v_max3_f32 v21, v48, v49, v50
mv 180,248,164
s_add_u32 m0, s16, 24576
s_add_u32 s23, s18, 417792
dn 9,36,23,0
v_max3_f32 v20, v20, v59, v60
v_max3_f32 v21, v21, v51, v52
v_max3_f32 v20, v20, v61, v62
v_add_u32 v1, 0x2000, v1
v_max3_f32 v21, v21, v53, v54
v_max_f32 v20, v20, v63
tr 224,1,40960
tr 226,1,47104
mv 184,216,160
v_max3_f32 v21, v21, v55, v64
v_mov_b32 v32, v20
v_max3_f32 v21, v21, v65, v66
v_max3_f32 v21, v21, v67, v68
dn 9,40,23,1024
v_max3_f32 v21, v21, v69, v70
v_permlane32_swap_b32 v20, v32
v_max_f32 v21, v21, v71
v_max_f32 v20, v20, v32
mv 192,216,164
v_mov_b32 v32, v20
v_mov_b32 v36, v21
v_add_u32 v2, 0x2000, v2
v_permlane16_swap_b32 v20, v32
v_permlane32_swap_b32 v21, v36
v_max_f32 v20, v20, v32
v_max_f32 v21, v21, v36
tr 228,2,40960
dn 9,44,23,2048
tr 230,2,47104
mv 188,220,160
v_mov_b32 v36, v21
v_sub_f32 v40, v40, v20
v_sub_f32 v41, v41, v20
v_sub_f32 v42, v42, v20
v_sub_f32 v43, v43, v20
v_exp_f32 v40, v40
v_exp_f32 v41, v41
v_sub_f32 v44, v44, v20
v_sub_f32 v45, v45, v20
v_sub_f32 v46, v46, v20
v_sub_f32 v47, v47, v20
v_exp_f32 v42, v42
v_exp_f32 v43, v43
v_permlane16_swap_b32 v21, v36
v_exp_f32 v44, v44
v_exp_f32 v45, v45
v_exp_f32 v46, v46
v_exp_f32 v47, v47
v_max_f32 v21, v21, v36
cv 40,40,41
mv 196,220,164
v_sub_f32 v48, v48, v21
v_sub_f32 v49, v49, v21
cv 41,42,43
v_sub_f32 v50, v50, v21
v_sub_f32 v51, v51, v21
cv 42,44,45
cv 43,46,47
v_exp_f32 v48, v48
v_exp_f32 v49, v49
v_sub_f32 v52, v52, v21
v_sub_f32 v53, v53, v21
v_sub_f32 v54, v54, v21
v_sub_f32 v55, v55, v21
v_exp_f32 v50, v50
v_exp_f32 v51, v51
v_exp_f32 v52, v52
v_exp_f32 v53, v53
v_exp_f32 v54, v54
v_exp_f32 v55, v55
v_add_u32 v3, 0x2000, v3
cv 48,48,49
tr 236,3,40960
tr 238,3,47104
v_mfma_f32_16x16x32_bf16 a[244:247], a[240:243], v[40:43], 0
cv 49,50,51
cv 50,52,53
cv 51,54,55
v_add_u32 v4, 0x2000, v4
tr 232,4,40960
v_mfma_f32_16x16x32_bf16 a[248:251], a[240:243], v[48:51], 0
tr 234,4,47104
tr 252,1,41984
s_waitcnt lgkmcnt(5)
maz 0,224,40
tr 254,1,48128
tr 248,2,41984
maz 8,224,48
tr 250,2,48128
tr 216,3,41984
maz 4,228,40
tr 218,3,48128
tr 220,4,41984
maz 12,228,48
tr 222,4,48128
tr 224,1,43008
tr 226,1,49152
s_waitcnt lgkmcnt(12)
maz 16,236,40
tr 228,2,43008
tr 230,2,49152
maz 24,236,48
tr 236,3,43008
tr 238,3,49152
v_sub_f32 v56, v56, v20
s_waitcnt lgkmcnt(12)
maz 20,232,40
v_sub_f32 v57, v57, v20
v_sub_f32 v58, v58, v20
maz 28,232,48
tr 232,4,43008
tr 234,4,49152
maz 32,252,40
v_sub_f32 v59, v59, v20
v_exp_f32 v56, v56
maz 40,252,48
tr 252,1,53248
tr 254,1,59392
s_waitcnt lgkmcnt(7)
maz 36,248,40
v_exp_f32 v57, v57
maz 44,248,48
tr 248,2,53248
tr 250,2,59392
maz 48,216,40
v_sub_f32 v60, v60, v20
v_sub_f32 v61, v61, v20
maz 56,216,48
v_sub_f32 v62, v62, v20
v_sub_f32 v63, v63, v20
maz 52,220,40
v_exp_f32 v58, v58
maz 60,220,48
v_exp_f32 v59, v59
maz 64,224,40
v_exp_f32 v60, v60
maz 72,224,48
v_exp_f32 v61, v61
s_waitcnt lgkmcnt(0)
maz 68,228,40
v_exp_f32 v62, v62
maz 76,228,48
s_add_u32 m0, s16, 27648
v_exp_f32 v63, v63
dn 10,36,23,0
maz 80,236,40
cv 56,56,57
v_sub_f32 v64, v64, v21
maz 88,236,48
v_sub_f32 v65, v65, v21
cv 57,58,59
maz 84,232,40
v_sub_f32 v66, v66, v21
v_sub_f32 v67, v67, v21
maz 92,232,48
dn 10,40,23,1024
cv 58,60,61
cv 59,62,63
v_exp_f32 v64, v64
v_exp_f32 v65, v65
v_sub_f32 v68, v68, v21
v_sub_f32 v69, v69, v21
v_sub_f32 v70, v70, v21
v_sub_f32 v71, v71, v21
v_exp_f32 v66, v66
v_exp_f32 v67, v67
dn 10,44,23,2048
v_exp_f32 v68, v68
v_exp_f32 v69, v69
v_exp_f32 v70, v70
v_exp_f32 v71, v71
cv 64,64,65
tr 216,3,53248
tr 218,3,59392
v_mfma_f32_16x16x32_bf16 a[244:247], a[240:243], v[56:59], a[244:247]
cv 65,66,67
cv 66,68,69
cv 67,70,71
tr 220,4,53248
tr 222,4,59392
v_mfma_f32_16x16x32_bf16 a[248:251], a[240:243], v[64:67], a[248:251]
tr 224,1,54272
tr 226,1,60416
ma 0,252,56
tr 228,2,54272
tr 230,2,60416
ma 8,252,64
tr 236,3,54272
tr 238,3,60416
ma 4,248,56
tr 232,4,54272
tr 234,4,60416
ma 12,248,64
tr 252,1,55296
tr 254,1,61440
tr 248,2,55296
tr 250,2,61440
s_waitcnt lgkmcnt(14)
ma 16,216,56
v_add_u32 v1, 0xffffe000, v1
cv 176,176,177
ma 24,216,64
tr 216,3,55296
tr 218,3,61440
s_waitcnt lgkmcnt(11)
ma 20,220,56
cv 177,178,179
cv 178,180,181
ma 28,220,64
tr 220,4,55296
tr 222,4,61440
ma 32,224,56
cv 179,182,183
aw 196,176
ma 40,224,64
aw 197,177
aw 198,178
s_waitcnt lgkmcnt(4)
ma 36,228,56
aw 199,179
cv 168,168,169
ma 44,228,64
cv 169,170,171
cv 170,172,173
ma 48,236,56
s_waitcnt vmcnt(12) lgkmcnt(0)
s_barrier
ma 56,236,64
tr 224,1,0
tr 226,1,6144
ma 52,232,56
tr 228,1,12288
tr 230,1,18432
ma 60,232,64
tr 236,1,1024
tr 238,1,7168
ma 64,252,56
cv 171,174,175
aw 192,168
ma 72,252,64
aw 193,169
aw 194,170
ma 68,248,56
aw 195,171
tr 232,1,13312
ma 76,248,64
s_add_u32 m0, s16, 49152
s_add_u32 s24, s20, 246144
dl 15,72,24,0
ma 80,216,56
tr 234,1,19456
tr 252,1,2048
ma 88,216,64
tr 254,1,8192
tr 248,1,14336
ma 84,220,56
tr 250,1,20480
v_add_u32 v2, 0xffffe000, v2
ma 92,220,64
dl 15,76,24,1024
v_mfma_f32_16x16x32_bf16 v[176:179], v[242:245], a[212:215], 0
tr 216,2,0
tr 218,2,6144
v_mfma_f32_16x16x32_bf16 v[168:171], v[242:245], a[208:211], 0
tr 220,2,12288
tr 222,2,18432
s_waitcnt lgkmcnt(9)
mv 168,224,120
v_add_u32 v3, 0xffffe000, v3
cv 192,192,193
mv 168,228,128
dl 15,80,24,2048
mv 176,228,132
tr 228,2,1024
tr 230,2,7168
mv 176,224,124
tr 224,2,13312
tr 226,2,19456
mv 176,236,140
cv 193,194,195
cv 194,196,197
s_waitcnt lgkmcnt(6)
mv 176,232,148
cv 195,198,199
aw 204,192
mv 168,232,144
tr 232,2,2048
tr 234,2,8192
mv 168,236,136
tr 236,2,14336
tr 238,2,20480
mv 168,252,152
aw 205,193
aw 206,194
mv 168,248,160
aw 207,195
cv 184,184,185
mv 176,248,164
tr 248,3,0
tr 250,3,6144
mv 176,252,156
tr 252,3,12288
tr 254,3,18432
v_mfma_f32_16x16x32_bf16 v[180:183], v[240:243], a[212:215], 0
cv 185,186,187
cv 186,188,189
v_mfma_f32_16x16x32_bf16 v[172:175], v[240:243], a[208:211], 0
cv 187,190,191
aw 200,184
mv 172,216,120
aw 201,185
aw 202,186
s_waitcnt lgkmcnt(4)
mv 172,220,128
aw 203,187
v_add_u32 v4, 0xffffe000, v4
mv 180,220,132
tr 220,3,1024
tr 222,3,7168
mv 180,216,124
tr 216,3,13312
tr 218,3,19456
mv 180,228,140
v_add_u32 v1, 0x8000, v1
cv 176,176,177
mv 180,224,148
cv 177,178,179
aw 212,176
mv 172,224,144
tr 224,3,2048
tr 226,3,8192
mv 172,228,136
s_add_u32 m0, s16, 52224
tr 228,3,14336
dl 16,72,24,0
mv 172,232,152
tr 230,3,20480
aw 213,177
mv 172,236,160
cv 168,168,169
cv 169,170,171
mv 180,236,164
tr 236,4,0
tr 238,4,6144
mv 180,232,156
dl 16,76,24,1024
v_mfma_f32_16x16x32_bf16 v[192:195], v[242:245], a[220:223], 0
tr 232,4,12288
tr 234,4,18432
v_mfma_f32_16x16x32_bf16 v[184:187], v[242:245], a[216:219], 0
cv 170,172,173
cv 171,174,175
s_waitcnt lgkmcnt(5)
mv 184,248,120
cv 178,180,181
cv 179,182,183
mv 184,252,128
dl 16,80,24,2048
mv 192,252,132
tr 252,4,1024
tr 254,4,7168
mv 192,248,124
tr 248,4,13312
tr 250,4,19456
mv 192,220,140
aw 214,178
aw 215,179
mv 192,216,148
aw 208,168
aw 209,169
mv 184,216,144
tr 216,4,2048
tr 218,4,8192
mv 184,220,136
tr 220,4,14336
tr 222,4,20480
mv 184,224,152
aw 210,170
aw 211,171
s_waitcnt lgkmcnt(6)
mv 184,228,160
v_add_u32 v2, 0x8000, v2
v_add_u32 v3, 0x8000, v3
mv 192,228,164
v_add_u32 v4, 0x8000, v4
v_add_u32 v5, 0xffffa000, v5
mv 192,224,156
v_add_u32 v6, 0xffffa000, v6
v_sub_f32 v32, 0, v20
v_mfma_f32_16x16x32_bf16 v[196:199], v[240:243], a[220:223], 0
s_waitcnt vmcnt(12) lgkmcnt(0)
s_barrier
v_mfma_f32_16x16x32_bf16 v[188:191], v[240:243], a[216:219], 0
tr 228,1,40960
tr 230,1,47104
mv 188,236,120
tr 224,1,53248
tr 226,1,59392
mv 188,232,128
cv 192,192,193
cv 193,194,195
mv 196,232,132
tr 232,1,41984
tr 234,1,48128
mv 196,236,124
tr 236,1,54272
tr 238,1,60416
mv 196,252,140
aw 220,192
aw 221,193
mv 196,248,148
cv 184,184,185
cv 185,186,187
mv 188,248,144
tr 248,1,43008
tr 250,1,49152
mv 188,252,136
s_add_u32 m0, s16, 0
s_add_u32 s25, s18, 417792
dn 11,48,25,0
mv 188,216,152
tr 252,1,55296
tr 254,1,61440
mv 188,220,160
aw 216,184
aw 217,185
mv 196,220,164
tr 220,2,40960
tr 222,2,47104
mv 196,216,156
dn 11,52,25,1024
v_mfma_f32_16x16x32_bf16 v[176:179], v[242:245], a[228:231], 0
tr 216,2,53248
tr 218,2,59392
v_mfma_f32_16x16x32_bf16 v[168:171], v[242:245], a[224:227], 0
cv 186,188,189
cv 187,190,191
s_waitcnt lgkmcnt(6)
mv 168,228,120
cv 194,196,197
cv 195,198,199
mv 168,224,128
dn 11,56,25,2048
mv 176,224,132
tr 224,2,41984
tr 226,2,48128
mv 176,228,124
tr 228,2,54272
tr 230,2,60416
mv 176,232,140
aw 222,194
aw 223,195
mv 176,236,148
aw 218,186
aw 219,187
mv 168,236,144
tr 236,2,43008
tr 238,2,49152
mv 168,232,136
tr 232,2,55296
tr 234,2,61440
mv 168,248,152
v_sub_f32 v33, 0, v20
v_sub_f32 v34, 0, v20
s_waitcnt lgkmcnt(6)
mv 168,252,160
v_sub_f32 v35, 0, v20
v_sub_f32 v36, 0, v21
mv 176,252,164
tr 252,3,40960
tr 254,3,47104
mv 176,248,156
tr 248,3,53248
tr 250,3,59392
v_mfma_f32_16x16x32_bf16 v[180:183], v[240:243], a[228:231], 0
v_sub_f32 v37, 0, v21
v_sub_f32 v38, 0, v21
v_mfma_f32_16x16x32_bf16 v[172:175], v[240:243], a[224:227], 0
v_sub_f32 v39, 0, v21
rd 204,19,53248
mv 172,220,120
rd 200,19,49152
cv 176,176,177
mv 172,216,128
cv 177,178,179
aw 228,176
mv 180,216,132
tr 216,3,41984
tr 218,3,48128
mv 180,220,124
tr 220,3,54272
tr 222,3,60416
mv 180,224,140
aw 229,177
cv 168,168,169
s_waitcnt lgkmcnt(6)
mv 180,228,148
cv 169,170,171
aw 224,168
mv 172,228,144
tr 228,3,43008
tr 230,3,49152
mv 172,224,136
s_add_u32 m0, s16, 3072
tr 224,3,55296
dn 12,48,25,0
mv 172,236,152
tr 226,3,61440
aw 225,169
mv 172,232,160
rd 208,19,57344
mv 180,232,164
tr 232,4,40960
tr 234,4,47104
mv 180,236,156
dn 12,52,25,1024
v_mfma_f32_16x16x32_bf16 v[192:195], v[242:245], a[236:239], 0
tr 236,4,53248
tr 238,4,59392
v_mfma_f32_16x16x32_bf16 v[184:187], v[242:245], a[232:235], 0
cv 170,172,173
cv 171,174,175
mv 184,252,120
cv 178,180,181
cv 179,182,183
mv 184,248,128
dn 12,56,25,2048
mv 192,248,132
tr 248,4,41984
tr 250,4,48128
mv 192,252,124
tr 252,4,54272
tr 254,4,60416
s_waitcnt lgkmcnt(6)
mv 192,216,140
aw 230,178
aw 231,179
mv 192,220,148
aw 226,170
aw 227,171
mv 184,220,144
tr 220,4,43008
tr 222,4,49152
mv 184,216,136
tr 216,4,55296
tr 218,4,61440
mv 184,228,152
mv 184,224,160
mv 192,224,164
mv 192,228,156
v_mfma_f32_16x16x32_bf16 v[196:199], v[240:243], a[236:239], 0
s_waitcnt vmcnt(12) lgkmcnt(0)
s_barrier
v_mfma_f32_16x16x32_bf16 v[188:191], v[240:243], a[232:235], 0
rd 224,5,16384
rd 228,6,16384
mv 188,232,120
s_nop 1
cv 192,192,193
cv 193,194,195
mv 188,236,128
aw 236,192
aw 237,193
mv 196,236,132
rd 236,5,17408
cv 184,184,185
mv 196,232,124
rd 232,6,17408
cv 185,186,187
mv 196,248,140
aw 232,184
aw 233,185
mv 196,252,148
mv 188,252,144
rd 252,5,18432
mv 188,248,136
s_add_u32 m0, s16, 73728
s_add_u32 s22, s20, 246272
dl 15,72,22,0
mv 188,220,152
rd 248,6,18432
mv 188,216,160
mv 196,216,164
rd 216,5,22528
mv 196,220,156
dl 15,76,22,1024
s_waitcnt lgkmcnt(3)
mvc 40,224,72,32
rd 220,6,22528
s_nop 0
cv 186,188,189
mv 40,228,80
cv 187,190,191
cv 194,196,197
mvc 48,228,84,36
rd 228,5,23552
cv 195,198,199
mv 48,224,76
dl 15,80,22,2048
mv 48,236,92
rd 224,6,23552
aw 238,194
mv 48,232,100
aw 239,195
aw 234,186
mv 40,232,96
rd 232,5,24576
aw 235,187
mv 40,236,88
rd 236,6,24576
s_waitcnt lgkmcnt(4)
mv 40,252,104
mv 40,248,112
mv 48,248,116
rd 248,5,28672
mv 48,252,108
rd 252,6,28672
mvc 44,216,72,32
s_nop 2
v_max3_f32 v20, v40, v41, v42
mv 44,220,80
mvc 52,220,84,36
rd 220,5,29696
v_max3_f32 v20, v20, v43, v48
mv 52,216,76
rd 216,6,29696
v_max3_f32 v20, v20, v49, v50
s_waitcnt lgkmcnt(4)
mv 52,228,92
mv 52,224,100
mv 44,224,96
rd 224,5,30720
mv 44,228,88
s_add_u32 m0, s16, 76800
rd 228,6,30720
dl 16,72,22,0
mv 44,232,104
mv 44,236,112
mv 52,236,116
rd 236,5,34816
mv 52,232,108
dl 16,76,22,1024
s_waitcnt lgkmcnt(3)
mvc 56,248,72,32
rd 232,6,34816
s_nop 0
v_max3_f32 v20, v20, v51, v44
mv 56,252,80
v_max3_f32 v20, v20, v45, v46
v_max3_f32 v20, v20, v47, v52
mvc 64,252,84,36
rd 252,5,35840
v_max3_f32 v20, v20, v53, v54
mv 64,248,76
dl 16,80,22,2048
mv 64,220,92
rd 248,6,35840
mv 64,216,100
mv 56,216,96
rd 216,5,36864
mv 56,220,88
rd 220,6,36864
s_waitcnt lgkmcnt(4)
mv 56,224,104
mv 56,228,112
mv 64,228,116
mv 64,224,108
s_waitcnt vmcnt(12) lgkmcnt(0)
s_barrier
mvc 60,236,72,32
tr 228,1,16384
tr 230,1,22528
mv 60,232,80
tr 224,1,28672
tr 226,1,34816
mvc 68,232,84,36
tr 232,1,17408
tr 234,1,23552
mv 68,236,76
tr 236,1,29696
tr 238,1,35840
mv 68,252,92
v_max3_f32 v20, v20, v55, v56
v_max3_f32 v20, v20, v57, v58
mv 68,248,100
v_max3_f32 v20, v20, v59, v64
v_max3_f32 v20, v20, v65, v66
mv 60,248,96
tr 248,1,18432
tr 250,1,24576
mv 60,252,88
s_add_u32 m0, s16, 24576
s_add_u32 s23, s18, 442368
dn 9,36,23,0
mv 60,216,104
tr 252,1,30720
tr 254,1,36864
mv 60,220,112
v_add_u32 v1, 0xffff8000, v1
mv 68,220,116
tr 220,2,16384
tr 222,2,22528
mv 68,216,108
dn 9,40,23,1024
mvz 176,242,204
tr 216,2,28672
tr 218,2,34816
mvz 168,242,200
v_max3_f32 v20, v20, v67, v60
v_max3_f32 v20, v20, v61, v62
s_waitcnt lgkmcnt(6)
mv 168,228,120
v_max3_f32 v20, v20, v63, v68
v_max3_f32 v20, v20, v69, v70
mv 168,224,128
dn 9,44,23,2048
mv 176,224,132
tr 224,2,17408
tr 226,2,23552
mv 176,228,124
tr 228,2,29696
tr 230,2,35840
mv 176,232,140
v_max_f32 v20, v20, v71
v_cmp_lt_f32 vcc, s27, v20
s_cbranch_vccz Lc14
s_swappc_b64 s[30:31], s[28:29]
Lc14:
mv 176,236,148
v_exp_f32 v40, v40
mv 168,236,144
tr 236,2,18432
tr 238,2,24576
mv 168,232,136
tr 232,2,30720
tr 234,2,36864
mv 168,248,152
v_add_u32 v2, 0xffff8000, v2
v_exp_f32 v41, v41
s_waitcnt lgkmcnt(6)
mv 168,252,160
v_exp_f32 v42, v42
mv 176,252,164
tr 252,3,16384
tr 254,3,22528
mv 176,248,156
tr 248,3,28672
tr 250,3,34816
mvz 180,240,204
v_exp_f32 v43, v43
mvz 172,240,200
v_exp_f32 v44, v44
mv 172,220,120
v_exp_f32 v45, v45
mv 172,216,128
v_exp_f32 v46, v46
mv 180,216,132
tr 216,3,17408
tr 218,3,23552
mv 180,220,124
tr 220,3,29696
tr 222,3,35840
mv 180,224,140
v_exp_f32 v47, v47
s_waitcnt lgkmcnt(4)
mv 180,228,148
cv 40,40,41
cv 41,42,43
mv 172,228,144
tr 228,3,18432
tr 230,3,24576
mv 172,224,136
s_add_u32 m0, s16, 27648
tr 224,3,30720
dn 10,36,23,0
mv 172,236,152
tr 226,3,36864
cv 42,44,45
mv 172,232,160
cv 43,46,47
v_exp_f32 v48, v48
mv 180,232,164
tr 232,4,16384
tr 234,4,22528
mv 180,236,156
dn 10,40,23,1024
mvz 192,242,212
tr 236,4,28672
tr 238,4,34816
mvz 184,242,208
v_exp_f32 v49, v49
mv 184,252,120
v_exp_f32 v50, v50
mv 184,248,128
dn 10,44,23,2048
mv 192,248,132
tr 248,4,17408
tr 250,4,23552
mv 192,252,124
tr 252,4,29696
tr 254,4,35840
s_waitcnt lgkmcnt(6)
mv 192,216,140
v_exp_f32 v51, v51
mv 192,220,148
v_exp_f32 v52, v52
mv 184,220,144
tr 220,4,18432
tr 222,4,24576
mv 184,216,136
tr 216,4,30720
tr 218,4,36864
mv 184,228,152
v_exp_f32 v53, v53
mv 184,224,160
v_exp_f32 v54, v54
mv 192,224,164
v_exp_f32 v55, v55
mv 192,228,156
v_add_u32 v3, 0xffff8000, v3
cv 48,48,49
mvz 196,240,212
s_waitcnt vmcnt(12) lgkmcnt(0)
s_barrier
mvz 188,240,208
tr 224,1,0
tr 226,1,6144
mv 188,232,120
tr 228,2,0
tr 230,2,6144
mv 188,236,128
cv 49,50,51
cv 50,52,53
mv 196,236,132
tr 236,3,0
tr 238,3,6144
mv 196,232,124
cv 51,54,55
v_add_u32 v4, 0xffff8000, v4
mv 196,248,140
tr 232,4,0
tr 234,4,6144
mv 196,252,148
v_exp_f32 v56, v56
mv 188,252,144
tr 252,1,1024
tr 254,1,7168
mv 188,248,136
s_add_u32 m0, s16, 49152
s_add_u32 s24, s20, 246400
dl 15,72,24,0
mv 188,220,152
tr 248,2,1024
tr 250,2,7168
mv 188,216,160
v_exp_f32 v57, v57
mv 196,216,164
tr 216,3,1024
tr 218,3,7168
mv 196,220,156
dl 15,76,24,1024
v_mfma_f32_16x16x32_bf16 a[244:247], a[240:243], v[40:43], a[244:247]
tr 220,4,1024
tr 222,4,7168
v_mfma_f32_16x16x32_bf16 a[248:251], a[240:243], v[48:51], a[248:251]
v_exp_f32 v58, v58
s_waitcnt lgkmcnt(6)
ma 0,224,40
v_exp_f32 v59, v59
ma 8,224,48
dl 15,80,24,2048
ma 4,228,40
tr 224,1,2048
tr 226,1,8192
ma 12,228,48
tr 228,2,2048
tr 230,2,8192
ma 16,236,40
v_exp_f32 v60, v60
ma 24,236,48
tr 236,3,2048
tr 238,3,8192
ma 20,232,40
v_exp_f32 v61, v61
ma 28,232,48
tr 232,4,2048
tr 234,4,8192
ma 32,252,40
v_exp_f32 v62, v62
ma 40,252,48
tr 252,1,12288
tr 254,1,18432
s_waitcnt lgkmcnt(6)
ma 36,248,40
v_exp_f32 v63, v63
ma 44,248,48
tr 248,2,12288
tr 250,2,18432
ma 48,216,40
cv 56,56,57
cv 57,58,59
ma 56,216,48
cv 58,60,61
cv 59,62,63
ma 52,220,40
v_exp_f32 v64, v64
ma 60,220,48
v_exp_f32 v65, v65
ma 64,224,40
v_exp_f32 v66, v66
ma 72,224,48
v_exp_f32 v67, v67
ma 68,228,40
v_exp_f32 v68, v68
ma 76,228,48
s_add_u32 m0, s16, 52224
v_exp_f32 v69, v69
dl 16,72,24,0
s_waitcnt lgkmcnt(0)
ma 80,236,40
v_exp_f32 v70, v70
ma 88,236,48
v_exp_f32 v71, v71
ma 84,232,40
cv 64,64,65
tr 216,3,12288
ma 92,232,48
dl 16,76,24,1024
tr 218,3,18432
v_mfma_f32_16x16x32_bf16 a[244:247], a[240:243], v[56:59], a[244:247]
cv 65,66,67
cv 66,68,69
cv 67,70,71
tr 220,4,12288
tr 222,4,18432
v_mfma_f32_16x16x32_bf16 a[248:251], a[240:243], v[64:67], a[248:251]
tr 224,1,13312
tr 226,1,19456
ma 0,252,56
dl 16,80,24,2048
ma 8,252,64
tr 228,2,13312
tr 230,2,19456
ma 4,248,56
tr 236,3,13312
tr 238,3,19456
ma 12,248,64
tr 232,4,13312
tr 234,4,19456
tr 252,1,14336
tr 254,1,20480
s_waitcnt lgkmcnt(12)
ma 16,216,56
tr 248,2,14336
tr 250,2,20480
ma 24,216,64
tr 216,3,14336
tr 218,3,20480
s_waitcnt lgkmcnt(12)
ma 20,220,56
v_add_u32 v1, 0x8000, v1
cv 176,176,177
ma 28,220,64
tr 220,4,14336
tr 222,4,20480
ma 32,224,56
cv 177,178,179
cv 178,180,181
ma 40,224,64
cv 179,182,183
wr 176,53248
s_waitcnt lgkmcnt(7)
ma 36,228,56
cv 168,168,169
cv 169,170,171
ma 44,228,64
cv 170,172,173
cv 171,174,175
ma 48,236,56
s_waitcnt vmcnt(12) lgkmcnt(0)
s_barrier
ma 56,236,64
tr 224,1,40960
tr 226,1,47104
ma 52,232,56
tr 228,1,53248
tr 230,1,59392
ma 60,232,64
tr 236,1,41984
tr 238,1,48128
ma 64,252,56
wr 168,49152
tr 232,1,54272
ma 72,252,64
tr 234,1,60416
tr 252,1,43008
ma 68,248,56
tr 254,1,49152
v_add_u32 v2, 0x8000, v2
ma 76,248,64
s_add_u32 m0, s16, 0
s_add_u32 s25, s18, 442368
dn 11,48,25,0
ma 80,216,56
tr 248,1,55296
tr 250,1,61440
ma 88,216,64
tr 216,2,40960
tr 218,2,47104
ma 84,220,56
v_add_u32 v3, 0x8000, v3
cv 192,192,193
ma 92,220,64
dn 11,52,25,1024
v_mfma_f32_16x16x32_bf16 v[176:179], v[242:245], a[100:103], 0
tr 220,2,53248
tr 222,2,59392
v_mfma_f32_16x16x32_bf16 v[168:171], v[242:245], a[96:99], 0
cv 193,194,195
cv 194,196,197
s_waitcnt lgkmcnt(6)
mv 168,224,120
cv 195,198,199
wr 192,61440
mv 168,228,128
dn 11,56,25,2048
mv 176,228,132
tr 228,2,41984
tr 230,2,48128
mv 176,224,124
tr 224,2,54272
tr 226,2,60416
mv 176,236,140
cv 184,184,185
cv 185,186,187
mv 176,232,148
cv 186,188,189
cv 187,190,191
mv 168,232,144
tr 232,2,43008
tr 234,2,49152
mv 168,236,136
tr 236,2,55296
tr 238,2,61440
mv 168,252,152
wr 184,57344
v_add_u32 v4, 0x8000, v4
s_waitcnt lgkmcnt(7)
mv 168,248,160
mv 176,248,164
tr 248,3,40960
tr 250,3,47104
mv 176,252,156
tr 252,3,53248
tr 254,3,59392
v_mfma_f32_16x16x32_bf16 v[180:183], v[240:243], a[100:103], 0
s_nop 0
cv 168,168,169
cv 169,170,171
v_mfma_f32_16x16x32_bf16 v[172:175], v[240:243], a[96:99], 0
aw 96,168
aw 97,169
mv 172,216,120
cv 176,176,177
cv 177,178,179
mv 172,220,128
aw 100,176
aw 101,177
mv 180,220,132
tr 220,3,41984
tr 222,3,48128
mv 180,216,124
tr 216,3,54272
tr 218,3,60416
mv 180,228,140
s_waitcnt lgkmcnt(4)
mv 180,224,148
mv 172,224,144
tr 224,3,43008
tr 226,3,49152
mv 172,228,136
s_add_u32 m0, s16, 3072
tr 228,3,55296
dn 12,48,25,0
mv 172,232,152
tr 230,3,61440
mv 172,236,160
mv 180,236,164
tr 236,4,40960
tr 238,4,47104
mv 180,232,156
dn 12,52,25,1024
v_mfma_f32_16x16x32_bf16 v[192:195], v[242:245], a[108:111], 0
tr 232,4,53248
tr 234,4,59392
v_mfma_f32_16x16x32_bf16 v[184:187], v[242:245], a[104:107], 0
cv 170,172,173
cv 171,174,175
mv 184,248,120
cv 178,180,181
cv 179,182,183
mv 184,252,128
dn 12,56,25,2048
mv 192,252,132
tr 252,4,41984
tr 254,4,48128
mv 192,248,124
tr 248,4,54272
tr 250,4,60416
s_waitcnt lgkmcnt(6)
mv 192,220,140
aw 102,178
aw 103,179
mv 192,216,148
aw 98,170
aw 99,171
mv 184,216,144
tr 216,4,43008
tr 218,4,49152
mv 184,220,136
tr 220,4,55296
tr 222,4,61440
mv 184,224,152
mv 184,228,160
mv 192,228,164
mv 192,224,156
v_mfma_f32_16x16x32_bf16 v[196:199], v[240:243], a[108:111], 0
s_waitcnt vmcnt(12) lgkmcnt(0)
s_barrier
v_mfma_f32_16x16x32_bf16 v[188:191], v[240:243], a[104:107], 0
rd 228,5,16384
rd 224,6,16384
mv 188,236,120
s_nop 1
cv 192,192,193
cv 193,194,195
mv 188,232,128
aw 108,192
aw 109,193
mv 196,232,132
rd 232,5,17408
cv 184,184,185
mv 196,236,124
rd 236,6,17408
cv 185,186,187
mv 196,252,140
aw 104,184
aw 105,185
mv 196,248,148
mv 188,248,144
rd 248,5,18432
mv 188,252,136
s_add_u32 m0, s16, 73728
s_add_u32 s22, s20, 246528
dl 15,72,22,0
mv 188,216,152
rd 252,6,18432
mv 188,220,160
mv 196,220,164
rd 220,5,22528
mv 196,216,156
dl 15,76,22,1024
s_waitcnt lgkmcnt(3)
mvc 40,228,72,32
rd 216,6,22528
s_nop 0
cv 186,188,189
mv 40,224,80
cv 187,190,191
cv 194,196,197
mvc 48,224,84,36
rd 224,5,23552
cv 195,198,199
mv 48,228,76
dl 15,80,22,2048
mv 48,232,92
rd 228,6,23552
aw 110,194
mv 48,236,100
aw 111,195
aw 106,186
mv 40,236,96
rd 236,5,24576
aw 107,187
mv 40,232,88
rd 232,6,24576
s_waitcnt lgkmcnt(4)
mv 40,248,104
mv 40,252,112
mv 48,252,116
rd 252,5,28672
mv 48,248,108
rd 248,6,28672
mvc 44,220,72,32
s_nop 2
v_max3_f32 v20, v40, v41, v42
mv 44,216,80
mvc 52,216,84,36
rd 216,5,29696
v_max3_f32 v20, v20, v43, v48
mv 52,220,76
rd 220,6,29696
v_max3_f32 v20, v20, v49, v50
s_waitcnt lgkmcnt(4)
mv 52,224,92
mv 52,228,100
mv 44,228,96
rd 228,5,30720
mv 44,224,88
s_add_u32 m0, s16, 76800
rd 224,6,30720
dl 16,72,22,0
mv 44,236,104
mv 44,232,112
mv 52,232,116
rd 232,5,34816
mv 52,236,108
dl 16,76,22,1024
s_waitcnt lgkmcnt(3)
mvc 56,252,72,32
rd 236,6,34816
s_nop 0
v_max3_f32 v20, v20, v51, v44
mv 56,248,80
v_max3_f32 v20, v20, v45, v46
v_max3_f32 v20, v20, v47, v52
mvc 64,248,84,36
rd 248,5,35840
v_max3_f32 v20, v20, v53, v54
mv 64,252,76
dl 16,80,22,2048
mv 64,216,92
rd 252,6,35840
mv 64,220,100
mv 56,220,96
rd 220,5,36864
mv 56,216,88
rd 216,6,36864
s_waitcnt lgkmcnt(4)
mv 56,228,104
mv 56,224,112
mv 64,224,116
mv 64,228,108
s_waitcnt vmcnt(12) lgkmcnt(0)
s_barrier
mvc 60,232,72,32
tr 224,1,16384
tr 226,1,22528
mv 60,236,80
tr 228,1,28672
tr 230,1,34816
mvc 68,236,84,36
tr 236,1,17408
tr 238,1,23552
mv 68,232,76
tr 232,1,29696
tr 234,1,35840
mv 68,248,92
v_max3_f32 v20, v20, v55, v56
v_max3_f32 v20, v20, v57, v58
mv 68,252,100
v_max3_f32 v20, v20, v59, v64
v_max3_f32 v20, v20, v65, v66
mv 60,252,96
tr 252,1,18432
tr 254,1,24576
mv 60,248,88
s_add_u32 m0, s16, 24576
s_add_u32 s23, s20, 246656
dl 15,72,23,0
mv 60,220,104
tr 248,1,30720
tr 250,1,36864
mv 60,216,112
v_add_u32 v1, 0xffff8000, v1
mv 68,216,116
tr 216,2,16384
tr 218,2,22528
mv 68,220,108
dl 15,76,23,1024
v_mfma_f32_16x16x32_bf16 v[176:179], v[242:245], a[116:119], 0
tr 220,2,28672
tr 222,2,34816
v_mfma_f32_16x16x32_bf16 v[168:171], v[242:245], a[112:115], 0
v_max3_f32 v20, v20, v67, v60
v_max3_f32 v20, v20, v61, v62
s_waitcnt lgkmcnt(6)
mv 168,224,120
v_max3_f32 v20, v20, v63, v68
v_max3_f32 v20, v20, v69, v70
mv 168,228,128
dl 15,80,23,2048
mv 176,228,132
tr 228,2,17408
tr 230,2,23552
mv 176,224,124
tr 224,2,29696
tr 226,2,35840
mv 176,236,140
v_max_f32 v20, v20, v71
v_cmp_lt_f32 vcc, s27, v20
s_cbranch_vccz Lc15
s_swappc_b64 s[30:31], s[28:29]
Lc15:
mv 176,232,148
v_exp_f32 v40, v40
mv 168,232,144
tr 232,2,18432
tr 234,2,24576
mv 168,236,136
tr 236,2,30720
tr 238,2,36864
mv 168,252,152
v_add_u32 v2, 0xffff8000, v2
v_exp_f32 v41, v41
s_waitcnt lgkmcnt(6)
mv 168,248,160
v_exp_f32 v42, v42
mv 176,248,164
tr 248,3,16384
tr 250,3,22528
mv 176,252,156
tr 252,3,28672
tr 254,3,34816
v_mfma_f32_16x16x32_bf16 v[180:183], v[240:243], a[116:119], 0
v_exp_f32 v43, v43
v_mfma_f32_16x16x32_bf16 v[172:175], v[240:243], a[112:115], 0
v_exp_f32 v44, v44
mv 172,216,120
v_exp_f32 v45, v45
mv 172,220,128
v_exp_f32 v46, v46
mv 180,220,132
tr 220,3,17408
tr 222,3,23552
mv 180,216,124
tr 216,3,29696
tr 218,3,35840
mv 180,228,140
v_exp_f32 v47, v47
s_waitcnt lgkmcnt(4)
mv 180,224,148
cv 40,40,41
cv 41,42,43
mv 172,224,144
tr 224,3,18432
tr 226,3,24576
mv 172,228,136
s_add_u32 m0, s16, 27648
tr 228,3,30720
dl 16,72,23,0
mv 172,232,152
tr 230,3,36864
cv 42,44,45
mv 172,236,160
cv 43,46,47
v_exp_f32 v48, v48
mv 180,236,164
tr 236,4,16384
tr 238,4,22528
mv 180,232,156
dl 16,76,23,1024
v_mfma_f32_16x16x32_bf16 v[192:195], v[242:245], a[124:127], 0
tr 232,4,28672
tr 234,4,34816
v_mfma_f32_16x16x32_bf16 v[184:187], v[242:245], a[120:123], 0
v_exp_f32 v49, v49
mv 184,248,120
v_exp_f32 v50, v50
mv 184,252,128
dl 16,80,23,2048
mv 192,252,132
tr 252,4,17408
tr 254,4,23552
mv 192,248,124
tr 248,4,29696
tr 250,4,35840
s_waitcnt lgkmcnt(6)
mv 192,220,140
v_exp_f32 v51, v51
mv 192,216,148
v_exp_f32 v52, v52
mv 184,216,144
tr 216,4,18432
tr 218,4,24576
mv 184,220,136
tr 220,4,30720
tr 222,4,36864
mv 184,224,152
v_exp_f32 v53, v53
mv 184,228,160
v_exp_f32 v54, v54
mv 192,228,164
v_exp_f32 v55, v55
mv 192,224,156
v_add_u32 v3, 0xffff8000, v3
cv 48,48,49
)"
R"(v_mfma_f32_16x16x32_bf16 v[196:199], v[240:243], a[124:127], 0
s_waitcnt vmcnt(12) lgkmcnt(0)
s_barrier
v_mfma_f32_16x16x32_bf16 v[188:191], v[240:243], a[120:123], 0
tr 228,1,0
tr 230,1,6144
mv 188,236,120
tr 224,2,0
tr 226,2,6144
mv 188,232,128
cv 49,50,51
cv 50,52,53
mv 196,232,132
tr 232,3,0
tr 234,3,6144
mv 196,236,124
cv 51,54,55
v_add_u32 v4, 0xffff8000, v4
mv 196,252,140
tr 236,4,0
tr 238,4,6144
mv 196,248,148
v_exp_f32 v56, v56
mv 188,248,144
tr 248,1,1024
tr 250,1,7168
mv 188,252,136
s_add_u32 m0, s16, 49152
s_add_u32 s24, s18, 466944
dn 9,36,24,0
mv 188,216,152
tr 252,2,1024
tr 254,2,7168
mv 188,220,160
v_exp_f32 v57, v57
mv 196,220,164
tr 220,3,1024
tr 222,3,7168
mv 196,216,156
dn 9,40,24,1024
v_mfma_f32_16x16x32_bf16 a[244:247], a[240:243], v[40:43], a[244:247]
tr 216,4,1024
tr 218,4,7168
v_mfma_f32_16x16x32_bf16 a[248:251], a[240:243], v[48:51], a[248:251]
v_exp_f32 v58, v58
s_waitcnt lgkmcnt(6)
ma 0,228,40
v_exp_f32 v59, v59
ma 8,228,48
dn 9,44,24,2048
ma 4,224,40
tr 228,1,2048
tr 230,1,8192
ma 12,224,48
tr 224,2,2048
tr 226,2,8192
ma 16,232,40
v_exp_f32 v60, v60
ma 24,232,48
tr 232,3,2048
tr 234,3,8192
ma 20,236,40
v_exp_f32 v61, v61
ma 28,236,48
tr 236,4,2048
tr 238,4,8192
ma 32,248,40
v_exp_f32 v62, v62
ma 40,248,48
tr 248,1,12288
tr 250,1,18432
s_waitcnt lgkmcnt(6)
ma 36,252,40
v_exp_f32 v63, v63
ma 44,252,48
tr 252,2,12288
tr 254,2,18432
ma 48,220,40
cv 56,56,57
cv 57,58,59
ma 56,220,48
cv 58,60,61
cv 59,62,63
ma 52,216,40
v_exp_f32 v64, v64
ma 60,216,48
v_exp_f32 v65, v65
ma 64,228,40
v_exp_f32 v66, v66
ma 72,228,48
v_exp_f32 v67, v67
ma 68,224,40
v_exp_f32 v68, v68
ma 76,224,48
s_add_u32 m0, s16, 52224
v_exp_f32 v69, v69
dn 10,36,24,0
s_waitcnt lgkmcnt(0)
ma 80,232,40
v_exp_f32 v70, v70
ma 88,232,48
v_exp_f32 v71, v71
ma 84,236,40
cv 64,64,65
tr 220,3,12288
ma 92,236,48
dn 10,40,24,1024
tr 222,3,18432
v_mfma_f32_16x16x32_bf16 a[244:247], a[240:243], v[56:59], a[244:247]
cv 65,66,67
cv 66,68,69
cv 67,70,71
tr 216,4,12288
tr 218,4,18432
v_mfma_f32_16x16x32_bf16 a[248:251], a[240:243], v[64:67], a[248:251]
tr 228,1,13312
tr 230,1,19456
ma 0,248,56
dn 10,44,24,2048
ma 8,248,64
tr 224,2,13312
tr 226,2,19456
ma 4,252,56
tr 232,3,13312
tr 234,3,19456
ma 12,252,64
tr 236,4,13312
tr 238,4,19456
tr 248,1,14336
tr 250,1,20480
s_waitcnt lgkmcnt(12)
ma 16,220,56
tr 252,2,14336
tr 254,2,20480
ma 24,220,64
tr 220,3,14336
tr 222,3,20480
s_waitcnt lgkmcnt(12)
ma 20,216,56
v_add_u32 v1, 0x8000, v1
cv 176,176,177
ma 28,216,64
tr 216,4,14336
tr 218,4,20480
ma 32,228,56
cv 177,178,179
cv 178,180,181
ma 40,228,64
cv 179,182,183
aw 116,176
s_waitcnt lgkmcnt(6)
ma 36,224,56
aw 117,177
aw 118,178
ma 44,224,64
aw 119,179
cv 168,168,169
ma 48,232,56
s_waitcnt vmcnt(12) lgkmcnt(0)
s_barrier
ma 56,232,64
tr 228,1,40960
tr 230,1,47104
ma 52,236,56
tr 224,1,53248
tr 226,1,59392
ma 60,236,64
tr 232,1,41984
tr 234,1,48128
ma 64,248,56
cv 169,170,171
cv 170,172,173
ma 72,248,64
cv 171,174,175
aw 112,168
ma 68,252,56
aw 113,169
aw 114,170
ma 76,252,64
s_add_u32 m0, s16, 0
s_add_u32 s25, s20, 246784
dl 15,72,25,0
ma 80,220,56
aw 115,171
tr 236,1,54272
ma 88,220,64
tr 238,1,60416
tr 248,1,43008
ma 84,216,56
tr 250,1,49152
tr 252,1,55296
ma 92,216,64
dl 15,76,25,1024
v_mfma_f32_16x16x32_bf16 v[176:179], v[242:245], a[132:135], 0
tr 254,1,61440
v_add_u32 v2, 0x8000, v2
v_mfma_f32_16x16x32_bf16 v[168:171], v[242:245], a[128:131], 0
tr 220,2,40960
tr 222,2,47104
s_waitcnt lgkmcnt(8)
mv 168,228,120
tr 216,2,53248
tr 218,2,59392
mv 168,224,128
dl 15,80,25,2048
mv 176,224,132
tr 224,2,41984
tr 226,2,48128
mv 176,228,124
tr 228,2,54272
tr 230,2,60416
mv 176,232,140
v_add_u32 v3, 0x8000, v3
cv 192,192,193
s_waitcnt lgkmcnt(8)
mv 176,236,148
cv 193,194,195
cv 194,196,197
mv 168,236,144
tr 236,2,43008
tr 238,2,49152
mv 168,232,136
tr 232,2,55296
tr 234,2,61440
mv 168,248,152
cv 195,198,199
aw 124,192
mv 168,252,160
aw 125,193
aw 126,194
mv 176,252,164
tr 252,3,40960
tr 254,3,47104
mv 176,248,156
tr 248,3,53248
tr 250,3,59392
v_mfma_f32_16x16x32_bf16 v[180:183], v[240:243], a[132:135], 0
aw 127,195
cv 184,184,185
v_mfma_f32_16x16x32_bf16 v[172:175], v[240:243], a[128:131], 0
cv 185,186,187
cv 186,188,189
s_waitcnt lgkmcnt(4)
mv 172,220,120
cv 187,190,191
aw 120,184
mv 172,216,128
aw 121,185
aw 122,186
mv 180,216,132
tr 216,3,41984
tr 218,3,48128
mv 180,220,124
aw 123,187
tr 220,3,54272
mv 180,224,140
tr 222,3,60416
v_add_u32 v4, 0x8000, v4
mv 180,228,148
v_add_u32 v1, 0xffff8000, v1
cv 176,176,177
mv 172,228,144
tr 228,3,43008
tr 230,3,49152
mv 172,224,136
s_add_u32 m0, s16, 3072
tr 224,3,55296
dl 16,72,25,0
mv 172,236,152
tr 226,3,61440
cv 177,178,179
mv 172,232,160
aw 132,176
aw 133,177
mv 180,232,164
tr 232,4,40960
tr 234,4,47104
mv 180,236,156
dl 16,76,25,1024
v_mfma_f32_16x16x32_bf16 v[192:195], v[242:245], a[140:143], 0
tr 236,4,53248
tr 238,4,59392
v_mfma_f32_16x16x32_bf16 v[184:187], v[242:245], a[136:139], 0
cv 168,168,169
cv 169,170,171
s_waitcnt lgkmcnt(5)
mv 184,252,120
cv 178,180,181
cv 179,182,183
mv 184,248,128
dl 16,80,25,2048
mv 192,248,132
tr 248,4,41984
tr 250,4,48128
mv 192,252,124
tr 252,4,54272
tr 254,4,60416
mv 192,216,140
aw 134,178
aw 135,179
mv 192,220,148
cv 170,172,173
cv 171,174,175
mv 184,220,144
tr 220,4,43008
tr 222,4,49152
mv 184,216,136
tr 216,4,55296
tr 218,4,61440
mv 184,228,152
aw 128,168
aw 129,169
s_waitcnt lgkmcnt(6)
mv 184,224,160
aw 130,170
aw 131,171
mv 192,224,164
v_add_u32 v2, 0xffff8000, v2
v_add_u32 v3, 0xffff8000, v3
mv 192,228,156
v_add_u32 v4, 0xffff8000, v4
v_mfma_f32_16x16x32_bf16 v[196:199], v[240:243], a[140:143], 0
s_waitcnt vmcnt(12) lgkmcnt(0)
s_barrier
v_mfma_f32_16x16x32_bf16 v[188:191], v[240:243], a[136:139], 0
tr 224,1,24576
tr 226,1,30720
mv 188,232,120
tr 228,1,36864
tr 230,1,43008
mv 188,236,128
cv 192,192,193
cv 193,194,195
mv 196,236,132
tr 236,1,25600
tr 238,1,31744
mv 196,232,124
tr 232,1,37888
tr 234,1,44032
mv 196,248,140
aw 140,192
aw 141,193
mv 196,252,148
cv 184,184,185
cv 185,186,187
mv 188,252,144
tr 252,1,26624
tr 254,1,32768
mv 188,248,136
s_add_u32 m0, s16, 73728
s_add_u32 s22, s18, 466944
dn 11,48,22,0
mv 188,220,152
tr 248,1,38912
tr 250,1,45056
mv 188,216,160
aw 136,184
aw 137,185
mv 196,216,164
tr 216,2,24576
tr 218,2,30720
mv 196,220,156
dn 11,52,22,1024
v_mfma_f32_16x16x32_bf16 v[176:179], v[242:245], a[148:151], 0
tr 220,2,36864
tr 222,2,43008
v_mfma_f32_16x16x32_bf16 v[168:171], v[242:245], a[144:147], 0
cv 186,188,189
cv 187,190,191
s_waitcnt lgkmcnt(6)
mv 168,224,120
cv 194,196,197
cv 195,198,199
mv 168,228,128
dn 11,56,22,2048
mv 176,228,132
tr 228,2,25600
tr 230,2,31744
mv 176,224,124
tr 224,2,37888
tr 226,2,44032
mv 176,236,140
aw 142,194
aw 143,195
mv 176,232,148
aw 138,186
aw 139,187
mv 168,232,144
tr 232,2,26624
tr 234,2,32768
mv 168,236,136
tr 236,2,38912
tr 238,2,45056
mv 168,252,152
s_waitcnt lgkmcnt(6)
mv 168,248,160
mv 176,248,164
tr 248,3,24576
tr 250,3,30720
mv 176,252,156
tr 252,3,36864
tr 254,3,43008
v_mfma_f32_16x16x32_bf16 v[180:183], v[240:243], a[148:151], 0
s_nop 0
cv 168,168,169
cv 169,170,171
v_mfma_f32_16x16x32_bf16 v[172:175], v[240:243], a[144:147], 0
aw 144,168
aw 145,169
mv 172,216,120
cv 176,176,177
cv 177,178,179
mv 172,220,128
aw 148,176
aw 149,177
mv 180,220,132
tr 220,3,25600
tr 222,3,31744
mv 180,216,124
tr 216,3,37888
tr 218,3,44032
mv 180,228,140
s_waitcnt lgkmcnt(4)
mv 180,224,148
mv 172,224,144
tr 224,3,26624
tr 226,3,32768
mv 172,228,136
s_add_u32 m0, s16, 76800
tr 228,3,38912
dn 12,48,22,0
mv 172,232,152
tr 230,3,45056
mv 172,236,160
mv 180,236,164
tr 236,4,24576
tr 238,4,30720
mv 180,232,156
dn 12,52,22,1024
v_mfma_f32_16x16x32_bf16 v[192:195], v[242:245], a[156:159], 0
tr 232,4,36864
tr 234,4,43008
v_mfma_f32_16x16x32_bf16 v[184:187], v[242:245], a[152:155], 0
cv 170,172,173
cv 171,174,175
mv 184,248,120
cv 178,180,181
cv 179,182,183
mv 184,252,128
dn 12,56,22,2048
mv 192,252,132
tr 252,4,25600
tr 254,4,31744
mv 192,248,124
tr 248,4,37888
tr 250,4,44032
s_waitcnt lgkmcnt(6)
mv 192,220,140
aw 150,178
aw 151,179
mv 192,216,148
aw 146,170
aw 147,171
mv 184,216,144
tr 216,4,26624
tr 218,4,32768
mv 184,220,136
tr 220,4,38912
tr 222,4,45056
mv 184,224,152
mv 184,228,160
mv 192,228,164
mv 192,224,156
v_mfma_f32_16x16x32_bf16 v[196:199], v[240:243], a[156:159], 0
s_waitcnt vmcnt(12) lgkmcnt(0)
s_barrier
v_mfma_f32_16x16x32_bf16 v[188:191], v[240:243], a[152:155], 0
rd 228,5,40960
rd 224,6,40960
mv 188,236,120
s_nop 1
cv 192,192,193
cv 193,194,195
mv 188,232,128
aw 156,192
aw 157,193
mv 196,232,132
rd 232,5,41984
cv 184,184,185
mv 196,236,124
rd 236,6,41984
cv 185,186,187
mv 196,252,140
aw 152,184
aw 153,185
mv 196,248,148
mv 188,248,144
rd 248,5,43008
mv 188,252,136
s_add_u32 m0, s16, 24576
s_add_u32 s23, s20, 246912
dl 15,72,23,0
mv 188,216,152
rd 252,6,43008
mv 188,220,160
mv 196,220,164
rd 220,5,47104
mv 196,216,156
dl 15,76,23,1024
s_waitcnt lgkmcnt(3)
mvc 40,228,72,32
rd 216,6,47104
s_nop 0
cv 186,188,189
mv 40,224,80
cv 187,190,191
cv 194,196,197
mvc 48,224,84,36
rd 224,5,48128
cv 195,198,199
mv 48,228,76
dl 15,80,23,2048
mv 48,232,92
rd 228,6,48128
aw 158,194
mv 48,236,100
aw 159,195
aw 154,186
mv 40,236,96
rd 236,5,49152
aw 155,187
mv 40,232,88
rd 232,6,49152
s_waitcnt lgkmcnt(4)
mv 40,248,104
mv 40,252,112
mv 48,252,116
rd 252,5,53248
mv 48,248,108
rd 248,6,53248
mvc 44,220,72,32
s_nop 2
v_max3_f32 v20, v40, v41, v42
mv 44,216,80
mvc 52,216,84,36
rd 216,5,54272
v_max3_f32 v20, v20, v43, v48
mv 52,220,76
rd 220,6,54272
v_max3_f32 v20, v20, v49, v50
s_waitcnt lgkmcnt(4)
mv 52,224,92
mv 52,228,100
mv 44,228,96
rd 228,5,55296
mv 44,224,88
s_add_u32 m0, s16, 27648
rd 224,6,55296
dl 16,72,23,0
mv 44,236,104
mv 44,232,112
mv 52,232,116
rd 232,5,59392
mv 52,236,108
dl 16,76,23,1024
s_waitcnt lgkmcnt(3)
mvc 56,252,72,32
rd 236,6,59392
s_nop 0
v_max3_f32 v20, v20, v51, v44
mv 56,248,80
v_max3_f32 v20, v20, v45, v46
v_max3_f32 v20, v20, v47, v52
mvc 64,248,84,36
rd 248,5,60416
v_max3_f32 v20, v20, v53, v54
mv 64,252,76
dl 16,80,23,2048
mv 64,216,92
rd 252,6,60416
mv 64,220,100
mv 56,220,96
rd 220,5,61440
mv 56,216,88
rd 216,6,61440
s_waitcnt lgkmcnt(4)
mv 56,228,104
mv 56,224,112
mv 64,224,116
mv 64,228,108
s_waitcnt vmcnt(12) lgkmcnt(0)
s_barrier
mvc 60,232,72,32
tr 224,1,0
tr 226,1,6144
mv 60,236,80
tr 228,1,12288
tr 230,1,18432
mvc 68,236,84,36
tr 236,1,1024
tr 238,1,7168
mv 68,232,76
tr 232,1,13312
tr 234,1,19456
mv 68,248,92
v_max3_f32 v20, v20, v55, v56
v_max3_f32 v20, v20, v57, v58
mv 68,252,100
v_max3_f32 v20, v20, v59, v64
v_max3_f32 v20, v20, v65, v66
mv 60,252,96
tr 252,1,2048
tr 254,1,8192
mv 60,248,88
s_add_u32 m0, s16, 49152
s_add_u32 s24, s18, 491520
dn 9,36,24,0
mv 60,220,104
tr 248,1,14336
tr 250,1,20480
mv 60,216,112
v_add_u32 v1, 0x8000, v1
mv 68,216,116
tr 216,2,0
tr 218,2,6144
mv 68,220,108
dn 9,40,24,1024
v_mfma_f32_16x16x32_bf16 v[176:179], v[242:245], a[164:167], 0
tr 220,2,12288
tr 222,2,18432
v_mfma_f32_16x16x32_bf16 v[168:171], v[242:245], a[160:163], 0
v_max3_f32 v20, v20, v67, v60
v_max3_f32 v20, v20, v61, v62
s_waitcnt lgkmcnt(6)
mv 168,224,120
v_max3_f32 v20, v20, v63, v68
v_max3_f32 v20, v20, v69, v70
mv 168,228,128
dn 9,44,24,2048
mv 176,228,132
tr 228,2,1024
tr 230,2,7168
mv 176,224,124
tr 224,2,13312
tr 226,2,19456
mv 176,236,140
v_max_f32 v20, v20, v71
v_cmp_lt_f32 vcc, s27, v20
s_cbranch_vccz Lc16
s_swappc_b64 s[30:31], s[28:29]
Lc16:
mv 176,232,148
v_exp_f32 v40, v40
mv 168,232,144
tr 232,2,2048
tr 234,2,8192
mv 168,236,136
tr 236,2,14336
tr 238,2,20480
mv 168,252,152
v_add_u32 v2, 0x8000, v2
v_exp_f32 v41, v41
s_waitcnt lgkmcnt(6)
mv 168,248,160
v_exp_f32 v42, v42
mv 176,248,164
tr 248,3,0
tr 250,3,6144
mv 176,252,156
tr 252,3,12288
tr 254,3,18432
v_mfma_f32_16x16x32_bf16 v[180:183], v[240:243], a[164:167], 0
v_exp_f32 v43, v43
v_mfma_f32_16x16x32_bf16 v[172:175], v[240:243], a[160:163], 0
v_exp_f32 v44, v44
mv 172,216,120
v_exp_f32 v45, v45
mv 172,220,128
v_exp_f32 v46, v46
mv 180,220,132
tr 220,3,1024
tr 222,3,7168
mv 180,216,124
tr 216,3,13312
tr 218,3,19456
mv 180,228,140
v_exp_f32 v47, v47
s_waitcnt lgkmcnt(4)
mv 180,224,148
cv 40,40,41
cv 41,42,43
mv 172,224,144
tr 224,3,2048
tr 226,3,8192
mv 172,228,136
s_add_u32 m0, s16, 52224
tr 228,3,14336
dn 10,36,24,0
mv 172,232,152
tr 230,3,20480
cv 42,44,45
mv 172,236,160
cv 43,46,47
v_exp_f32 v48, v48
mv 180,236,164
tr 236,4,0
tr 238,4,6144
mv 180,232,156
dn 10,40,24,1024
v_mfma_f32_16x16x32_bf16 v[192:195], v[242:245], a[172:175], 0
tr 232,4,12288
tr 234,4,18432
v_mfma_f32_16x16x32_bf16 v[184:187], v[242:245], a[168:171], 0
v_exp_f32 v49, v49
mv 184,248,120
v_exp_f32 v50, v50
mv 184,252,128
dn 10,44,24,2048
mv 192,252,132
tr 252,4,1024
tr 254,4,7168
mv 192,248,124
tr 248,4,13312
tr 250,4,19456
s_waitcnt lgkmcnt(6)
mv 192,220,140
v_exp_f32 v51, v51
mv 192,216,148
v_exp_f32 v52, v52
mv 184,216,144
tr 216,4,2048
tr 218,4,8192
mv 184,220,136
tr 220,4,14336
tr 222,4,20480
mv 184,224,152
v_exp_f32 v53, v53
mv 184,228,160
v_exp_f32 v54, v54
mv 192,228,164
v_exp_f32 v55, v55
mv 192,224,156
v_add_u32 v3, 0x8000, v3
cv 48,48,49
v_mfma_f32_16x16x32_bf16 v[196:199], v[240:243], a[172:175], 0
s_waitcnt vmcnt(12) lgkmcnt(0)
s_barrier
v_mfma_f32_16x16x32_bf16 v[188:191], v[240:243], a[168:171], 0
tr 228,1,40960
tr 230,1,47104
mv 188,236,120
tr 224,2,40960
tr 226,2,47104
mv 188,232,128
cv 49,50,51
cv 50,52,53
mv 196,232,132
tr 232,3,40960
tr 234,3,47104
mv 196,236,124
cv 51,54,55
v_add_u32 v4, 0x8000, v4
mv 196,252,140
tr 236,4,40960
tr 238,4,47104
mv 196,248,148
v_exp_f32 v56, v56
mv 188,248,144
tr 248,1,41984
tr 250,1,48128
mv 188,252,136
s_add_u32 m0, s17, 131072
s_add_u32 s25, s21, 0
dl 17,4,25,0
mv 188,216,152
tr 252,2,41984
tr 254,2,48128
mv 188,220,160
v_exp_f32 v57, v57
mv 196,220,164
tr 220,3,41984
tr 222,3,48128
mv 196,216,156
dl 18,4,25,1024
v_mfma_f32_16x16x32_bf16 a[244:247], a[240:243], v[40:43], a[244:247]
tr 216,4,41984
tr 218,4,48128
v_mfma_f32_16x16x32_bf16 a[248:251], a[240:243], v[48:51], a[248:251]
v_exp_f32 v58, v58
s_waitcnt lgkmcnt(6)
ma 0,228,40
v_exp_f32 v59, v59
ma 8,228,48
dl 17,8,25,2048
ma 4,224,40
tr 228,1,43008
tr 230,1,49152
ma 12,224,48
tr 224,2,43008
tr 226,2,49152
ma 16,232,40
v_exp_f32 v60, v60
ma 24,232,48
dl 18,8,25,3072
ma 20,236,40
tr 232,3,43008
tr 234,3,49152
ma 28,236,48
tr 236,4,43008
tr 238,4,49152
ma 32,248,40
v_exp_f32 v61, v61
ma 40,248,48
tr 248,1,53248
tr 250,1,59392
s_waitcnt lgkmcnt(6)
ma 36,252,40
s_add_u32 s22, s19, 1152
v_exp_f32 v62, v62
ma 44,252,48
tr 252,2,53248
tr 254,2,59392
ma 48,220,40
v_exp_f32 v63, v63
ma 56,220,48
cv 56,56,57
s_add_u32 m0, s16, 0
ma 52,216,40
dn 13,60,22,0
ma 60,216,48
cv 57,58,59
cv 58,60,61
ma 64,228,40
cv 59,62,63
v_exp_f32 v64, v64
ma 72,228,48
v_exp_f32 v65, v65
ma 68,224,40
dn 13,64,22,1024
ma 76,224,48
v_exp_f32 v66, v66
s_waitcnt lgkmcnt(0)
ma 80,232,40
v_exp_f32 v67, v67
ma 88,232,48
v_exp_f32 v68, v68
ma 84,236,40
dn 13,68,22,2048
ma 92,236,48
v_exp_f32 v69, v69
v_exp_f32 v70, v70
v_exp_f32 v71, v71
cv 64,64,65
tr 220,3,53248
tr 222,3,59392
v_mfma_f32_16x16x32_bf16 a[244:247], a[240:243], v[56:59], a[244:247]
cv 65,66,67
cv 66,68,69
cv 67,70,71
tr 216,4,53248
tr 218,4,59392
v_mfma_f32_16x16x32_bf16 a[248:251], a[240:243], v[64:67], a[248:251]
tr 228,1,54272
tr 230,1,60416
ma 0,248,56
tr 224,2,54272
tr 226,2,60416
ma 8,248,64
tr 232,3,54272
tr 234,3,60416
ma 4,252,56
tr 236,4,54272
s_add_u32 m0, s16, 3072
ma 12,252,64
dn 14,60,22,0
tr 238,4,60416
tr 248,1,55296
s_waitcnt lgkmcnt(11)
ma 16,220,56
tr 250,1,61440
tr 252,2,55296
ma 24,220,64
tr 254,2,61440
tr 220,3,55296
s_waitcnt lgkmcnt(10)
ma 20,216,56
dn 14,64,22,1024
ma 28,216,64
tr 222,3,61440
tr 216,4,55296
ma 32,228,56
tr 218,4,61440
v_add_u32 v1, 0xffff8000, v1
ma 40,228,64
cv 176,176,177
cv 177,178,179
s_waitcnt lgkmcnt(7)
ma 36,224,56
dn 14,68,22,2048
ma 44,224,64
cv 178,180,181
cv 179,182,183
ma 48,232,56
aw 164,176
s_waitcnt vmcnt(16) lgkmcnt(0)
s_barrier
ma 56,232,64
tr 228,1,24576
tr 230,1,30720
ma 52,236,56
tr 224,1,36864
tr 226,1,43008
ma 60,236,64
aw 165,177
aw 166,178
ma 64,248,56
aw 167,179
tr 232,1,25600
ma 72,248,64
tr 234,1,31744
cv 168,168,169
ma 68,252,56
cv 169,170,171
cv 170,172,173
ma 76,252,64
s_add_u32 m0, s16, 73728
s_add_u32 s23, s18, 491520
dn 11,48,23,0
ma 80,220,56
cv 171,174,175
aw 160,168
ma 88,220,64
aw 161,169
aw 162,170
ma 84,216,56
aw 163,171
tr 236,1,37888
ma 92,216,64
dn 11,52,23,1024
v_mfma_f32_16x16x32_bf16 v[176:179], v[242:245], a[180:183], 0
tr 238,1,44032
tr 248,1,26624
v_mfma_f32_16x16x32_bf16 v[168:171], v[242:245], a[176:179], 0
tr 250,1,32768
tr 252,1,38912
s_waitcnt lgkmcnt(5)
mv 168,228,120
tr 254,1,45056
v_add_u32 v2, 0xffff8000, v2
mv 168,224,128
dn 11,56,23,2048
mv 176,224,132
tr 220,2,24576
tr 222,2,30720
mv 176,228,124
tr 216,2,36864
tr 218,2,43008
mv 176,232,140
tr 224,2,25600
tr 226,2,31744
tr 228,2,37888
tr 230,2,44032
v_add_u32 v3, 0xffff8000, v3
s_waitcnt lgkmcnt(9)
mv 176,236,148
cv 192,192,193
cv 193,194,195
mv 168,236,144
tr 236,2,26624
tr 238,2,32768
mv 168,232,136
tr 232,2,38912
tr 234,2,45056
mv 168,248,152
cv 194,196,197
cv 195,198,199
s_waitcnt lgkmcnt(8)
mv 168,252,160
aw 172,192
aw 173,193
mv 176,252,164
tr 252,3,24576
tr 254,3,30720
mv 176,248,156
tr 248,3,36864
tr 250,3,43008
v_mfma_f32_16x16x32_bf16 v[180:183], v[240:243], a[180:183], 0
aw 174,194
aw 175,195
v_mfma_f32_16x16x32_bf16 v[172:175], v[240:243], a[176:179], 0
cv 184,184,185
cv 185,186,187
mv 172,220,120
cv 186,188,189
cv 187,190,191
mv 172,216,128
aw 168,184
aw 169,185
mv 180,216,132
tr 216,3,25600
tr 218,3,31744
mv 180,220,124
aw 170,186
aw 171,187
s_waitcnt lgkmcnt(2)
mv 180,224,140
tr 220,3,37888
tr 222,3,44032
mv 180,228,148
v_add_u32 v4, 0xffff8000, v4
cv 168,168,169
mv 172,228,144
tr 228,3,26624
tr 230,3,32768
mv 172,224,136
s_add_u32 m0, s16, 76800
tr 224,3,38912
dn 12,48,23,0
mv 172,236,152
tr 226,3,45056
cv 169,170,171
mv 172,232,160
aw 176,168
aw 177,169
mv 180,232,164
tr 232,4,24576
tr 234,4,30720
mv 180,236,156
dn 12,52,23,1024
v_mfma_f32_16x16x32_bf16 v[192:195], v[242:245], a[188:191], 0
tr 236,4,36864
tr 238,4,43008
v_mfma_f32_16x16x32_bf16 v[184:187], v[242:245], a[184:187], 0
cv 170,172,173
cv 171,174,175
mv 184,252,120
aw 178,170
aw 179,171
mv 184,248,128
dn 12,56,23,2048
mv 192,248,132
tr 248,4,25600
tr 250,4,31744
mv 192,252,124
tr 252,4,37888
tr 254,4,44032
s_waitcnt lgkmcnt(6)
mv 192,216,140
cv 176,176,177
cv 177,178,179
mv 192,220,148
cv 178,180,181
cv 179,182,183
mv 184,220,144
tr 220,4,26624
tr 222,4,32768
mv 184,216,136
tr 216,4,38912
tr 218,4,45056
mv 184,228,152
aw 180,176
aw 181,177
mv 184,224,160
aw 182,178
aw 183,179
mv 192,224,164
v_add_u32 v1, 0x8000, v1
v_add_u32 v2, 0x8000, v2
mv 192,228,156
v_add_u32 v3, 0x8000, v3
v_add_u32 v4, 0x8000, v4
v_mfma_f32_16x16x32_bf16 v[196:199], v[240:243], a[188:191], 0
s_waitcnt vmcnt(16) lgkmcnt(0)
s_barrier
v_mfma_f32_16x16x32_bf16 v[188:191], v[240:243], a[184:187], 0
rd 224,5,40960
rd 228,6,40960
mv 188,232,120
cv 184,184,185
cv 185,186,187
mv 188,236,128
aw 184,184
aw 185,185
mv 196,236,132
rd 236,5,41984
cv 192,192,193
mv 196,232,124
rd 232,6,41984
cv 193,194,195
mv 196,248,140
aw 188,192
aw 189,193
mv 196,252,148
mv 188,252,144
rd 252,5,43008
mv 188,248,136
s_add_u32 m0, s17, 114688
s_add_u32 s24, s21, 128
dl 17,4,24,0
mv 188,220,152
rd 248,6,43008
mv 188,216,160
mv 196,216,164
rd 216,5,47104
mv 196,220,156
dl 18,4,24,1024
s_waitcnt lgkmcnt(3)
mvc 40,224,72,32
rd 220,6,47104
s_nop 0
cv 186,188,189
mv 40,228,80
cv 187,190,191
aw 186,186
mvc 48,228,84,36
rd 228,5,48128
aw 187,187
mv 48,224,76
dl 17,8,24,2048
mv 48,236,92
rd 224,6,48128
cv 194,196,197
mv 48,232,100
cv 195,198,199
aw 190,194
mv 40,232,96
rd 232,5,49152
aw 191,195
mv 40,236,88
dl 18,8,24,3072
s_waitcnt lgkmcnt(3)
mv 40,252,104
rd 236,6,49152
mv 40,248,112
mv 48,248,116
rd 248,5,53248
mv 48,252,108
s_add_u32 s25, s19, 787584
rd 252,6,53248
mvc 44,216,72,32
s_nop 1
v_max3_f32 v20, v40, v41, v42
mv 44,220,80
mvc 52,220,84,36
rd 220,5,54272
v_max3_f32 v20, v20, v43, v48
mv 52,216,76
rd 216,6,54272
v_max3_f32 v20, v20, v49, v50
s_add_u32 m0, s16, 24576
s_waitcnt lgkmcnt(4)
mv 52,228,92
dn 13,60,25,0
mv 52,224,100
mv 44,224,96
rd 224,5,55296
mv 44,228,88
rd 228,6,55296
mv 44,232,104
dn 13,64,25,1024
mv 44,236,112
mv 52,236,116
rd 236,5,59392
mv 52,232,108
rd 232,6,59392
s_waitcnt lgkmcnt(4)
mvc 56,248,72,32
dn 13,68,25,2048
mv 56,252,80
v_max3_f32 v20, v20, v51, v44
v_max3_f32 v20, v20, v45, v46
mvc 64,252,84,36
rd 252,5,60416
v_max3_f32 v20, v20, v47, v52
mv 64,248,76
rd 248,6,60416
v_max3_f32 v20, v20, v53, v54
mv 64,220,92
mv 64,216,100
mv 56,216,96
rd 216,5,61440
v_add_u32 v5, 0xffffe000, v5
mv 56,220,88
rd 220,6,61440
v_add_u32 v6, 0xffffe000, v6
s_waitcnt lgkmcnt(4)
mv 56,224,104
s_add_u32 m0, s16, 27648
mv 56,228,112
dn 14,60,25,0
mv 64,228,116
mv 64,224,108
s_waitcnt vmcnt(14) lgkmcnt(0)
s_barrier
mvc 60,236,72,32
rd 244,7,40960
rd 240,7,43008
mv 60,232,80
dn 14,64,25,1024
mvc 68,232,84,36
tr 232,22,0
tr 234,22,6144
mv 68,236,76
rd 228,7,45056
rd 224,7,47104
mv 68,252,92
tr 236,23,0
tr 238,23,6144
mv 68,248,100
dn 14,68,25,2048
mv 60,248,96
tr 248,22,1024
tr 250,22,7168
mv 60,252,88
s_add_u32 s22, s21, 256
tr 252,23,1024
mv 60,216,104
tr 254,23,7168
v_max3_f32 v20, v20, v55, v56
mv 60,220,112
v_max3_f32 v20, v20, v57, v58
v_max3_f32 v20, v20, v59, v64
mv 68,220,116
tr 220,22,2048
tr 222,22,8192
mv 68,216,108
tr 216,23,2048
tr 218,23,8192
s_waitcnt lgkmcnt(8)
mvz 120,232,244
v_max3_f32 v20, v20, v65, v66
v_max3_f32 v20, v20, v67, v60
mvz 128,232,240
v_max3_f32 v20, v20, v61, v62
v_max3_f32 v20, v20, v63, v68
s_add_u32 m0, s17, 98304
mvz 136,232,228
dl 17,4,22,0
mvz 144,232,224
rd 232,8,40960
v_max3_f32 v20, v20, v69, v70
mvz 148,236,224
v_max_f32 v20, v20, v71
v_cmp_lt_f32 vcc, s27, v20
s_cbranch_vccz Lc17
s_swappc_b64 s[30:31], s[28:29]
Lc17:
mvz 140,236,228
v_exp_f32 v40, v40
mvz 132,236,240
dl 18,4,22,1024
mvz 124,236,244
rd 236,8,43008
v_exp_f32 v41, v41
s_waitcnt lgkmcnt(2)
mvz 152,248,244
v_exp_f32 v42, v42
mvz 160,248,240
v_exp_f32 v43, v43
dl 17,8,22,2048
mvz 168,248,228
v_exp_f32 v44, v44
mvz 176,248,224
s_add_u32 s23, s19, 1574016
rd 248,8,45056
mvz 180,252,224
v_exp_f32 v45, v45
mvz 172,252,228
dl 18,8,22,3072
mvz 164,252,240
v_exp_f32 v46, v46
mvz 156,252,244
rd 252,8,47104
v_exp_f32 v47, v47
mvz 184,220,244
cv 40,40,41
cv 41,42,43
mvz 192,220,240
cv 42,44,45
cv 43,46,47
mvz 200,220,228
v_exp_f32 v48, v48
mvz 208,220,224
tr 220,22,12288
tr 222,22,18432
mvz 212,216,224
tr 224,23,12288
tr 226,23,18432
mvz 204,216,228
tr 228,22,13312
tr 230,22,19456
s_add_u32 m0, s16, 49152
mvz 196,216,240
dn 13,60,23,0
mvz 188,216,244
tr 240,23,13312
tr 242,23,19456
tr 216,22,14336
tr 218,22,20480
tr 244,23,14336
tr 246,23,20480
v_exp_f32 v49, v49
v_exp_f32 v50, v50
v_exp_f32 v51, v51
dn 13,64,23,1024
s_waitcnt lgkmcnt(8)
mv 120,220,232
v_exp_f32 v52, v52
mv 128,220,236
v_exp_f32 v53, v53
mv 136,220,248
s_waitcnt vmcnt(16) lgkmcnt(0)
s_barrier
dn 13,68,23,2048
mv 144,220,252
tr 220,1,40960
tr 222,1,47104
mv 148,224,252
v_exp_f32 v54, v54
mv 140,224,248
v_exp_f32 v55, v55
mv 132,224,236
cv 48,48,49
cv 49,50,51
mv 124,224,232
tr 224,2,40960
tr 226,2,47104
mv 152,228,232
cv 50,52,53
cv 51,54,55
mv 160,228,236
v_exp_f32 v56, v56
mv 168,228,248
v_exp_f32 v57, v57
mv 176,228,252
s_add_u32 m0, s16, 52224
tr 228,3,40960
dn 14,60,23,0
mv 180,240,252
tr 230,3,47104
v_exp_f32 v58, v58
mv 172,240,248
v_exp_f32 v59, v59
mv 164,240,236
v_exp_f32 v60, v60
mv 156,240,232
dn 14,64,23,1024
mv 184,216,232
tr 240,4,40960
tr 242,4,47104
mv 192,216,236
v_exp_f32 v61, v61
mv 200,216,248
v_exp_f32 v62, v62
mv 208,216,252
dn 14,68,23,2048
s_add_u32 s24, s18, 516096
mv 212,244,252
tr 216,1,41984
tr 218,1,48128
mv 204,244,248
tr 252,2,41984
tr 254,2,48128
mv 196,244,236
tr 248,3,41984
tr 250,3,48128
mv 188,244,232
tr 236,4,41984
tr 238,4,48128
v_mfma_f32_16x16x32_bf16 a[244:247], a[240:243], v[40:43], a[244:247]
tr 244,1,43008
tr 246,1,49152
v_mfma_f32_16x16x32_bf16 a[248:251], a[240:243], v[48:51], a[248:251]
tr 232,2,43008
tr 234,2,49152
s_waitcnt lgkmcnt(12)
ma 0,220,40
v_exp_f32 v63, v63
ma 8,220,48
tr 220,3,43008
s_add_u32 m0, s16, 0
ma 4,224,40
dn 9,36,24,0
ma 12,224,48
tr 222,3,49152
tr 224,4,43008
ma 16,228,40
tr 226,4,49152
cv 56,56,57
ma 24,228,48
tr 228,1,53248
tr 230,1,59392
ma 20,240,40
dn 9,40,24,1024
ma 28,240,48
tr 240,2,53248
tr 242,2,59392
s_waitcnt lgkmcnt(7)
ma 32,216,40
cv 57,58,59
cv 58,60,61
ma 40,216,48
cv 59,62,63
v_exp_f32 v64, v64
dn 9,44,24,2048
ma 36,252,40
v_exp_f32 v65, v65
ma 44,252,48
v_exp_f32 v66, v66
ma 48,248,40
v_exp_f32 v67, v67
ma 56,248,48
v_exp_f32 v68, v68
ma 52,236,40
v_exp_f32 v69, v69
ma 60,236,48
v_exp_f32 v70, v70
ma 64,244,40
v_exp_f32 v71, v71
ma 72,244,48
cv 64,64,65
tr 216,3,53248
ma 68,232,40
tr 218,3,59392
cv 65,66,67
ma 76,232,48
s_add_u32 m0, s16, 3072
cv 66,68,69
dn 10,36,24,0
s_waitcnt lgkmcnt(2)
ma 80,220,40
cv 67,70,71
tr 252,4,53248
ma 88,220,48
tr 254,4,59392
tr 248,1,54272
ma 84,224,40
tr 250,1,60416
tr 236,2,54272
ma 92,224,48
dn 10,40,24,1024
v_mfma_f32_16x16x32_bf16 a[244:247], a[240:243], v[56:59], a[244:247]
tr 238,2,60416
tr 244,3,54272
v_mfma_f32_16x16x32_bf16 a[248:251], a[240:243], v[64:67], a[248:251]
tr 246,3,60416
tr 232,4,54272
ma 0,228,56
tr 234,4,60416
tr 220,1,55296
ma 8,228,64
dn 10,44,24,2048
ma 4,240,56
tr 222,1,61440
tr 224,2,55296
ma 12,240,64
tr 226,2,61440
tr 228,3,55296
s_waitcnt lgkmcnt(10)
ma 16,216,56
tr 230,3,61440
tr 240,4,55296
ma 24,216,64
tr 242,4,61440
v_add_u32 v1, 0xffffa000, v1
ma 20,252,56
v_add_u32 v2, 0xffffa000, v2
v_add_u32 v3, 0xffffa000, v3
ma 28,252,64
v_add_u32 v4, 0xffffa000, v4
ma 32,248,56
ma 40,248,64
s_waitcnt vmcnt(16) lgkmcnt(0)
s_barrier
ma 36,236,56
rd 216,7,24576
rd 252,7,26624
ma 44,236,64
rd 248,7,28672
rd 236,7,30720
ma 48,244,56
ma 56,244,64
tr 244,22,24576
tr 246,22,30720
ma 52,232,56
ma 60,232,64
tr 232,23,24576
tr 234,23,30720
ma 64,220,56
ma 72,220,64
tr 220,22,25600
tr 222,22,31744
ma 68,224,56
ma 76,224,64
s_add_u32 m0, s17, 131072
s_add_u32 s25, s21, 384
dl 17,4,25,0
ma 80,228,56
tr 224,23,25600
tr 226,23,31744
)"
R"(ma 88,228,64
tr 228,22,26624
tr 230,22,32768
ma 84,240,56
ma 92,240,64
dl 18,4,25,1024
s_waitcnt lgkmcnt(4)
mv 120,244,216
tr 240,23,26624
tr 242,23,32768
mv 128,244,252
mv 136,244,248
mv 144,244,236
dl 17,8,25,2048
mv 148,232,236
rd 244,8,24576
mv 140,232,248
mv 132,232,252
mv 124,232,216
dl 18,8,25,3072
mv 152,220,216
rd 232,8,26624
mv 160,220,252
mv 168,220,248
mv 176,220,236
s_add_u32 s22, s19, 2360448
rd 220,8,28672
s_waitcnt lgkmcnt(2)
mv 180,224,236
mv 172,224,248
mv 164,224,252
mv 156,224,216
rd 224,8,30720
s_add_u32 m0, s16, 73728
mv 184,228,216
dn 13,60,22,0
mv 192,228,252
mv 200,228,248
mv 208,228,236
tr 228,22,36864
tr 230,22,43008
mv 212,240,236
dn 13,64,22,1024
mv 204,240,248
tr 236,23,36864
tr 238,23,43008
mv 196,240,252
tr 248,22,37888
tr 250,22,44032
mv 188,240,216
tr 252,23,37888
tr 254,23,44032
tr 240,22,38912
tr 242,22,45056
dn 13,68,22,2048
tr 216,23,38912
tr 218,23,45056
v_add_u32 v22, 0x8000, v22
v_add_u32 v23, 0x8000, v23
s_waitcnt lgkmcnt(10)
mv 120,228,244
mv 128,228,232
mv 136,228,220
s_waitcnt vmcnt(13) lgkmcnt(0)
s_barrier
mv 144,228,224
rd 228,7,8192
mv 148,236,224
mv 140,236,220
s_add_u32 m0, s16, 76800
s_nop 0
dn 14,60,22,0
mv 132,236,232
mv 124,236,244
rd 236,7,10240
mv 152,248,244
mv 160,248,232
dn 14,64,22,1024
mv 168,248,220
mv 176,248,224
rd 248,7,12288
mv 180,252,224
mv 172,252,220
dn 14,68,22,2048
mv 164,252,232
mv 156,252,244
rd 252,7,14336
mv 184,240,244
mv 192,240,232
mv 200,240,220
mv 208,240,224
s_add_u32 s23, s18, 516096
tr 240,22,16384
tr 242,22,22528
mv 212,216,224
tr 224,23,16384
tr 226,23,22528
mv 204,216,220
tr 220,22,17408
tr 222,22,23552
s_add_u32 m0, s16, 24576
mv 196,216,232
dn 11,48,23,0
mv 188,216,244
tr 232,23,17408
tr 234,23,23552
tr 216,22,18432
tr 218,22,24576
tr 244,23,18432
tr 246,23,24576
dn 11,52,23,1024
s_waitcnt lgkmcnt(8)
mv 120,240,228
mv 128,240,236
mv 136,240,248
mv 144,240,252
dn 11,56,23,2048
mv 148,224,252
rd 240,8,8192
mv 140,224,248
mv 132,224,236
mv 124,224,228
rd 224,8,10240
s_waitcnt lgkmcnt(2)
mv 152,220,228
mv 160,220,236
mv 168,220,248
mv 176,220,252
rd 220,8,12288
mv 180,232,252
mv 172,232,248
mv 164,232,236
mv 156,232,228
rd 232,8,14336
mv 184,216,228
mv 192,216,236
mv 200,216,248
mv 208,216,252
s_add_u32 m0, s16, 27648
tr 216,22,28672
dn 12,48,23,0
tr 218,22,34816
mv 212,244,252
tr 252,23,28672
tr 254,23,34816
mv 204,244,248
tr 248,22,29696
tr 250,22,35840
mv 196,244,236
tr 236,23,29696
tr 238,23,35840
mv 188,244,228
dn 12,52,23,1024
tr 244,22,30720
tr 246,22,36864
tr 228,23,30720
tr 230,23,36864
s_waitcnt lgkmcnt(8)
mv 120,216,240
dn 12,56,23,2048
mv 128,216,224
s_waitcnt vmcnt(16) lgkmcnt(0)
s_barrier
mv 136,216,220
mv 144,216,232
rd 216,5,0
mv 148,252,232
mv 140,252,220
mv 132,252,224
mv 124,252,240
rd 252,6,0
mv 152,248,240
mv 160,248,224
mv 168,248,220
mv 176,248,232
rd 248,5,1024
mv 180,236,232
mv 172,236,220
mv 164,236,224
mv 156,236,240
rd 236,6,1024
mv 184,244,240
mv 192,244,224
mv 200,244,220
mv 208,244,232
s_add_u32 m0, s17, 114688
s_add_u32 s24, s21, 512
dl 17,4,24,0
mv 212,228,232
rd 244,5,2048
rd 232,6,2048
mv 204,228,220
rd 220,5,6144
mv 196,228,224
rd 224,6,6144
mv 188,228,240
dl 18,4,24,1024
s_waitcnt lgkmcnt(4)
mvc 40,216,72,32
rd 228,5,7168
rd 240,6,7168
mv 40,252,80
mvc 48,252,84,36
rd 252,5,8192
mv 48,216,76
dl 17,8,24,2048
mv 48,248,92
rd 216,6,8192
mv 48,236,100
mv 40,236,96
rd 236,5,12288
mv 40,248,88
dl 18,8,24,3072
s_waitcnt lgkmcnt(3)
mv 40,244,104
rd 248,6,12288
mv 40,232,112
mv 48,232,116
rd 232,5,13312
mv 48,244,108
s_add_u32 s25, s19, 3146880
rd 244,6,13312
mvc 44,220,72,32
s_nop 1
v_max3_f32 v20, v40, v41, v42
mv 44,224,80
mvc 52,224,84,36
rd 224,5,14336
v_max3_f32 v20, v20, v43, v48
mv 52,220,76
rd 220,6,14336
v_max3_f32 v20, v20, v49, v50
s_add_u32 m0, s16, 49152
mv 52,228,92
dn 13,60,25,0
mv 52,240,100
mv 44,240,96
rd 240,5,18432
mv 44,228,88
rd 228,6,18432
s_waitcnt lgkmcnt(4)
mv 44,252,104
dn 13,64,25,1024
mv 44,216,112
mv 52,216,116
rd 216,5,19456
mv 52,252,108
rd 252,6,19456
mvc 56,236,72,32
dn 13,68,25,2048
mv 56,248,80
s_nop 0
v_max3_f32 v20, v20, v51, v44
v_max3_f32 v20, v20, v45, v46
mvc 64,248,84,36
rd 248,5,20480
v_max3_f32 v20, v20, v47, v52
mv 64,236,76
rd 236,6,20480
v_max3_f32 v20, v20, v53, v54
mv 64,232,92
v_add_u32 v5, 0x8000, v5
v_add_u32 v6, 0x8000, v6
mv 64,244,100
mv 56,244,96
mv 56,232,88
s_waitcnt lgkmcnt(2)
mv 56,224,104
s_waitcnt vmcnt(13) lgkmcnt(0)
s_barrier
s_add_u32 m0, s16, 52224
mv 56,220,112
dn 14,60,25,0
mv 64,220,116
rd 244,7,40960
rd 232,7,43008
mv 64,224,108
rd 220,7,45056
rd 224,7,47104
mvc 60,240,72,32
v_max3_f32 v20, v20, v55, v56
v_max3_f32 v20, v20, v57, v58
mv 60,228,80
dn 14,64,25,1024
mvc 68,228,84,36
tr 228,22,40960
tr 230,22,47104
mv 68,240,76
tr 240,23,40960
tr 242,23,47104
mv 68,216,92
v_max3_f32 v20, v20, v59, v64
v_max3_f32 v20, v20, v65, v66
mv 68,252,100
dn 14,68,25,2048
mv 60,252,96
tr 252,22,41984
tr 254,22,48128
mv 60,216,88
s_add_u32 s22, s21, 640
tr 216,23,41984
mv 60,248,104
tr 218,23,48128
mv 60,236,112
mv 68,236,116
tr 236,22,43008
tr 238,22,49152
mv 68,248,108
tr 248,23,43008
tr 250,23,49152
s_waitcnt lgkmcnt(8)
mv 120,228,244
v_max3_f32 v20, v20, v67, v60
v_max3_f32 v20, v20, v61, v62
mv 128,228,232
s_nop 0
v_max3_f32 v20, v20, v63, v68
s_add_u32 m0, s17, 98304
mv 136,228,220
dl 17,4,22,0
mv 144,228,224
rd 228,8,40960
v_max3_f32 v20, v20, v69, v70
mv 148,240,224
v_max_f32 v20, v20, v71
v_cmp_lt_f32 vcc, s27, v20
s_cbranch_vccz Lc18
s_swappc_b64 s[30:31], s[28:29]
Lc18:
mv 140,240,220
v_exp_f32 v40, v40
mv 132,240,232
dl 18,4,22,1024
mv 124,240,244
rd 240,8,43008
v_exp_f32 v41, v41
s_waitcnt lgkmcnt(2)
mv 152,252,244
v_exp_f32 v42, v42
mv 160,252,232
v_exp_f32 v43, v43
dl 17,8,22,2048
mv 168,252,220
v_exp_f32 v44, v44
mv 176,252,224
s_add_u32 s23, s19, 3933312
rd 252,8,45056
mv 180,216,224
v_exp_f32 v45, v45
mv 172,216,220
dl 18,8,22,3072
mv 164,216,232
v_exp_f32 v46, v46
mv 156,216,244
rd 216,8,47104
v_exp_f32 v47, v47
mv 184,236,244
cv 40,40,41
cv 41,42,43
mv 192,236,232
cv 42,44,45
cv 43,46,47
mv 200,236,220
v_exp_f32 v48, v48
mv 208,236,224
tr 236,22,53248
tr 238,22,59392
mv 212,248,224
tr 224,23,53248
tr 226,23,59392
mv 204,248,220
tr 220,22,54272
tr 222,22,60416
s_add_u32 m0, s16, 0
mv 196,248,232
dn 13,60,23,0
mv 188,248,244
tr 232,23,54272
tr 234,23,60416
tr 248,22,55296
tr 250,22,61440
tr 244,23,55296
tr 246,23,61440
v_exp_f32 v49, v49
v_exp_f32 v50, v50
v_exp_f32 v51, v51
dn 13,64,23,1024
s_waitcnt lgkmcnt(8)
mv 120,236,228
v_exp_f32 v52, v52
mv 128,236,240
v_exp_f32 v53, v53
mv 136,236,252
s_waitcnt vmcnt(16) lgkmcnt(0)
s_barrier
dn 13,68,23,2048
mv 144,236,216
tr 236,1,16384
tr 238,1,22528
mv 148,224,216
v_exp_f32 v54, v54
mv 140,224,252
v_exp_f32 v55, v55
mv 132,224,240
cv 48,48,49
cv 49,50,51
mv 124,224,228
tr 224,2,16384
tr 226,2,22528
mv 152,220,228
cv 50,52,53
cv 51,54,55
mv 160,220,240
v_exp_f32 v56, v56
mv 168,220,252
v_exp_f32 v57, v57
mv 176,220,216
s_add_u32 m0, s16, 3072
tr 220,3,16384
dn 14,60,23,0
mv 180,232,216
tr 222,3,22528
v_exp_f32 v58, v58
mv 172,232,252
v_exp_f32 v59, v59
mv 164,232,240
v_exp_f32 v60, v60
mv 156,232,228
dn 14,64,23,1024
mv 184,248,228
tr 232,4,16384
tr 234,4,22528
mv 192,248,240
v_exp_f32 v61, v61
mv 200,248,252
v_exp_f32 v62, v62
mv 208,248,216
dn 14,68,23,2048
s_add_u32 s24, s18, 540672
mv 212,244,216
tr 248,1,17408
tr 250,1,23552
mv 204,244,252
tr 216,2,17408
tr 218,2,23552
mv 196,244,240
tr 252,3,17408
tr 254,3,23552
mv 188,244,228
tr 240,4,17408
tr 242,4,23552
v_mfma_f32_16x16x32_bf16 a[244:247], a[240:243], v[40:43], a[244:247]
tr 244,1,18432
tr 246,1,24576
v_mfma_f32_16x16x32_bf16 a[248:251], a[240:243], v[48:51], a[248:251]
tr 228,2,18432
tr 230,2,24576
s_waitcnt lgkmcnt(12)
ma 0,236,40
v_exp_f32 v63, v63
ma 8,236,48
tr 236,3,18432
s_add_u32 m0, s16, 73728
ma 4,224,40
dn 9,36,24,0
ma 12,224,48
tr 238,3,24576
tr 224,4,18432
ma 16,220,40
tr 226,4,24576
cv 56,56,57
ma 24,220,48
tr 220,1,28672
tr 222,1,34816
ma 20,232,40
dn 9,40,24,1024
ma 28,232,48
tr 232,2,28672
tr 234,2,34816
s_waitcnt lgkmcnt(7)
ma 32,248,40
cv 57,58,59
cv 58,60,61
ma 40,248,48
cv 59,62,63
v_exp_f32 v64, v64
dn 9,44,24,2048
ma 36,216,40
v_exp_f32 v65, v65
ma 44,216,48
v_exp_f32 v66, v66
ma 48,252,40
v_exp_f32 v67, v67
ma 56,252,48
v_exp_f32 v68, v68
ma 52,240,40
v_exp_f32 v69, v69
ma 60,240,48
v_exp_f32 v70, v70
ma 64,244,40
v_exp_f32 v71, v71
ma 72,244,48
cv 64,64,65
tr 248,3,28672
ma 68,228,40
tr 250,3,34816
cv 65,66,67
ma 76,228,48
s_add_u32 m0, s16, 76800
cv 66,68,69
dn 10,36,24,0
s_waitcnt lgkmcnt(2)
ma 80,236,40
cv 67,70,71
tr 216,4,28672
ma 88,236,48
tr 218,4,34816
tr 252,1,29696
ma 84,224,40
tr 254,1,35840
tr 240,2,29696
ma 92,224,48
dn 10,40,24,1024
v_mfma_f32_16x16x32_bf16 a[244:247], a[240:243], v[56:59], a[244:247]
tr 242,2,35840
tr 244,3,29696
v_mfma_f32_16x16x32_bf16 a[248:251], a[240:243], v[64:67], a[248:251]
tr 246,3,35840
tr 228,4,29696
ma 0,220,56
tr 230,4,35840
tr 236,1,30720
ma 8,220,64
dn 10,44,24,2048
ma 4,232,56
tr 238,1,36864
tr 224,2,30720
ma 12,232,64
tr 226,2,36864
tr 220,3,30720
s_waitcnt lgkmcnt(10)
ma 16,248,56
tr 222,3,36864
tr 232,4,30720
ma 24,248,64
tr 234,4,36864
ma 20,216,56
ma 28,216,64
ma 32,252,56
ma 40,252,64
s_waitcnt vmcnt(16) lgkmcnt(0)
s_barrier
ma 36,240,56
rd 248,7,24576
rd 216,7,26624
ma 44,240,64
rd 252,7,28672
rd 240,7,30720
ma 48,244,56
ma 56,244,64
tr 244,22,16384
tr 246,22,22528
ma 52,228,56
ma 60,228,64
tr 228,23,16384
tr 230,23,22528
ma 64,236,56
ma 72,236,64
tr 236,22,17408
tr 238,22,23552
ma 68,224,56
ma 76,224,64
s_add_u32 m0, s17, 131072
s_add_u32 s25, s21, 768
dl 17,4,25,0
ma 80,220,56
tr 224,23,17408
tr 226,23,23552
ma 88,220,64
tr 220,22,18432
tr 222,22,24576
ma 84,232,56
ma 92,232,64
dl 18,4,25,1024
s_waitcnt lgkmcnt(4)
mv 120,244,248
tr 232,23,18432
tr 234,23,24576
mv 128,244,216
mv 136,244,252
mv 144,244,240
dl 17,8,25,2048
mv 148,228,240
rd 244,8,24576
mv 140,228,252
mv 132,228,216
mv 124,228,248
dl 18,8,25,3072
mv 152,236,248
rd 228,8,26624
mv 160,236,216
mv 168,236,252
mv 176,236,240
s_add_u32 s22, s19, 4719744
rd 236,8,28672
s_waitcnt lgkmcnt(2)
mv 180,224,240
mv 172,224,252
mv 164,224,216
mv 156,224,248
rd 224,8,30720
s_add_u32 m0, s16, 24576
mv 184,220,248
dn 13,60,22,0
mv 192,220,216
mv 200,220,252
mv 208,220,240
tr 220,22,28672
tr 222,22,34816
mv 212,232,240
dn 13,64,22,1024
mv 204,232,252
tr 240,23,28672
tr 242,23,34816
mv 196,232,216
tr 252,22,29696
tr 254,22,35840
mv 188,232,248
tr 216,23,29696
tr 218,23,35840
tr 232,22,30720
tr 234,22,36864
dn 13,68,22,2048
tr 248,23,30720
tr 250,23,36864
v_add_u32 v22, 0xffff8000, v22
v_add_u32 v23, 0xffff8000, v23
s_waitcnt lgkmcnt(10)
mv 120,220,244
mv 128,220,228
mv 136,220,236
s_waitcnt vmcnt(13) lgkmcnt(0)
s_barrier
mv 144,220,224
rd 220,7,8192
mv 148,240,224
mv 140,240,236
s_add_u32 m0, s16, 27648
s_nop 0
dn 14,60,22,0
mv 132,240,228
mv 124,240,244
rd 240,7,10240
mv 152,252,244
mv 160,252,228
dn 14,64,22,1024
mv 168,252,236
mv 176,252,224
rd 252,7,12288
mv 180,216,224
mv 172,216,236
dn 14,68,22,2048
mv 164,216,228
mv 156,216,244
rd 216,7,14336
mv 184,232,244
mv 192,232,228
mv 200,232,236
mv 208,232,224
s_add_u32 s23, s18, 540672
tr 232,22,0
tr 234,22,6144
mv 212,248,224
tr 224,23,0
tr 226,23,6144
mv 204,248,236
tr 236,22,1024
tr 238,22,7168
s_add_u32 m0, s16, 49152
mv 196,248,228
dn 11,48,23,0
mv 188,248,244
tr 228,23,1024
tr 230,23,7168
tr 248,22,2048
tr 250,22,8192
tr 244,23,2048
tr 246,23,8192
dn 11,52,23,1024
s_waitcnt lgkmcnt(8)
mv 120,232,220
mv 128,232,240
mv 136,232,252
mv 144,232,216
dn 11,56,23,2048
mv 148,224,216
rd 232,8,8192
mv 140,224,252
mv 132,224,240
mv 124,224,220
rd 224,8,10240
s_waitcnt lgkmcnt(2)
mv 152,236,220
mv 160,236,240
mv 168,236,252
mv 176,236,216
rd 236,8,12288
mv 180,228,216
mv 172,228,252
mv 164,228,240
mv 156,228,220
rd 228,8,14336
mv 184,248,220
mv 192,248,240
mv 200,248,252
mv 208,248,216
s_add_u32 m0, s16, 52224
tr 248,22,12288
dn 12,48,23,0
tr 250,22,18432
mv 212,244,216
tr 216,23,12288
tr 218,23,18432
mv 204,244,252
tr 252,22,13312
tr 254,22,19456
mv 196,244,240
tr 240,23,13312
tr 242,23,19456
mv 188,244,220
dn 12,52,23,1024
tr 244,22,14336
tr 246,22,20480
tr 220,23,14336
tr 222,23,20480
s_waitcnt lgkmcnt(8)
mv 120,248,232
dn 12,56,23,2048
mv 128,248,224
s_waitcnt vmcnt(16) lgkmcnt(0)
s_barrier
mv 136,248,236
mv 144,248,228
rd 248,5,40960
mv 148,216,228
mv 140,216,236
mv 132,216,224
mv 124,216,232
rd 216,6,40960
mv 152,252,232
mv 160,252,224
mv 168,252,236
mv 176,252,228
rd 252,5,41984
mv 180,240,228
mv 172,240,236
mv 164,240,224
mv 156,240,232
rd 240,6,41984
mv 184,244,232
mv 192,244,224
mv 200,244,236
mv 208,244,228
s_add_u32 m0, s17, 114688
s_add_u32 s24, s21, 896
dl 17,4,24,0
mv 212,220,228
rd 244,5,43008
rd 228,6,43008
mv 204,220,236
rd 236,5,47104
mv 196,220,224
rd 224,6,47104
mv 188,220,232
dl 18,4,24,1024
s_waitcnt lgkmcnt(4)
mvc 40,248,72,32
rd 220,5,48128
rd 232,6,48128
mv 40,216,80
mvc 48,216,84,36
rd 216,5,49152
mv 48,248,76
dl 17,8,24,2048
mv 48,252,92
rd 248,6,49152
mv 48,240,100
mv 40,240,96
rd 240,5,53248
mv 40,252,88
dl 18,8,24,3072
s_waitcnt lgkmcnt(3)
mv 40,244,104
rd 252,6,53248
mv 40,228,112
mv 48,228,116
rd 228,5,54272
mv 48,244,108
s_add_u32 s25, s19, 5506176
rd 244,6,54272
mvc 44,236,72,32
s_nop 1
v_max3_f32 v20, v40, v41, v42
mv 44,224,80
mvc 52,224,84,36
rd 224,5,55296
v_max3_f32 v20, v20, v43, v48
mv 52,236,76
rd 236,6,55296
v_max3_f32 v20, v20, v49, v50
s_add_u32 m0, s16, 0
mv 52,220,92
dn 13,60,25,0
mv 52,232,100
mv 44,232,96
rd 232,5,59392
mv 44,220,88
rd 220,6,59392
s_waitcnt lgkmcnt(4)
mv 44,216,104
dn 13,64,25,1024
mv 44,248,112
mv 52,248,116
rd 248,5,60416
mv 52,216,108
rd 216,6,60416
mvc 56,240,72,32
dn 13,68,25,2048
mv 56,252,80
s_nop 0
v_max3_f32 v20, v20, v51, v44
v_max3_f32 v20, v20, v45, v46
mvc 64,252,84,36
rd 252,5,61440
v_max3_f32 v20, v20, v47, v52
mv 64,240,76
rd 240,6,61440
v_max3_f32 v20, v20, v53, v54
mv 64,228,92
v_add_u32 v5, 0xffff8000, v5
v_add_u32 v6, 0xffff8000, v6
mv 64,244,100
mv 56,244,96
mv 56,228,88
s_waitcnt lgkmcnt(2)
mv 56,224,104
s_waitcnt vmcnt(13) lgkmcnt(0)
s_barrier
s_add_u32 m0, s16, 3072
mv 56,236,112
dn 14,60,25,0
mv 64,236,116
rd 244,7,40960
rd 228,7,43008
mv 64,224,108
rd 236,7,45056
rd 224,7,47104
mvc 60,232,72,32
v_max3_f32 v20, v20, v55, v56
v_max3_f32 v20, v20, v57, v58
mv 60,220,80
dn 14,64,25,1024
mvc 68,220,84,36
tr 220,22,24576
tr 222,22,30720
mv 68,232,76
tr 232,23,24576
tr 234,23,30720
mv 68,248,92
v_max3_f32 v20, v20, v59, v64
v_max3_f32 v20, v20, v65, v66
mv 68,216,100
dn 14,68,25,2048
mv 60,216,96
tr 216,22,25600
tr 218,22,31744
mv 60,248,88
s_add_u32 s22, s21, 1024
tr 248,23,25600
mv 60,252,104
tr 250,23,31744
mv 60,240,112
mv 68,240,116
tr 240,22,26624
tr 242,22,32768
mv 68,252,108
tr 252,23,26624
tr 254,23,32768
s_waitcnt lgkmcnt(8)
mv 120,220,244
v_max3_f32 v20, v20, v67, v60
v_max3_f32 v20, v20, v61, v62
mv 128,220,228
s_nop 0
v_max3_f32 v20, v20, v63, v68
s_add_u32 m0, s17, 98304
mv 136,220,236
dl 17,4,22,0
mv 144,220,224
rd 220,8,40960
v_max3_f32 v20, v20, v69, v70
mv 148,232,224
v_max_f32 v20, v20, v71
v_cmp_lt_f32 vcc, s27, v20
s_cbranch_vccz Lc19
s_swappc_b64 s[30:31], s[28:29]
Lc19:
mv 140,232,236
v_exp_f32 v40, v40
mv 132,232,228
dl 18,4,22,1024
mv 124,232,244
rd 232,8,43008
v_exp_f32 v41, v41
s_waitcnt lgkmcnt(2)
mv 152,216,244
v_exp_f32 v42, v42
mv 160,216,228
v_exp_f32 v43, v43
dl 17,8,22,2048
mv 168,216,236
v_exp_f32 v44, v44
mv 176,216,224
s_add_u32 s23, s19, 6292608
rd 216,8,45056
mv 180,248,224
v_exp_f32 v45, v45
mv 172,248,236
dl 18,8,22,3072
mv 164,248,228
v_exp_f32 v46, v46
mv 156,248,244
rd 248,8,47104
v_exp_f32 v47, v47
mv 184,240,244
cv 40,40,41
cv 41,42,43
mv 192,240,228
cv 42,44,45
cv 43,46,47
mv 200,240,236
v_exp_f32 v48, v48
mv 208,240,224
tr 240,22,36864
tr 242,22,43008
mv 212,252,224
tr 224,23,36864
tr 226,23,43008
mv 204,252,236
tr 236,22,37888
tr 238,22,44032
s_add_u32 m0, s16, 73728
mv 196,252,228
dn 13,60,23,0
mv 188,252,244
tr 228,23,37888
tr 230,23,44032
tr 252,22,38912
tr 254,22,45056
tr 244,23,38912
tr 246,23,45056
v_exp_f32 v49, v49
v_exp_f32 v50, v50
v_exp_f32 v51, v51
dn 13,64,23,1024
s_waitcnt lgkmcnt(8)
mv 120,240,220
v_exp_f32 v52, v52
mv 128,240,232
v_exp_f32 v53, v53
mv 136,240,216
s_waitcnt vmcnt(16) lgkmcnt(0)
s_barrier
dn 13,68,23,2048
mv 144,240,248
tr 240,1,40960
tr 242,1,47104
mv 148,224,248
v_exp_f32 v54, v54
mv 140,224,216
v_exp_f32 v55, v55
mv 132,224,232
cv 48,48,49
cv 49,50,51
mv 124,224,220
tr 224,2,40960
tr 226,2,47104
mv 152,236,220
cv 50,52,53
cv 51,54,55
mv 160,236,232
v_exp_f32 v56, v56
mv 168,236,216
v_exp_f32 v57, v57
mv 176,236,248
s_add_u32 m0, s16, 76800
tr 236,3,40960
dn 14,60,23,0
mv 180,228,248
tr 238,3,47104
v_exp_f32 v58, v58
mv 172,228,216
v_exp_f32 v59, v59
mv 164,228,232
v_exp_f32 v60, v60
mv 156,228,220
dn 14,64,23,1024
mv 184,252,220
tr 228,4,40960
tr 230,4,47104
mv 192,252,232
v_exp_f32 v61, v61
mv 200,252,216
v_exp_f32 v62, v62
mv 208,252,248
dn 14,68,23,2048
s_add_u32 s24, s18, 565248
mv 212,244,248
tr 252,1,41984
tr 254,1,48128
mv 204,244,216
tr 248,2,41984
tr 250,2,48128
mv 196,244,232
tr 216,3,41984
tr 218,3,48128
mv 188,244,220
tr 232,4,41984
tr 234,4,48128
v_mfma_f32_16x16x32_bf16 a[244:247], a[240:243], v[40:43], a[244:247]
tr 244,1,43008
tr 246,1,49152
v_mfma_f32_16x16x32_bf16 a[248:251], a[240:243], v[48:51], a[248:251]
tr 220,2,43008
tr 222,2,49152
s_waitcnt lgkmcnt(12)
ma 0,240,40
v_exp_f32 v63, v63
ma 8,240,48
tr 240,3,43008
s_add_u32 m0, s16, 24576
ma 4,224,40
dn 9,36,24,0
ma 12,224,48
tr 242,3,49152
tr 224,4,43008
ma 16,236,40
tr 226,4,49152
cv 56,56,57
ma 24,236,48
tr 236,1,53248
tr 238,1,59392
ma 20,228,40
dn 9,40,24,1024
ma 28,228,48
tr 228,2,53248
tr 230,2,59392
s_waitcnt lgkmcnt(7)
ma 32,252,40
cv 57,58,59
cv 58,60,61
ma 40,252,48
cv 59,62,63
v_exp_f32 v64, v64
dn 9,44,24,2048
ma 36,248,40
v_exp_f32 v65, v65
ma 44,248,48
v_exp_f32 v66, v66
ma 48,216,40
v_exp_f32 v67, v67
ma 56,216,48
v_exp_f32 v68, v68
ma 52,232,40
v_exp_f32 v69, v69
ma 60,232,48
v_exp_f32 v70, v70
ma 64,244,40
v_exp_f32 v71, v71
ma 72,244,48
cv 64,64,65
tr 252,3,53248
ma 68,220,40
tr 254,3,59392
cv 65,66,67
ma 76,220,48
s_add_u32 m0, s16, 27648
cv 66,68,69
dn 10,36,24,0
s_waitcnt lgkmcnt(2)
ma 80,240,40
cv 67,70,71
tr 248,4,53248
ma 88,240,48
tr 250,4,59392
tr 216,1,54272
ma 84,224,40
tr 218,1,60416
tr 232,2,54272
ma 92,224,48
dn 10,40,24,1024
v_mfma_f32_16x16x32_bf16 a[244:247], a[240:243], v[56:59], a[244:247]
tr 234,2,60416
tr 244,3,54272
v_mfma_f32_16x16x32_bf16 a[248:251], a[240:243], v[64:67], a[248:251]
tr 246,3,60416
tr 220,4,54272
ma 0,236,56
tr 222,4,60416
tr 240,1,55296
ma 8,236,64
dn 10,44,24,2048
ma 4,228,56
tr 242,1,61440
tr 224,2,55296
ma 12,228,64
tr 226,2,61440
tr 236,3,55296
s_waitcnt lgkmcnt(10)
ma 16,252,56
tr 238,3,61440
tr 228,4,55296
ma 24,252,64
tr 230,4,61440
v_add_u32 v1, 0xffffe000, v1
ma 20,248,56
v_add_u32 v2, 0xffffe000, v2
v_add_u32 v3, 0xffffe000, v3
ma 28,248,64
v_add_u32 v4, 0xffffe000, v4
ma 32,216,56
ma 40,216,64
s_waitcnt vmcnt(16) lgkmcnt(0)
s_barrier
ma 36,232,56
rd 252,7,24576
rd 248,7,26624
ma 44,232,64
rd 216,7,28672
rd 232,7,30720
ma 48,244,56
ma 56,244,64
tr 244,22,0
tr 246,22,6144
ma 52,220,56
ma 60,220,64
tr 220,23,0
tr 222,23,6144
ma 64,240,56
ma 72,240,64
tr 240,22,1024
tr 242,22,7168
ma 68,224,56
ma 76,224,64
s_add_u32 m0, s17, 131072
s_add_u32 s25, s21, 1152
dl 17,4,25,0
ma 80,236,56
tr 224,23,1024
tr 226,23,7168
ma 88,236,64
tr 236,22,2048
tr 238,22,8192
ma 84,228,56
ma 92,228,64
dl 18,4,25,1024
s_waitcnt lgkmcnt(4)
mv 120,244,252
tr 228,23,2048
tr 230,23,8192
mv 128,244,248
mv 136,244,216
mv 144,244,232
dl 17,8,25,2048
mv 148,220,232
rd 244,8,24576
mv 140,220,216
mv 132,220,248
mv 124,220,252
dl 18,8,25,3072
mv 152,240,252
rd 220,8,26624
mv 160,240,248
mv 168,240,216
mv 176,240,232
s_add_u32 s22, s19, 7079040
rd 240,8,28672
s_waitcnt lgkmcnt(2)
mv 180,224,232
mv 172,224,216
mv 164,224,248
mv 156,224,252
rd 224,8,30720
s_add_u32 m0, s16, 49152
mv 184,236,252
dn 13,60,22,0
mv 192,236,248
mv 200,236,216
mv 208,236,232
tr 236,22,12288
tr 238,22,18432
mv 212,228,232
dn 13,64,22,1024
mv 204,228,216
tr 232,23,12288
tr 234,23,18432
mv 196,228,248
tr 216,22,13312
tr 218,22,19456
mv 188,228,252
tr 248,23,13312
tr 250,23,19456
tr 228,22,14336
tr 230,22,20480
dn 13,68,22,2048
tr 252,23,14336
tr 254,23,20480
v_add_u32 v22, 0x8000, v22
v_add_u32 v23, 0x8000, v23
s_waitcnt lgkmcnt(10)
mv 120,236,244
mv 128,236,220
mv 136,236,240
s_waitcnt vmcnt(13) lgkmcnt(0)
s_barrier
mv 144,236,224
rd 236,7,8192
mv 148,232,224
mv 140,232,240
s_add_u32 m0, s16, 52224
s_nop 0
dn 14,60,22,0
mv 132,232,220
mv 124,232,244
rd 232,7,10240
mv 152,216,244
mv 160,216,220
dn 14,64,22,1024
mv 168,216,240
mv 176,216,224
rd 216,7,12288
mv 180,248,224
mv 172,248,240
dn 14,68,22,2048
mv 164,248,220
mv 156,248,244
rd 248,7,14336
mv 184,228,244
mv 192,228,220
mv 200,228,240
mv 208,228,224
s_add_u32 s23, s18, 565248
tr 228,22,40960
tr 230,22,47104
mv 212,252,224
tr 224,23,40960
tr 226,23,47104
mv 204,252,240
tr 240,22,41984
tr 242,22,48128
s_add_u32 m0, s16, 0
mv 196,252,220
dn 11,48,23,0
mv 188,252,244
tr 220,23,41984
tr 222,23,48128
tr 252,22,43008
tr 254,22,49152
tr 244,23,43008
tr 246,23,49152
dn 11,52,23,1024
s_waitcnt lgkmcnt(8)
mv 120,228,236
mv 128,228,232
mv 136,228,216
mv 144,228,248
dn 11,56,23,2048
mv 148,224,248
rd 228,8,8192
mv 140,224,216
mv 132,224,232
mv 124,224,236
rd 224,8,10240
s_waitcnt lgkmcnt(2)
mv 152,240,236
mv 160,240,232
mv 168,240,216
mv 176,240,248
rd 240,8,12288
mv 180,220,248
mv 172,220,216
mv 164,220,232
mv 156,220,236
rd 220,8,14336
mv 184,252,236
mv 192,252,232
mv 200,252,216
mv 208,252,248
s_add_u32 m0, s16, 3072
tr 252,22,53248
dn 12,48,23,0
tr 254,22,59392
mv 212,244,248
tr 248,23,53248
tr 250,23,59392
mv 204,244,216
tr 216,22,54272
tr 218,22,60416
mv 196,244,232
tr 232,23,54272
tr 234,23,60416
mv 188,244,236
dn 12,52,23,1024
tr 244,22,55296
tr 246,22,61440
tr 236,23,55296
tr 238,23,61440
s_waitcnt lgkmcnt(8)
mv 120,252,228
dn 12,56,23,2048
mv 128,252,224
s_waitcnt vmcnt(16) lgkmcnt(0)
s_barrier
mv 136,252,240
mv 144,252,220
rd 252,5,24576
mv 148,248,220
mv 140,248,240
mv 132,248,224
mv 124,248,228
rd 248,6,24576
mv 152,216,228
mv 160,216,224
mv 168,216,240
mv 176,216,220
rd 216,5,25600
mv 180,232,220
mv 172,232,240
mv 164,232,224
mv 156,232,228
rd 232,6,25600
mv 184,244,228
mv 192,244,224
mv 200,244,240
mv 208,244,220
s_add_u32 m0, s16, 73728
s_add_u32 s24, s18, 589824
dn 9,36,24,0
mv 212,236,220
rd 244,5,26624
rd 220,6,26624
mv 204,236,240
rd 240,5,30720
mv 196,236,224
rd 224,6,30720
mv 188,236,228
dn 9,40,24,1024
s_waitcnt lgkmcnt(4)
mvc 40,252,72,32
rd 236,5,31744
rd 228,6,31744
mv 40,248,80
mvc 48,248,84,36
rd 248,5,32768
mv 48,252,76
dn 9,44,24,2048
mv 48,216,92
rd 252,6,32768
mv 48,232,100
mv 40,232,96
rd 232,5,36864
mv 40,216,88
rd 216,6,36864
s_waitcnt lgkmcnt(4)
mv 40,244,104
mv 40,220,112
mv 48,220,116
rd 220,5,37888
mv 48,244,108
rd 244,6,37888
mvc 44,240,72,32
s_nop 2
v_max3_f32 v20, v40, v41, v42
mv 44,224,80
mvc 52,224,84,36
rd 224,5,38912
v_max3_f32 v20, v20, v43, v48
mv 52,240,76
rd 240,6,38912
v_max3_f32 v20, v20, v49, v50
mv 52,236,92
mv 52,228,100
mv 44,228,96
rd 228,5,43008
mv 44,236,88
s_add_u32 m0, s16, 76800
rd 236,6,43008
dn 10,36,24,0
s_waitcnt lgkmcnt(4)
mv 44,248,104
mv 44,252,112
mv 52,252,116
rd 252,5,44032
mv 52,248,108
dn 10,40,24,1024
mvc 56,232,72,32
rd 248,6,44032
s_nop 1
v_max3_f32 v20, v20, v51, v44
mv 56,216,80
v_max3_f32 v20, v20, v45, v46
mvc 64,216,84,36
rd 216,5,45056
v_max3_f32 v20, v20, v47, v52
mv 64,232,76
dn 10,44,24,2048
mv 64,220,92
rd 232,6,45056
v_max3_f32 v20, v20, v53, v54
mv 64,244,100
v_add_u32 v5, 0x8000, v5
v_add_u32 v6, 0x8000, v6
mv 56,244,96
mv 56,220,88
s_waitcnt lgkmcnt(2)
mv 56,224,104
s_waitcnt vmcnt(12) lgkmcnt(0)
s_barrier
mv 56,240,112
rd 244,7,40960
rd 220,7,43008
mv 64,240,116
rd 240,7,45056
mv 64,224,108
rd 224,7,47104
v_mbcnt_lo_u32_b32 v7, -1, 0
mvc 60,228,72,32
v_max3_f32 v20, v20, v55, v56
v_max3_f32 v20, v20, v57, v58
mv 60,236,80
v_mbcnt_hi_u32_b32 v7, -1, v7
v_lshlrev_b32 v7, 4, v7
mvc 68,236,84,36
tr 236,22,16384
tr 238,22,22528
mv 68,228,76
tr 228,23,16384
tr 230,23,22528
mv 68,252,92
v_max3_f32 v20, v20, v59, v64
v_max3_f32 v20, v20, v65, v66
mv 68,248,100
mv 60,248,96
tr 248,22,17408
tr 250,22,23552
mv 60,252,88
s_add_u32 m0, s16, 24576
s_add_u32 s25, s20, 491520
dl 15,72,25,0
mv 60,216,104
tr 252,23,17408
tr 254,23,23552
mv 60,232,112
mv 68,232,116
tr 232,22,18432
tr 234,22,24576
mv 68,216,108
dl 15,76,25,1024
s_waitcnt lgkmcnt(6)
mv 120,236,244
tr 216,23,18432
tr 218,23,24576
mv 128,236,220
v_max3_f32 v20, v20, v67, v60
v_max3_f32 v20, v20, v61, v62
mv 136,236,240
v_max3_f32 v20, v20, v63, v68
v_max3_f32 v20, v20, v69, v70
mv 144,236,224
dl 15,80,25,2048
mv 148,228,224
rd 236,8,40960
v_max_f32 v20, v20, v71
mv 140,228,240
v_cmp_lt_f32 vcc, s27, v20
s_cbranch_vccz Lc20
s_swappc_b64 s[30:31], s[28:29]
Lc20:
mv 132,228,220
v_exp_f32 v40, v40
mv 124,228,244
rd 228,8,43008
v_exp_f32 v41, v41
s_waitcnt lgkmcnt(2)
mv 152,248,244
v_exp_f32 v42, v42
mv 160,248,220
v_exp_f32 v43, v43
mv 168,248,240
v_exp_f32 v44, v44
)"
R"(mv 176,248,224
rd 248,8,45056
v_exp_f32 v45, v45
mv 180,252,224
v_exp_f32 v46, v46
mv 172,252,240
v_exp_f32 v47, v47
mv 164,252,220
cv 40,40,41
cv 41,42,43
mv 156,252,244
rd 252,8,47104
cv 42,44,45
mv 184,232,244
cv 43,46,47
v_exp_f32 v48, v48
mv 192,232,220
v_exp_f32 v49, v49
mv 200,232,240
v_exp_f32 v50, v50
mv 208,232,224
s_add_u32 m0, s16, 27648
tr 232,22,28672
dl 16,72,25,0
tr 234,22,34816
mv 212,216,224
tr 224,23,28672
tr 226,23,34816
mv 204,216,240
tr 240,22,29696
tr 242,22,35840
mv 196,216,220
tr 220,23,29696
tr 222,23,35840
mv 188,216,244
dl 16,76,25,1024
tr 216,22,30720
tr 218,22,36864
tr 244,23,30720
tr 246,23,36864
v_exp_f32 v51, v51
v_exp_f32 v52, v52
v_exp_f32 v53, v53
v_exp_f32 v54, v54
v_exp_f32 v55, v55
dl 16,80,25,2048
s_waitcnt lgkmcnt(6)
mv 120,232,236
cv 48,48,49
cv 49,50,51
mv 128,232,228
s_waitcnt vmcnt(12) lgkmcnt(0)
s_barrier
mv 136,232,248
cv 50,52,53
cv 51,54,55
mv 144,232,252
tr 232,1,0
tr 234,1,6144
mv 148,224,252
v_exp_f32 v56, v56
mv 140,224,248
v_exp_f32 v57, v57
mv 132,224,228
v_exp_f32 v58, v58
mv 124,224,236
tr 224,2,0
tr 226,2,6144
mv 152,240,236
v_exp_f32 v59, v59
mv 160,240,228
v_exp_f32 v60, v60
mv 168,240,248
v_exp_f32 v61, v61
mv 176,240,252
tr 240,3,0
tr 242,3,6144
mv 180,220,252
v_exp_f32 v62, v62
mv 172,220,248
v_exp_f32 v63, v63
mv 164,220,228
cv 56,56,57
cv 57,58,59
mv 156,220,236
tr 220,4,0
tr 222,4,6144
mv 184,216,236
cv 58,60,61
cv 59,62,63
mv 192,216,228
v_exp_f32 v64, v64
mv 200,216,248
v_exp_f32 v65, v65
mv 208,216,252
s_add_u32 m0, s16, 49152
s_add_u32 s22, s18, 589824
dn 11,48,22,0
mv 212,244,252
tr 216,1,1024
tr 218,1,7168
mv 204,244,248
tr 252,2,1024
tr 254,2,7168
mv 196,244,228
tr 248,3,1024
tr 250,3,7168
mv 188,244,236
dn 11,52,22,1024
v_mfma_f32_16x16x32_bf16 a[244:247], a[240:243], v[40:43], a[244:247]
tr 228,4,1024
tr 230,4,7168
v_mfma_f32_16x16x32_bf16 a[248:251], a[240:243], v[48:51], a[248:251]
tr 244,1,2048
tr 246,1,8192
s_waitcnt lgkmcnt(10)
ma 0,232,40
tr 236,2,2048
tr 238,2,8192
ma 8,232,48
dn 11,56,22,2048
ma 4,224,40
tr 232,3,2048
tr 234,3,8192
ma 12,224,48
tr 224,4,2048
tr 226,4,8192
ma 16,240,40
v_exp_f32 v66, v66
ma 24,240,48
tr 240,1,12288
tr 242,1,18432
ma 20,220,40
v_exp_f32 v67, v67
ma 28,220,48
tr 220,2,12288
tr 222,2,18432
s_waitcnt lgkmcnt(8)
ma 32,216,40
v_exp_f32 v68, v68
ma 40,216,48
v_exp_f32 v69, v69
ma 36,252,40
v_exp_f32 v70, v70
ma 44,252,48
v_exp_f32 v71, v71
ma 48,248,40
cv 64,64,65
tr 216,3,12288
ma 56,248,48
tr 218,3,18432
cv 65,66,67
ma 52,228,40
cv 66,68,69
cv 67,70,71
ma 60,228,48
tr 252,4,12288
tr 254,4,18432
ma 64,244,40
tr 248,1,13312
tr 250,1,19456
ma 72,244,48
tr 228,2,13312
tr 230,2,19456
ma 68,236,40
tr 244,3,13312
tr 246,3,19456
ma 76,236,48
s_add_u32 m0, s16, 52224
tr 236,4,13312
dn 12,48,22,0
s_waitcnt lgkmcnt(9)
ma 80,232,40
tr 238,4,19456
v_mul_f32 v120, s26, v120
ma 88,232,48
tr 232,1,14336
tr 234,1,20480
ma 84,224,40
v_mul_f32 v121, s26, v121
v_mul_f32 v128, s26, v128
ma 92,224,48
dn 12,52,22,1024
v_mfma_f32_16x16x32_bf16 a[244:247], a[240:243], v[56:59], a[244:247]
v_mul_f32 v129, s26, v129
v_mul_f32 v136, s26, v136
v_mfma_f32_16x16x32_bf16 a[248:251], a[240:243], v[64:67], a[248:251]
v_mul_f32 v137, s26, v137
v_mul_f32 v144, s26, v144
ma 0,240,56
v_mul_f32 v145, s26, v145
v_mul_f32 v152, s26, v152
ma 8,240,64
dn 12,56,22,2048
ma 4,220,56
v_mul_f32 v153, s26, v153
v_mul_f32 v160, s26, v160
ma 12,220,64
v_mul_f32 v161, s26, v161
v_mul_f32 v168, s26, v168
ma 16,216,56
v_mul_f32 v169, s26, v169
v_mul_f32 v176, s26, v176
ma 24,216,64
v_mul_f32 v177, s26, v177
v_mul_f32 v184, s26, v184
s_waitcnt lgkmcnt(0)
ma 20,252,56
v_mul_f32 v185, s26, v185
v_mul_f32 v192, s26, v192
ma 28,252,64
v_mul_f32 v193, s26, v193
v_mul_f32 v200, s26, v200
ma 32,248,56
v_mul_f32 v201, s26, v201
v_mul_f32 v208, s26, v208
ma 40,248,64
v_mul_f32 v209, s26, v209
v_mul_f32 v122, s26, v122
ma 36,228,56
v_mul_f32 v123, s26, v123
cv 120,120,121
ma 44,228,64
v_mul_f32 v130, s26, v130
v_mul_f32 v131, s26, v131
cv 128,128,129
v_mul_f32 v138, s26, v138
v_mul_f32 v139, s26, v139
cv 136,136,137
v_mul_f32 v146, s26, v146
v_mul_f32 v147, s26, v147
cv 144,144,145
v_mul_f32 v154, s26, v154
v_mul_f32 v155, s26, v155
cv 152,152,153
v_mul_f32 v162, s26, v162
v_mul_f32 v163, s26, v163
cv 160,160,161
v_mul_f32 v170, s26, v170
v_mul_f32 v171, s26, v171
cv 168,168,169
v_mul_f32 v178, s26, v178
v_mul_f32 v179, s26, v179
cv 176,176,177
v_mul_f32 v186, s26, v186
v_mul_f32 v187, s26, v187
cv 184,184,185
v_mul_f32 v194, s26, v194
v_mul_f32 v195, s26, v195
cv 192,192,193
v_mul_f32 v202, s26, v202
v_mul_f32 v203, s26, v203
cv 200,200,201
v_mul_f32 v210, s26, v210
v_mul_f32 v211, s26, v211
cv 208,208,209
tr 224,2,14336
tr 226,2,20480
ma 48,244,56
v_mul_f32 v124, s26, v124
v_mul_f32 v125, s26, v125
v_mul_f32 v126, s26, v126
v_mul_f32 v127, s26, v127
cv 121,122,123
v_mul_f32 v132, s26, v132
v_mul_f32 v133, s26, v133
v_mul_f32 v134, s26, v134
v_mul_f32 v135, s26, v135
cv 129,130,131
v_mul_f32 v140, s26, v140
v_mul_f32 v141, s26, v141
v_mul_f32 v142, s26, v142
v_mul_f32 v143, s26, v143
cv 137,138,139
v_mul_f32 v148, s26, v148
v_mul_f32 v149, s26, v149
v_mul_f32 v150, s26, v150
v_mul_f32 v151, s26, v151
cv 145,146,147
v_mul_f32 v156, s26, v156
v_mul_f32 v157, s26, v157
v_mul_f32 v158, s26, v158
v_mul_f32 v159, s26, v159
cv 153,154,155
v_mul_f32 v164, s26, v164
v_mul_f32 v165, s26, v165
v_mul_f32 v166, s26, v166
v_mul_f32 v167, s26, v167
cv 161,162,163
v_mul_f32 v172, s26, v172
v_mul_f32 v173, s26, v173
v_mul_f32 v174, s26, v174
v_mul_f32 v175, s26, v175
cv 169,170,171
v_mul_f32 v180, s26, v180
v_mul_f32 v181, s26, v181
v_mul_f32 v182, s26, v182
v_mul_f32 v183, s26, v183
cv 177,178,179
v_mul_f32 v188, s26, v188
v_mul_f32 v189, s26, v189
v_mul_f32 v190, s26, v190
v_mul_f32 v191, s26, v191
cv 185,186,187
v_mul_f32 v196, s26, v196
v_mul_f32 v197, s26, v197
v_mul_f32 v198, s26, v198
v_mul_f32 v199, s26, v199
cv 193,194,195
v_mul_f32 v204, s26, v204
v_mul_f32 v205, s26, v205
v_mul_f32 v206, s26, v206
v_mul_f32 v207, s26, v207
cv 201,202,203
v_mul_f32 v212, s26, v212
v_mul_f32 v213, s26, v213
v_mul_f32 v214, s26, v214
v_mul_f32 v215, s26, v215
cv 209,210,211
cv 122,124,125
cv 123,126,127
cv 130,132,133
cv 131,134,135
cv 138,140,141
cv 139,142,143
cv 146,148,149
cv 147,150,151
cv 154,156,157
cv 155,158,159
cv 162,164,165
cv 163,166,167
cv 170,172,173
cv 171,174,175
cv 178,180,181
cv 179,182,183
cv 186,188,189
cv 187,190,191
cv 194,196,197
cv 195,198,199
cv 202,204,205
cv 203,206,207
cv 210,212,213
cv 211,214,215
v_permlane32_swap_b32 v120, v122
v_permlane32_swap_b32 v121, v123
v_permlane32_swap_b32 v128, v130
v_permlane32_swap_b32 v129, v131
v_permlane32_swap_b32 v136, v138
v_permlane32_swap_b32 v137, v139
v_permlane32_swap_b32 v144, v146
v_permlane32_swap_b32 v145, v147
v_permlane32_swap_b32 v152, v154
v_permlane32_swap_b32 v153, v155
v_permlane32_swap_b32 v160, v162
v_permlane32_swap_b32 v161, v163
v_permlane32_swap_b32 v168, v170
v_permlane32_swap_b32 v169, v171
v_permlane32_swap_b32 v176, v178
v_permlane32_swap_b32 v177, v179
v_permlane32_swap_b32 v184, v186
v_permlane32_swap_b32 v185, v187
v_permlane32_swap_b32 v192, v194
v_permlane32_swap_b32 v193, v195
v_permlane32_swap_b32 v200, v202
v_permlane32_swap_b32 v201, v203
v_permlane32_swap_b32 v208, v210
v_permlane32_swap_b32 v209, v211
ma 56,244,64
v_add_u32 v124, s89, v19
v_permlane16_swap_b32 v120, v122
v_permlane16_swap_b32 v121, v123
v_add_u32 v132, s89, v19
v_permlane16_swap_b32 v128, v130
v_permlane16_swap_b32 v129, v131
v_add_u32 v140, s89, v19
v_permlane16_swap_b32 v136, v138
v_permlane16_swap_b32 v137, v139
v_add_u32 v148, s89, v19
v_permlane16_swap_b32 v144, v146
v_permlane16_swap_b32 v145, v147
v_add_u32 v156, s89, v19
v_permlane16_swap_b32 v152, v154
v_permlane16_swap_b32 v153, v155
v_add_u32 v164, s89, v19
v_permlane16_swap_b32 v160, v162
v_permlane16_swap_b32 v161, v163
v_add_u32 v172, s89, v19
v_permlane16_swap_b32 v168, v170
v_permlane16_swap_b32 v169, v171
v_add_u32 v180, s89, v19
v_permlane16_swap_b32 v176, v178
v_permlane16_swap_b32 v177, v179
v_add_u32 v188, s89, v19
v_permlane16_swap_b32 v184, v186
v_permlane16_swap_b32 v185, v187
v_add_u32 v196, s89, v19
v_permlane16_swap_b32 v192, v194
v_permlane16_swap_b32 v193, v195
v_add_u32 v204, s89, v19
v_permlane16_swap_b32 v200, v202
v_permlane16_swap_b32 v201, v203
v_add_u32 v212, s89, v19
v_permlane16_swap_b32 v208, v210
v_permlane16_swap_b32 v209, v211
ds_write_b128 v124, v[120:123] offset:0
ds_write_b128 v132, v[128:131] offset:4096
ds_write_b128 v140, v[136:139] offset:1024
ds_write_b128 v148, v[144:147] offset:5120
ds_write_b128 v156, v[152:155] offset:16384
ds_write_b128 v164, v[160:163] offset:20480
ds_write_b128 v172, v[168:171] offset:17408
ds_write_b128 v180, v[176:179] offset:21504
ds_write_b128 v188, v[184:187] offset:32768
ds_write_b128 v196, v[192:195] offset:36864
ds_write_b128 v204, v[200:203] offset:33792
ds_write_b128 v212, v[208:211] offset:37888
tr 240,3,14336
tr 242,3,20480
tr 220,4,14336
tr 222,4,20480
ma 52,236,56
ar 208,244
v_rcp_f32 v208, v208
ma 60,236,64
ar 200,0
ar 201,1
ma 64,232,56
ar 202,2
ar 203,3
ma 72,232,64
ar 204,4
ar 205,5
s_waitcnt vmcnt(12) lgkmcnt(0)
s_barrier
rd 72,19,0
rd 216,5,40960
ma 68,224,56
rd 80,19,8192
rd 252,6,40960
ma 76,224,64
rd 84,19,12288
rd 76,19,4096
ma 80,240,56
rd 92,19,20480
rd 248,5,41984
ma 88,240,64
rd 100,19,28672
rd 228,6,41984
ma 84,220,56
rd 96,19,24576
rd 88,19,16384
ma 92,220,64
rd 104,19,32768
rd 244,5,43008
rd 112,19,40960
rd 236,6,43008
rd 116,19,45056
rd 108,19,36864
rd 232,5,47104
s_waitcnt lgkmcnt(15)
mvz 40,216,72
rd 224,6,47104
rd 240,5,48128
mv 40,252,80
rd 220,6,48128
ar 206,6
s_waitcnt lgkmcnt(14)
mvz 48,252,84
rd 252,5,49152
ar 207,7
mv 48,216,76
rd 216,6,49152
v_mul_f32 v200, v208, v200
mv 48,248,92
v_mul_f32 v201, v208, v201
v_mul_f32 v202, v208, v202
s_waitcnt lgkmcnt(7)
mv 48,228,100
v_mul_f32 v203, v208, v203
v_mul_f32 v204, v208, v204
mv 40,228,96
rd 228,5,53248
v_mul_f32 v205, v208, v205
mv 40,248,88
rd 248,6,53248
v_mul_f32 v206, v208, v206
mv 40,244,104
v_mul_f32 v207, v208, v207
ar 209,248
mv 40,236,112
cv 120,200,201
cv 121,202,203
mv 48,236,116
rd 236,5,54272
cv 122,204,205
s_waitcnt lgkmcnt(3)
mv 48,244,108
rd 244,6,54272
cv 123,206,207
mvz 44,232,72
v_rcp_f32 v209, v209
mv 44,224,80
ar 24,8
ar 25,9
mvz 52,224,84
rd 224,5,55296
ar 26,10
mv 52,232,76
rd 232,6,55296
ar 27,11
mv 52,240,92
ar 28,12
ar 29,13
mv 52,220,100
ar 30,14
ar 31,15
mv 44,220,96
rd 220,5,59392
v_mul_f32 v24, v209, v24
mv 44,240,88
rd 240,6,59392
v_mul_f32 v25, v209, v25
mv 44,252,104
v_mul_f32 v26, v209, v26
v_mul_f32 v27, v209, v27
mv 44,216,112
v_mul_f32 v28, v209, v28
v_mul_f32 v29, v209, v29
mv 52,216,116
rd 216,5,60416
v_mul_f32 v30, v209, v30
mv 52,252,108
rd 252,6,60416
v_mul_f32 v31, v209, v31
s_waitcnt lgkmcnt(4)
mvz 56,228,72
cv 124,24,25
cv 125,26,27
mv 56,248,80
cv 126,28,29
cv 127,30,31
mvz 64,248,84
rd 248,5,61440
ar 200,16
mv 64,228,76
rd 228,6,61440
ar 201,17
mv 64,236,92
ar 202,18
ar 203,19
mv 64,244,100
ar 204,20
ar 205,21
mv 56,244,96
v_mov_b32 v244, 0
v_mov_b32 v245, 0
mv 56,236,88
ar 206,22
ar 207,23
mv 56,224,104
s_waitcnt vmcnt(6) lgkmcnt(0)
s_barrier
mv 56,232,112
tr 236,1,24576
tr 238,1,30720
mv 64,232,116
tr 232,2,24576
tr 234,2,30720
mv 64,224,108
tr 224,3,24576
tr 226,3,30720
mvz 60,220,72
v_mul_f32 v200, v208, v200
v_mul_f32 v201, v208, v201
mv 60,240,80
v_mul_f32 v202, v208, v202
v_mul_f32 v203, v208, v203
mvz 68,240,84
ar 242,252
ar 243,253
mv 68,220,76
v_mov_b32 v240, 0
v_mov_b32 v241, 0
mv 68,216,92
tr 220,4,24576
tr 222,4,30720
mv 68,252,100
v_mul_f32 v204, v208, v204
v_mul_f32 v205, v208, v205
mv 60,252,96
tr 252,1,36864
tr 254,1,43008
mv 60,216,88
s_add_u32 m0, s16, 0
s_add_u32 s23, s18, 614400
dn 9,36,23,0
mv 60,248,104
tr 216,2,36864
tr 218,2,43008
mv 60,228,112
v_mul_f32 v206, v208, v206
v_mul_f32 v207, v208, v207
mv 68,228,116
tr 228,3,36864
tr 230,3,43008
mv 68,248,108
dn 9,40,23,1024
v_mfma_f32_16x16x32_bf16 v[176:179], v[242:245], a[196:199], 0
cv 128,200,201
cv 129,202,203
v_mfma_f32_16x16x32_bf16 v[168:171], v[242:245], a[192:195], 0
cv 130,204,205
cv 131,206,207
s_waitcnt lgkmcnt(4)
mv 168,236,120
ar 24,24
ar 25,25
mv 176,236,124
dn 9,44,23,2048
v_mfma_f32_16x16x32_bf16 v[180:183], v[240:243], a[196:199], 0
ar 26,26
ar 27,27
v_mfma_f32_16x16x32_bf16 v[172:175], v[240:243], a[192:195], 0
ar 28,28
ar 29,29
mv 172,232,120
ar 30,30
ar 31,31
mv 180,232,124
tr 248,4,36864
tr 250,4,43008
v_mfma_f32_16x16x32_bf16 v[192:195], v[242:245], a[204:207], 0
v_mul_f32 v24, v209, v24
v_mul_f32 v25, v209, v25
v_mfma_f32_16x16x32_bf16 v[184:187], v[242:245], a[200:203], 0
v_mul_f32 v26, v209, v26
v_mul_f32 v27, v209, v27
mv 184,224,120
v_mul_f32 v28, v209, v28
v_mul_f32 v29, v209, v29
mv 192,224,124
v_mul_f32 v30, v209, v30
v_mul_f32 v31, v209, v31
v_mfma_f32_16x16x32_bf16 v[196:199], v[240:243], a[204:207], 0
cv 132,24,25
cv 133,26,27
v_mfma_f32_16x16x32_bf16 v[188:191], v[240:243], a[200:203], 0
cv 134,28,29
cv 135,30,31
mv 188,220,120
tr 236,1,25600
tr 238,1,31744
mv 196,220,124
tr 232,2,25600
tr 234,2,31744
mv 168,252,128
tr 224,3,25600
tr 226,3,31744
mv 176,252,132
ar 200,32
ar 201,33
s_waitcnt lgkmcnt(6)
mv 172,216,128
ar 202,34
ar 203,35
mv 180,216,132
ar 204,36
ar 205,37
mv 184,228,128
ar 206,38
ar 207,39
mv 192,228,132
v_mul_f32 v200, v208, v200
v_mul_f32 v201, v208, v201
mv 188,248,128
v_mul_f32 v202, v208, v202
v_mul_f32 v203, v208, v203
mv 196,248,132
s_add_u32 m0, s16, 3072
v_mul_f32 v204, v208, v204
dn 10,36,23,0
v_mul_f32 v205, v208, v205
v_mul_f32 v206, v208, v206
v_mul_f32 v207, v208, v207
cv 136,200,201
cv 137,202,203
cv 138,204,205
cv 139,206,207
ar 24,40
ar 25,41
ar 26,42
ar 27,43
ar 28,44
ar 29,45
ar 30,46
dn 10,40,23,1024
ar 31,47
tr 220,4,25600
tr 222,4,31744
s_waitcnt lgkmcnt(2)
mv 168,236,136
v_mul_f32 v24, v209, v24
v_mul_f32 v25, v209, v25
v_mul_f32 v26, v209, v26
v_mul_f32 v27, v209, v27
v_mul_f32 v28, v209, v28
v_mul_f32 v29, v209, v29
v_mul_f32 v30, v209, v30
v_mul_f32 v31, v209, v31
dn 10,44,23,2048
cv 140,24,25
cv 141,26,27
cv 142,28,29
cv 143,30,31
tr 252,1,37888
tr 254,1,44032
mv 176,236,140
tr 216,2,37888
tr 218,2,44032
mv 172,232,136
tr 228,3,37888
tr 230,3,44032
mv 180,232,140
ar 200,48
ar 201,49
mv 184,224,136
ar 202,50
ar 203,51
mv 192,224,140
ar 204,52
ar 205,53
s_waitcnt lgkmcnt(6)
mv 188,220,136
ar 206,54
ar 207,55
mv 196,220,140
v_mul_f32 v200, v208, v200
v_mul_f32 v201, v208, v201
v_mul_f32 v202, v208, v202
v_mul_f32 v203, v208, v203
v_mul_f32 v204, v208, v204
v_mul_f32 v205, v208, v205
v_mul_f32 v206, v208, v206
v_mul_f32 v207, v208, v207
cv 144,200,201
cv 145,202,203
cv 146,204,205
cv 147,206,207
ar 24,56
ar 25,57
ar 26,58
ar 27,59
ar 28,60
ar 29,61
ar 30,62
ar 31,63
tr 248,4,37888
tr 250,4,44032
s_waitcnt lgkmcnt(2)
mv 168,252,144
v_mul_f32 v24, v209, v24
v_mul_f32 v25, v209, v25
v_mul_f32 v26, v209, v26
v_mul_f32 v27, v209, v27
v_mul_f32 v28, v209, v28
v_mul_f32 v29, v209, v29
v_mul_f32 v30, v209, v30
v_mul_f32 v31, v209, v31
cv 148,24,25
cv 149,26,27
cv 150,28,29
cv 151,30,31
tr 236,1,26624
tr 238,1,32768
mv 176,252,148
tr 232,2,26624
tr 234,2,32768
mv 172,216,144
tr 224,3,26624
tr 226,3,32768
mv 180,216,148
ar 200,64
ar 201,65
mv 184,228,144
ar 202,66
ar 203,67
mv 192,228,148
ar 204,68
ar 205,69
s_waitcnt lgkmcnt(6)
mv 188,248,144
ar 206,70
ar 207,71
mv 196,248,148
v_mul_f32 v200, v208, v200
v_mul_f32 v201, v208, v201
v_mul_f32 v202, v208, v202
v_mul_f32 v203, v208, v203
v_mul_f32 v204, v208, v204
v_mul_f32 v205, v208, v205
v_mul_f32 v206, v208, v206
v_mul_f32 v207, v208, v207
cv 152,200,201
cv 153,202,203
cv 154,204,205
cv 155,206,207
ar 24,72
ar 25,73
ar 26,74
ar 27,75
ar 28,76
ar 29,77
ar 30,78
ar 31,79
tr 220,4,26624
tr 222,4,32768
s_waitcnt lgkmcnt(2)
mv 168,236,152
v_mul_f32 v24, v209, v24
v_mul_f32 v25, v209, v25
v_mul_f32 v26, v209, v26
v_mul_f32 v27, v209, v27
v_mul_f32 v28, v209, v28
v_mul_f32 v29, v209, v29
v_mul_f32 v30, v209, v30
v_mul_f32 v31, v209, v31
cv 156,24,25
cv 157,26,27
cv 158,28,29
cv 159,30,31
tr 252,1,38912
tr 254,1,45056
mv 176,236,156
tr 216,2,38912
tr 218,2,45056
tr 228,3,38912
tr 230,3,45056
tr 248,4,38912
tr 250,4,45056
mv 172,232,152
ar 200,80
ar 201,81
mv 180,232,156
ar 202,82
ar 203,83
mv 184,224,152
ar 204,84
ar 205,85
mv 192,224,156
ar 206,86
ar 207,87
s_waitcnt lgkmcnt(6)
mv 188,220,152
s_waitcnt vmcnt(6) lgkmcnt(0)
s_barrier
mv 196,220,156
v_mul_f32 v200, v208, v200
v_mul_f32 v201, v208, v201
v_mul_f32 v202, v208, v202
v_mul_f32 v203, v208, v203
v_mul_f32 v204, v208, v204
v_mul_f32 v205, v208, v205
v_mul_f32 v206, v208, v206
v_mul_f32 v207, v208, v207
cv 160,200,201
cv 161,202,203
cv 162,204,205
cv 163,206,207
ar 24,88
ar 25,89
ar 26,90
ar 27,91
ar 28,92
ar 29,93
ar 30,94
ar 31,95
mv 168,252,160
v_mul_f32 v24, v209, v24
v_mul_f32 v25, v209, v25
v_mul_f32 v26, v209, v26
v_mul_f32 v27, v209, v27
v_mul_f32 v28, v209, v28
v_mul_f32 v29, v209, v29
v_mul_f32 v30, v209, v30
v_mul_f32 v31, v209, v31
cv 164,24,25
cv 165,26,27
cv 166,28,29
cv 167,30,31
v_max3_f32 v20, v40, v41, v42
v_max3_f32 v20, v20, v43, v44
mv 176,252,164
v_max3_f32 v20, v20, v45, v46
v_max3_f32 v20, v20, v47, v56
mv 172,216,160
v_max3_f32 v20, v20, v57, v58
v_max3_f32 v21, v48, v49, v50
mv 180,216,164
s_add_u32 m0, s16, 24576
s_add_u32 s24, s20, 491648
dl 15,72,24,0
v_max3_f32 v20, v20, v59, v60
v_max3_f32 v21, v21, v51, v52
v_max3_f32 v20, v20, v61, v62
v_add_u32 v1, 0x2000, v1
v_max3_f32 v21, v21, v53, v54
v_max_f32 v20, v20, v63
tr 236,1,40960
tr 238,1,47104
mv 184,228,160
v_max3_f32 v21, v21, v55, v64
v_mov_b32 v32, v20
v_max3_f32 v21, v21, v65, v66
v_max3_f32 v21, v21, v67, v68
dl 15,76,24,1024
v_max3_f32 v21, v21, v69, v70
v_permlane32_swap_b32 v20, v32
v_max_f32 v21, v21, v71
v_max_f32 v20, v20, v32
mv 192,228,164
v_mov_b32 v32, v20
v_mov_b32 v36, v21
v_add_u32 v2, 0x2000, v2
v_permlane16_swap_b32 v20, v32
v_permlane32_swap_b32 v21, v36
v_max_f32 v20, v20, v32
v_max_f32 v21, v21, v36
tr 232,2,40960
dl 15,80,24,2048
tr 234,2,47104
mv 188,248,160
v_mov_b32 v36, v21
v_sub_f32 v40, v40, v20
v_sub_f32 v41, v41, v20
v_sub_f32 v42, v42, v20
v_sub_f32 v43, v43, v20
v_exp_f32 v40, v40
v_exp_f32 v41, v41
v_sub_f32 v44, v44, v20
v_sub_f32 v45, v45, v20
v_sub_f32 v46, v46, v20
v_sub_f32 v47, v47, v20
v_exp_f32 v42, v42
v_exp_f32 v43, v43
v_permlane16_swap_b32 v21, v36
v_exp_f32 v44, v44
v_exp_f32 v45, v45
v_exp_f32 v46, v46
v_exp_f32 v47, v47
v_max_f32 v21, v21, v36
cv 40,40,41
mv 196,248,164
v_sub_f32 v48, v48, v21
v_sub_f32 v49, v49, v21
cv 41,42,43
v_sub_f32 v50, v50, v21
v_sub_f32 v51, v51, v21
cv 42,44,45
cv 43,46,47
v_exp_f32 v48, v48
v_exp_f32 v49, v49
v_sub_f32 v52, v52, v21
v_sub_f32 v53, v53, v21
v_sub_f32 v54, v54, v21
v_sub_f32 v55, v55, v21
v_exp_f32 v50, v50
v_exp_f32 v51, v51
v_exp_f32 v52, v52
v_exp_f32 v53, v53
v_exp_f32 v54, v54
v_exp_f32 v55, v55
v_add_u32 v3, 0x2000, v3
cv 48,48,49
tr 224,3,40960
tr 226,3,47104
v_mfma_f32_16x16x32_bf16 a[244:247], a[240:243], v[40:43], 0
cv 49,50,51
cv 50,52,53
cv 51,54,55
v_add_u32 v4, 0x2000, v4
tr 220,4,40960
v_mfma_f32_16x16x32_bf16 a[248:251], a[240:243], v[48:51], 0
tr 222,4,47104
tr 252,1,41984
s_waitcnt lgkmcnt(5)
maz 0,236,40
tr 254,1,48128
tr 216,2,41984
maz 8,236,48
tr 218,2,48128
tr 228,3,41984
maz 4,232,40
tr 230,3,48128
tr 248,4,41984
maz 12,232,48
tr 250,4,48128
tr 236,1,43008
tr 238,1,49152
s_waitcnt lgkmcnt(12)
maz 16,224,40
tr 232,2,43008
tr 234,2,49152
maz 24,224,48
tr 224,3,43008
tr 226,3,49152
v_sub_f32 v56, v56, v20
s_waitcnt lgkmcnt(12)
maz 20,220,40
v_sub_f32 v57, v57, v20
v_sub_f32 v58, v58, v20
maz 28,220,48
tr 220,4,43008
tr 222,4,49152
maz 32,252,40
v_sub_f32 v59, v59, v20
v_exp_f32 v56, v56
maz 40,252,48
tr 252,1,53248
tr 254,1,59392
s_waitcnt lgkmcnt(7)
maz 36,216,40
v_exp_f32 v57, v57
maz 44,216,48
tr 216,2,53248
tr 218,2,59392
maz 48,228,40
v_sub_f32 v60, v60, v20
v_sub_f32 v61, v61, v20
maz 56,228,48
v_sub_f32 v62, v62, v20
v_sub_f32 v63, v63, v20
maz 52,248,40
v_exp_f32 v58, v58
maz 60,248,48
v_exp_f32 v59, v59
maz 64,236,40
v_exp_f32 v60, v60
maz 72,236,48
v_exp_f32 v61, v61
s_waitcnt lgkmcnt(0)
maz 68,232,40
v_exp_f32 v62, v62
maz 76,232,48
s_add_u32 m0, s16, 27648
v_exp_f32 v63, v63
dl 16,72,24,0
maz 80,224,40
cv 56,56,57
v_sub_f32 v64, v64, v21
maz 88,224,48
v_sub_f32 v65, v65, v21
cv 57,58,59
maz 84,220,40
v_sub_f32 v66, v66, v21
v_sub_f32 v67, v67, v21
maz 92,220,48
dl 16,76,24,1024
cv 58,60,61
cv 59,62,63
v_exp_f32 v64, v64
v_exp_f32 v65, v65
v_sub_f32 v68, v68, v21
v_sub_f32 v69, v69, v21
v_sub_f32 v70, v70, v21
v_sub_f32 v71, v71, v21
v_exp_f32 v66, v66
v_exp_f32 v67, v67
dl 16,80,24,2048
v_exp_f32 v68, v68
v_exp_f32 v69, v69
v_exp_f32 v70, v70
v_exp_f32 v71, v71
cv 64,64,65
tr 228,3,53248
tr 230,3,59392
v_mfma_f32_16x16x32_bf16 a[244:247], a[240:243], v[56:59], a[244:247]
cv 65,66,67
cv 66,68,69
cv 67,70,71
tr 248,4,53248
tr 250,4,59392
v_mfma_f32_16x16x32_bf16 a[248:251], a[240:243], v[64:67], a[248:251]
tr 236,1,54272
tr 238,1,60416
ma 0,252,56
tr 232,2,54272
tr 234,2,60416
ma 8,252,64
tr 224,3,54272
tr 226,3,60416
ma 4,216,56
tr 220,4,54272
tr 222,4,60416
ma 12,216,64
tr 252,1,55296
tr 254,1,61440
tr 216,2,55296
tr 218,2,61440
s_waitcnt lgkmcnt(14)
ma 16,228,56
v_add_u32 v5, 0xffff8000, v5
v_add_u32 v6, 0xffff8000, v6
ma 24,228,64
tr 228,3,55296
tr 230,3,61440
s_waitcnt lgkmcnt(11)
ma 20,248,56
v_sub_f32 v32, 0, v20
v_sub_f32 v33, 0, v20
ma 28,248,64
tr 248,4,55296
tr 250,4,61440
ma 32,236,56
v_sub_f32 v34, 0, v20
v_sub_f32 v35, 0, v20
ma 40,236,64
v_sub_f32 v36, 0, v21
v_sub_f32 v37, 0, v21
s_waitcnt lgkmcnt(4)
ma 36,232,56
v_sub_f32 v38, 0, v21
v_sub_f32 v39, 0, v21
ma 44,232,64
cv 176,176,177
cv 177,178,179
ma 48,224,56
s_waitcnt vmcnt(6) lgkmcnt(0)
s_barrier
ma 56,224,64
rd 236,5,0
rd 232,6,0
ma 52,220,56
rd 224,5,1024
cv 178,180,181
ma 60,220,64
rd 220,6,1024
cv 179,182,183
ma 64,252,56
aw 196,176
aw 197,177
ma 72,252,64
rd 252,5,2048
aw 198,178
ma 68,216,56
aw 199,179
cv 168,168,169
ma 76,216,64
s_add_u32 m0, s16, 49152
s_add_u32 s25, s18, 614400
dn 11,48,25,0
ma 80,228,56
rd 216,6,2048
cv 169,170,171
ma 88,228,64
rd 228,5,6144
cv 170,172,173
ma 84,248,56
cv 171,174,175
aw 192,168
ma 92,248,64
dn 11,52,25,1024
s_waitcnt lgkmcnt(2)
mvc 40,236,72,32
rd 248,6,6144
aw 193,169
mv 40,232,80
aw 194,170
aw 195,171
mvc 48,232,84,36
rd 232,5,7168
cv 192,192,193
mv 48,236,76
dn 11,56,25,2048
mv 48,224,92
rd 236,6,7168
cv 193,194,195
mv 48,220,100
cv 194,196,197
cv 195,198,199
mv 40,220,96
rd 220,5,8192
aw 204,192
mv 40,224,88
rd 224,6,8192
aw 205,193
mv 40,252,104
aw 206,194
aw 207,195
s_waitcnt lgkmcnt(3)
mv 40,216,112
cv 184,184,185
cv 185,186,187
mv 48,216,116
rd 216,5,12288
cv 186,188,189
mv 48,252,108
rd 252,6,12288
cv 187,190,191
mvc 44,228,72,32
aw 200,184
aw 201,185
mv 44,248,80
aw 202,186
aw 203,187
mvc 52,248,84,36
rd 248,5,13312
v_max3_f32 v20, v40, v41, v42
mv 52,228,76
rd 228,6,13312
v_max3_f32 v20, v20, v43, v48
mv 52,232,92
v_max3_f32 v20, v20, v49, v50
rd 204,19,53248
s_waitcnt lgkmcnt(4)
mv 52,236,100
rd 200,19,49152
rd 212,19,61440
mv 44,236,96
rd 236,5,14336
rd 208,19,57344
mv 44,232,88
s_add_u32 m0, s16, 52224
rd 232,6,14336
dn 12,48,25,0
mv 44,220,104
mv 44,224,112
mv 52,224,116
rd 224,5,18432
mv 52,220,108
dn 12,52,25,1024
mvc 56,216,72,32
rd 220,6,18432
s_nop 1
v_max3_f32 v20, v20, v51, v44
s_waitcnt lgkmcnt(3)
mv 56,252,80
v_max3_f32 v20, v20, v45, v46
v_max3_f32 v20, v20, v47, v52
mvc 64,252,84,36
rd 252,5,19456
v_max3_f32 v20, v20, v53, v54
mv 64,216,76
dn 12,56,25,2048
mv 64,248,92
rd 216,6,19456
mv 64,228,100
mv 56,228,96
rd 228,5,20480
mv 56,248,88
rd 248,6,20480
mv 56,236,104
s_waitcnt lgkmcnt(3)
mv 56,232,112
mv 64,232,116
mv 64,236,108
s_waitcnt vmcnt(6) lgkmcnt(0)
s_barrier
mvc 60,224,72,32
tr 232,1,16384
tr 234,1,22528
mv 60,220,80
tr 236,1,28672
tr 238,1,34816
mvc 68,220,84,36
tr 220,1,17408
tr 222,1,23552
mv 68,224,76
tr 224,1,29696
tr 226,1,35840
mv 68,252,92
v_max3_f32 v20, v20, v55, v56
v_max3_f32 v20, v20, v57, v58
mv 68,216,100
v_max3_f32 v20, v20, v59, v64
v_max3_f32 v20, v20, v65, v66
mv 60,216,96
tr 216,1,18432
tr 218,1,24576
mv 60,252,88
s_add_u32 m0, s16, 0
s_add_u32 s22, s18, 638976
dn 9,36,22,0
mv 60,228,104
tr 252,1,30720
tr 254,1,36864
mv 60,248,112
mv 68,248,116
tr 248,2,16384
tr 250,2,22528
mv 68,228,108
dn 9,40,22,1024
v_mfma_f32_16x16x32_bf16 v[176:179], v[242:245], a[212:215], 0
tr 228,2,28672
tr 230,2,34816
v_mfma_f32_16x16x32_bf16 v[168:171], v[242:245], a[208:211], 0
v_max3_f32 v20, v20, v67, v60
v_max3_f32 v20, v20, v61, v62
s_waitcnt lgkmcnt(6)
mv 168,232,120
v_max3_f32 v20, v20, v63, v68
v_max3_f32 v20, v20, v69, v70
mv 168,236,128
dn 9,44,22,2048
mv 176,236,132
tr 236,2,17408
tr 238,2,23552
mv 176,232,124
tr 232,2,29696
tr 234,2,35840
mv 176,220,140
v_max_f32 v20, v20, v71
v_cmp_lt_f32 vcc, s27, v20
s_cbranch_vccz Lc21
s_swappc_b64 s[30:31], s[28:29]
Lc21:
mv 176,224,148
v_exp_f32 v40, v40
mv 168,224,144
tr 224,2,18432
tr 226,2,24576
mv 168,220,136
tr 220,2,30720
tr 222,2,36864
mv 168,216,152
v_exp_f32 v41, v41
s_waitcnt lgkmcnt(6)
mv 168,252,160
v_exp_f32 v42, v42
mv 176,252,164
tr 252,3,16384
tr 254,3,22528
mv 176,216,156
tr 216,3,28672
tr 218,3,34816
v_mfma_f32_16x16x32_bf16 v[180:183], v[240:243], a[212:215], 0
v_exp_f32 v43, v43
v_mfma_f32_16x16x32_bf16 v[172:175], v[240:243], a[208:211], 0
v_exp_f32 v44, v44
mv 172,248,120
v_exp_f32 v45, v45
mv 172,228,128
v_exp_f32 v46, v46
mv 180,228,132
tr 228,3,17408
tr 230,3,23552
mv 180,248,124
tr 248,3,29696
tr 250,3,35840
mv 180,236,140
v_exp_f32 v47, v47
s_waitcnt lgkmcnt(4)
mv 180,232,148
cv 40,40,41
cv 41,42,43
mv 172,232,144
tr 232,3,18432
tr 234,3,24576
mv 172,236,136
s_add_u32 m0, s16, 3072
tr 236,3,30720
dn 10,36,22,0
mv 172,224,152
tr 238,3,36864
cv 42,44,45
mv 172,220,160
cv 43,46,47
v_exp_f32 v48, v48
mv 180,220,164
tr 220,4,16384
tr 222,4,22528
mv 180,224,156
dn 10,40,22,1024
v_mfma_f32_16x16x32_bf16 v[192:195], v[242:245], a[220:223], 0
tr 224,4,28672
tr 226,4,34816
v_mfma_f32_16x16x32_bf16 v[184:187], v[242:245], a[216:219], 0
v_exp_f32 v49, v49
mv 184,252,120
v_exp_f32 v50, v50
mv 184,216,128
dn 10,44,22,2048
mv 192,216,132
tr 216,4,17408
tr 218,4,23552
mv 192,252,124
tr 252,4,29696
tr 254,4,35840
s_waitcnt lgkmcnt(6)
mv 192,228,140
v_exp_f32 v51, v51
mv 192,248,148
v_exp_f32 v52, v52
mv 184,248,144
tr 248,4,18432
tr 250,4,24576
mv 184,228,136
tr 228,4,30720
tr 230,4,36864
mv 184,232,152
v_exp_f32 v53, v53
mv 184,236,160
v_exp_f32 v54, v54
mv 192,236,164
v_exp_f32 v55, v55
mv 192,232,156
cv 48,48,49
cv 49,50,51
v_mfma_f32_16x16x32_bf16 v[196:199], v[240:243], a[220:223], 0
s_waitcnt vmcnt(6) lgkmcnt(0)
s_barrier
v_mfma_f32_16x16x32_bf16 v[188:191], v[240:243], a[216:219], 0
tr 236,1,40960
tr 238,1,47104
mv 188,220,120
tr 232,2,40960
tr 234,2,47104
mv 188,224,128
cv 50,52,53
cv 51,54,55
mv 196,224,132
tr 224,3,40960
tr 226,3,47104
mv 196,220,124
tr 220,4,40960
tr 222,4,47104
mv 196,216,140
v_exp_f32 v56, v56
mv 196,252,148
v_exp_f32 v57, v57
mv 188,252,144
tr 252,1,41984
tr 254,1,48128
mv 188,216,136
s_add_u32 m0, s16, 24576
s_add_u32 s23, s20, 491776
dl 15,72,23,0
mv 188,248,152
tr 216,2,41984
tr 218,2,48128
mv 188,228,160
v_exp_f32 v58, v58
mv 196,228,164
tr 228,3,41984
tr 230,3,48128
mv 196,248,156
dl 15,76,23,1024
v_mfma_f32_16x16x32_bf16 a[244:247], a[240:243], v[40:43], a[244:247]
tr 248,4,41984
tr 250,4,48128
v_mfma_f32_16x16x32_bf16 a[248:251], a[240:243], v[48:51], a[248:251]
v_exp_f32 v59, v59
s_waitcnt lgkmcnt(6)
ma 0,236,40
v_exp_f32 v60, v60
ma 8,236,48
dl 15,80,23,2048
ma 4,232,40
tr 236,1,43008
tr 238,1,49152
ma 12,232,48
tr 232,2,43008
tr 234,2,49152
ma 16,224,40
v_exp_f32 v61, v61
ma 24,224,48
tr 224,3,43008
tr 226,3,49152
ma 20,220,40
v_exp_f32 v62, v62
ma 28,220,48
tr 220,4,43008
tr 222,4,49152
ma 32,252,40
v_exp_f32 v63, v63
ma 40,252,48
tr 252,1,53248
tr 254,1,59392
s_waitcnt lgkmcnt(6)
ma 36,216,40
cv 56,56,57
cv 57,58,59
ma 44,216,48
tr 216,2,53248
tr 218,2,59392
ma 48,228,40
cv 58,60,61
cv 59,62,63
ma 56,228,48
v_exp_f32 v64, v64
ma 52,248,40
v_exp_f32 v65, v65
ma 60,248,48
v_exp_f32 v66, v66
ma 64,236,40
v_exp_f32 v67, v67
ma 72,236,48
v_exp_f32 v68, v68
ma 68,232,40
v_exp_f32 v69, v69
ma 76,232,48
)"
R"(s_add_u32 m0, s16, 27648
v_exp_f32 v70, v70
dl 16,72,23,0
s_waitcnt lgkmcnt(0)
ma 80,224,40
v_exp_f32 v71, v71
ma 88,224,48
cv 64,64,65
tr 228,3,53248
ma 84,220,40
tr 230,3,59392
cv 65,66,67
ma 92,220,48
dl 16,76,23,1024
v_mfma_f32_16x16x32_bf16 a[244:247], a[240:243], v[56:59], a[244:247]
cv 66,68,69
cv 67,70,71
tr 248,4,53248
tr 250,4,59392
v_mfma_f32_16x16x32_bf16 a[248:251], a[240:243], v[64:67], a[248:251]
tr 236,1,54272
tr 238,1,60416
ma 0,252,56
tr 232,2,54272
tr 234,2,60416
dl 16,80,23,2048
ma 8,252,64
tr 224,3,54272
tr 226,3,60416
ma 4,216,56
tr 220,4,54272
tr 222,4,60416
ma 12,216,64
tr 252,1,55296
tr 254,1,61440
s_waitcnt lgkmcnt(12)
ma 16,228,56
tr 216,2,55296
tr 218,2,61440
ma 24,228,64
tr 228,3,55296
tr 230,3,61440
s_waitcnt lgkmcnt(11)
ma 20,248,56
cv 176,176,177
cv 177,178,179
ma 28,248,64
tr 248,4,55296
tr 250,4,61440
ma 32,236,56
cv 178,180,181
cv 179,182,183
ma 40,236,64
aw 212,176
aw 213,177
s_waitcnt lgkmcnt(6)
ma 36,232,56
aw 214,178
aw 215,179
ma 44,232,64
cv 168,168,169
cv 169,170,171
ma 48,224,56
s_waitcnt vmcnt(6) lgkmcnt(0)
s_barrier
ma 56,224,64
rd 236,5,0
rd 232,6,0
ma 52,220,56
rd 224,5,1024
cv 170,172,173
ma 60,220,64
rd 220,6,1024
cv 171,174,175
ma 64,252,56
aw 208,168
aw 209,169
ma 72,252,64
rd 252,5,2048
aw 210,170
ma 68,216,56
aw 211,171
cv 192,192,193
ma 76,216,64
s_add_u32 m0, s16, 49152
s_add_u32 s24, s18, 638976
dn 11,48,24,0
ma 80,228,56
rd 216,6,2048
cv 193,194,195
ma 88,228,64
rd 228,5,6144
cv 194,196,197
ma 84,248,56
cv 195,198,199
aw 220,192
ma 92,248,64
dn 11,52,24,1024
s_waitcnt lgkmcnt(2)
mvc 40,236,72,32
rd 248,6,6144
aw 221,193
mv 40,232,80
aw 222,194
aw 223,195
mvc 48,232,84,36
rd 232,5,7168
cv 184,184,185
mv 48,236,76
dn 11,56,24,2048
mv 48,224,92
rd 236,6,7168
cv 185,186,187
mv 48,220,100
cv 186,188,189
cv 187,190,191
mv 40,220,96
rd 220,5,8192
aw 216,184
mv 40,224,88
rd 224,6,8192
aw 217,185
mv 40,252,104
aw 218,186
aw 219,187
s_waitcnt lgkmcnt(3)
mv 40,216,112
mv 48,216,116
rd 216,5,12288
mv 48,252,108
rd 252,6,12288
mvc 44,228,72,32
s_nop 2
v_max3_f32 v20, v40, v41, v42
mv 44,248,80
mvc 52,248,84,36
rd 248,5,13312
v_max3_f32 v20, v20, v43, v48
mv 52,228,76
rd 228,6,13312
v_max3_f32 v20, v20, v49, v50
mv 52,232,92
s_waitcnt lgkmcnt(3)
mv 52,236,100
mv 44,236,96
rd 236,5,14336
mv 44,232,88
s_add_u32 m0, s16, 52224
rd 232,6,14336
dn 12,48,24,0
mv 44,220,104
mv 44,224,112
mv 52,224,116
rd 224,5,18432
mv 52,220,108
dn 12,52,24,1024
mvc 56,216,72,32
rd 220,6,18432
s_nop 1
v_max3_f32 v20, v20, v51, v44
s_waitcnt lgkmcnt(3)
mv 56,252,80
v_max3_f32 v20, v20, v45, v46
v_max3_f32 v20, v20, v47, v52
mvc 64,252,84,36
rd 252,5,19456
v_max3_f32 v20, v20, v53, v54
mv 64,216,76
dn 12,56,24,2048
mv 64,248,92
rd 216,6,19456
mv 64,228,100
mv 56,228,96
rd 228,5,20480
mv 56,248,88
rd 248,6,20480
mv 56,236,104
s_waitcnt lgkmcnt(3)
mv 56,232,112
mv 64,232,116
mv 64,236,108
s_waitcnt vmcnt(6) lgkmcnt(0)
s_barrier
mvc 60,224,72,32
tr 232,1,16384
tr 234,1,22528
mv 60,220,80
tr 236,1,28672
tr 238,1,34816
mvc 68,220,84,36
tr 220,1,17408
tr 222,1,23552
mv 68,224,76
tr 224,1,29696
tr 226,1,35840
mv 68,252,92
v_max3_f32 v20, v20, v55, v56
v_max3_f32 v20, v20, v57, v58
mv 68,216,100
v_max3_f32 v20, v20, v59, v64
v_max3_f32 v20, v20, v65, v66
mv 60,216,96
tr 216,1,18432
tr 218,1,24576
mv 60,252,88
s_add_u32 m0, s16, 0
s_add_u32 s25, s18, 663552
dn 9,36,25,0
mv 60,228,104
tr 252,1,30720
tr 254,1,36864
mv 60,248,112
mv 68,248,116
tr 248,2,16384
tr 250,2,22528
mv 68,228,108
dn 9,40,25,1024
v_mfma_f32_16x16x32_bf16 v[176:179], v[242:245], a[228:231], 0
tr 228,2,28672
tr 230,2,34816
v_mfma_f32_16x16x32_bf16 v[168:171], v[242:245], a[224:227], 0
v_max3_f32 v20, v20, v67, v60
v_max3_f32 v20, v20, v61, v62
s_waitcnt lgkmcnt(6)
mv 168,232,120
v_max3_f32 v20, v20, v63, v68
v_max3_f32 v20, v20, v69, v70
mv 168,236,128
dn 9,44,25,2048
mv 176,236,132
tr 236,2,17408
tr 238,2,23552
mv 176,232,124
tr 232,2,29696
tr 234,2,35840
mv 176,220,140
v_max_f32 v20, v20, v71
v_cmp_lt_f32 vcc, s27, v20
s_cbranch_vccz Lc22
s_swappc_b64 s[30:31], s[28:29]
Lc22:
mv 176,224,148
v_exp_f32 v40, v40
mv 168,224,144
tr 224,2,18432
tr 226,2,24576
mv 168,220,136
tr 220,2,30720
tr 222,2,36864
mv 168,216,152
v_exp_f32 v41, v41
s_waitcnt lgkmcnt(6)
mv 168,252,160
v_exp_f32 v42, v42
mv 176,252,164
tr 252,3,16384
tr 254,3,22528
mv 176,216,156
tr 216,3,28672
tr 218,3,34816
v_mfma_f32_16x16x32_bf16 v[180:183], v[240:243], a[228:231], 0
v_exp_f32 v43, v43
v_mfma_f32_16x16x32_bf16 v[172:175], v[240:243], a[224:227], 0
v_exp_f32 v44, v44
mv 172,248,120
v_exp_f32 v45, v45
mv 172,228,128
v_exp_f32 v46, v46
mv 180,228,132
tr 228,3,17408
tr 230,3,23552
mv 180,248,124
tr 248,3,29696
tr 250,3,35840
mv 180,236,140
v_exp_f32 v47, v47
s_waitcnt lgkmcnt(4)
mv 180,232,148
cv 40,40,41
cv 41,42,43
mv 172,232,144
tr 232,3,18432
tr 234,3,24576
mv 172,236,136
s_add_u32 m0, s16, 3072
tr 236,3,30720
dn 10,36,25,0
mv 172,224,152
tr 238,3,36864
cv 42,44,45
mv 172,220,160
cv 43,46,47
v_exp_f32 v48, v48
mv 180,220,164
tr 220,4,16384
tr 222,4,22528
mv 180,224,156
dn 10,40,25,1024
v_mfma_f32_16x16x32_bf16 v[192:195], v[242:245], a[236:239], 0
tr 224,4,28672
tr 226,4,34816
v_mfma_f32_16x16x32_bf16 v[184:187], v[242:245], a[232:235], 0
v_exp_f32 v49, v49
mv 184,252,120
v_exp_f32 v50, v50
mv 184,216,128
dn 10,44,25,2048
mv 192,216,132
tr 216,4,17408
tr 218,4,23552
mv 192,252,124
tr 252,4,29696
tr 254,4,35840
s_waitcnt lgkmcnt(6)
mv 192,228,140
v_exp_f32 v51, v51
mv 192,248,148
v_exp_f32 v52, v52
mv 184,248,144
tr 248,4,18432
tr 250,4,24576
mv 184,228,136
tr 228,4,30720
tr 230,4,36864
mv 184,232,152
v_exp_f32 v53, v53
mv 184,236,160
v_exp_f32 v54, v54
mv 192,236,164
v_exp_f32 v55, v55
mv 192,232,156
cv 48,48,49
cv 49,50,51
v_mfma_f32_16x16x32_bf16 v[196:199], v[240:243], a[236:239], 0
s_waitcnt vmcnt(6) lgkmcnt(0)
s_barrier
v_mfma_f32_16x16x32_bf16 v[188:191], v[240:243], a[232:235], 0
tr 236,1,40960
tr 238,1,47104
mv 188,220,120
tr 232,2,40960
tr 234,2,47104
mv 188,224,128
cv 50,52,53
cv 51,54,55
mv 196,224,132
tr 224,3,40960
tr 226,3,47104
mv 196,220,124
tr 220,4,40960
tr 222,4,47104
mv 196,216,140
v_exp_f32 v56, v56
mv 196,252,148
v_exp_f32 v57, v57
mv 188,252,144
tr 252,1,41984
tr 254,1,48128
mv 188,216,136
s_add_u32 m0, s16, 24576
s_add_u32 s22, s20, 491904
dl 15,72,22,0
mv 188,248,152
tr 216,2,41984
tr 218,2,48128
mv 188,228,160
v_exp_f32 v58, v58
mv 196,228,164
tr 228,3,41984
tr 230,3,48128
mv 196,248,156
dl 15,76,22,1024
v_mfma_f32_16x16x32_bf16 a[244:247], a[240:243], v[40:43], a[244:247]
tr 248,4,41984
tr 250,4,48128
v_mfma_f32_16x16x32_bf16 a[248:251], a[240:243], v[48:51], a[248:251]
v_exp_f32 v59, v59
s_waitcnt lgkmcnt(6)
ma 0,236,40
v_exp_f32 v60, v60
ma 8,236,48
dl 15,80,22,2048
ma 4,232,40
tr 236,1,43008
tr 238,1,49152
ma 12,232,48
tr 232,2,43008
tr 234,2,49152
ma 16,224,40
v_exp_f32 v61, v61
ma 24,224,48
tr 224,3,43008
tr 226,3,49152
ma 20,220,40
v_exp_f32 v62, v62
ma 28,220,48
tr 220,4,43008
tr 222,4,49152
ma 32,252,40
v_exp_f32 v63, v63
ma 40,252,48
tr 252,1,53248
tr 254,1,59392
s_waitcnt lgkmcnt(6)
ma 36,216,40
cv 56,56,57
cv 57,58,59
ma 44,216,48
tr 216,2,53248
tr 218,2,59392
ma 48,228,40
cv 58,60,61
cv 59,62,63
ma 56,228,48
v_exp_f32 v64, v64
ma 52,248,40
v_exp_f32 v65, v65
ma 60,248,48
v_exp_f32 v66, v66
ma 64,236,40
v_exp_f32 v67, v67
ma 72,236,48
v_exp_f32 v68, v68
ma 68,232,40
v_exp_f32 v69, v69
ma 76,232,48
s_add_u32 m0, s16, 27648
v_exp_f32 v70, v70
dl 16,72,22,0
s_waitcnt lgkmcnt(0)
ma 80,224,40
v_exp_f32 v71, v71
ma 88,224,48
cv 64,64,65
tr 228,3,53248
ma 84,220,40
tr 230,3,59392
cv 65,66,67
ma 92,220,48
dl 16,76,22,1024
v_mfma_f32_16x16x32_bf16 a[244:247], a[240:243], v[56:59], a[244:247]
cv 66,68,69
cv 67,70,71
tr 248,4,53248
tr 250,4,59392
v_mfma_f32_16x16x32_bf16 a[248:251], a[240:243], v[64:67], a[248:251]
tr 236,1,54272
tr 238,1,60416
ma 0,252,56
tr 232,2,54272
tr 234,2,60416
dl 16,80,22,2048
ma 8,252,64
tr 224,3,54272
tr 226,3,60416
ma 4,216,56
tr 220,4,54272
tr 222,4,60416
ma 12,216,64
tr 252,1,55296
tr 254,1,61440
s_waitcnt lgkmcnt(12)
ma 16,228,56
tr 216,2,55296
tr 218,2,61440
ma 24,228,64
tr 228,3,55296
tr 230,3,61440
s_waitcnt lgkmcnt(11)
ma 20,248,56
cv 176,176,177
cv 177,178,179
ma 28,248,64
tr 248,4,55296
tr 250,4,61440
ma 32,236,56
cv 178,180,181
cv 179,182,183
ma 40,236,64
aw 228,176
aw 229,177
s_waitcnt lgkmcnt(6)
ma 36,232,56
aw 230,178
aw 231,179
ma 44,232,64
cv 168,168,169
cv 169,170,171
ma 48,224,56
s_waitcnt vmcnt(6) lgkmcnt(0)
s_barrier
ma 56,224,64
rd 236,5,0
rd 232,6,0
ma 52,220,56
rd 224,5,1024
cv 170,172,173
ma 60,220,64
rd 220,6,1024
cv 171,174,175
ma 64,252,56
aw 224,168
aw 225,169
ma 72,252,64
rd 252,5,2048
aw 226,170
ma 68,216,56
aw 227,171
cv 192,192,193
ma 76,216,64
s_add_u32 m0, s16, 49152
s_add_u32 s23, s18, 663552
dn 11,48,23,0
ma 80,228,56
rd 216,6,2048
cv 193,194,195
ma 88,228,64
rd 228,5,6144
cv 194,196,197
ma 84,248,56
cv 195,198,199
aw 236,192
ma 92,248,64
dn 11,52,23,1024
s_waitcnt lgkmcnt(2)
mvc 40,236,72,32
rd 248,6,6144
aw 237,193
mv 40,232,80
aw 238,194
aw 239,195
mvc 48,232,84,36
rd 232,5,7168
cv 184,184,185
mv 48,236,76
dn 11,56,23,2048
mv 48,224,92
rd 236,6,7168
cv 185,186,187
mv 48,220,100
cv 186,188,189
cv 187,190,191
mv 40,220,96
rd 220,5,8192
aw 232,184
mv 40,224,88
rd 224,6,8192
aw 233,185
mv 40,252,104
aw 234,186
aw 235,187
s_waitcnt lgkmcnt(3)
mv 40,216,112
mv 48,216,116
rd 216,5,12288
mv 48,252,108
rd 252,6,12288
mvc 44,228,72,32
s_nop 2
v_max3_f32 v20, v40, v41, v42
mv 44,248,80
mvc 52,248,84,36
rd 248,5,13312
v_max3_f32 v20, v20, v43, v48
mv 52,228,76
rd 228,6,13312
v_max3_f32 v20, v20, v49, v50
mv 52,232,92
s_waitcnt lgkmcnt(3)
mv 52,236,100
mv 44,236,96
rd 236,5,14336
mv 44,232,88
s_add_u32 m0, s16, 52224
rd 232,6,14336
dn 12,48,23,0
mv 44,220,104
mv 44,224,112
mv 52,224,116
rd 224,5,18432
mv 52,220,108
dn 12,52,23,1024
mvc 56,216,72,32
rd 220,6,18432
s_nop 1
v_max3_f32 v20, v20, v51, v44
s_waitcnt lgkmcnt(3)
mv 56,252,80
v_max3_f32 v20, v20, v45, v46
v_max3_f32 v20, v20, v47, v52
mvc 64,252,84,36
rd 252,5,19456
v_max3_f32 v20, v20, v53, v54
mv 64,216,76
dn 12,56,23,2048
mv 64,248,92
rd 216,6,19456
mv 64,228,100
mv 56,228,96
rd 228,5,20480
mv 56,248,88
rd 248,6,20480
mv 56,236,104
s_waitcnt lgkmcnt(3)
mv 56,232,112
mv 64,232,116
mv 64,236,108
s_waitcnt vmcnt(6) lgkmcnt(0)
s_barrier
mvc 60,224,72,32
tr 232,1,16384
tr 234,1,22528
mv 60,220,80
tr 236,1,28672
tr 238,1,34816
mvc 68,220,84,36
tr 220,1,17408
tr 222,1,23552
mv 68,224,76
tr 224,1,29696
tr 226,1,35840
mv 68,252,92
v_max3_f32 v20, v20, v55, v56
v_max3_f32 v20, v20, v57, v58
mv 68,216,100
v_max3_f32 v20, v20, v59, v64
v_max3_f32 v20, v20, v65, v66
mv 60,216,96
tr 216,1,18432
tr 218,1,24576
mv 60,252,88
s_add_u32 m0, s16, 0
s_add_u32 s24, s18, 688128
dn 9,36,24,0
mv 60,228,104
tr 252,1,30720
tr 254,1,36864
mv 60,248,112
mv 68,248,116
tr 248,2,16384
tr 250,2,22528
mv 68,228,108
dn 9,40,24,1024
mvz 176,242,204
tr 228,2,28672
tr 230,2,34816
mvz 168,242,200
v_max3_f32 v20, v20, v67, v60
v_max3_f32 v20, v20, v61, v62
s_waitcnt lgkmcnt(6)
mv 168,232,120
v_max3_f32 v20, v20, v63, v68
v_max3_f32 v20, v20, v69, v70
mv 168,236,128
dn 9,44,24,2048
mv 176,236,132
tr 236,2,17408
tr 238,2,23552
mv 176,232,124
tr 232,2,29696
tr 234,2,35840
mv 176,220,140
v_max_f32 v20, v20, v71
v_cmp_lt_f32 vcc, s27, v20
s_cbranch_vccz Lc23
s_swappc_b64 s[30:31], s[28:29]
Lc23:
mv 176,224,148
v_exp_f32 v40, v40
mv 168,224,144
tr 224,2,18432
tr 226,2,24576
mv 168,220,136
tr 220,2,30720
tr 222,2,36864
mv 168,216,152
v_exp_f32 v41, v41
s_waitcnt lgkmcnt(6)
mv 168,252,160
v_exp_f32 v42, v42
mv 176,252,164
tr 252,3,16384
tr 254,3,22528
mv 176,216,156
tr 216,3,28672
tr 218,3,34816
mvz 180,240,204
v_exp_f32 v43, v43
mvz 172,240,200
v_exp_f32 v44, v44
mv 172,248,120
v_exp_f32 v45, v45
mv 172,228,128
v_exp_f32 v46, v46
mv 180,228,132
tr 228,3,17408
tr 230,3,23552
mv 180,248,124
tr 248,3,29696
tr 250,3,35840
mv 180,236,140
v_exp_f32 v47, v47
s_waitcnt lgkmcnt(4)
mv 180,232,148
cv 40,40,41
cv 41,42,43
mv 172,232,144
tr 232,3,18432
tr 234,3,24576
mv 172,236,136
s_add_u32 m0, s16, 3072
tr 236,3,30720
dn 10,36,24,0
mv 172,224,152
tr 238,3,36864
cv 42,44,45
mv 172,220,160
cv 43,46,47
v_exp_f32 v48, v48
mv 180,220,164
tr 220,4,16384
tr 222,4,22528
mv 180,224,156
dn 10,40,24,1024
mvz 192,242,212
tr 224,4,28672
tr 226,4,34816
mvz 184,242,208
v_exp_f32 v49, v49
mv 184,252,120
v_exp_f32 v50, v50
mv 184,216,128
dn 10,44,24,2048
mv 192,216,132
tr 216,4,17408
tr 218,4,23552
mv 192,252,124
tr 252,4,29696
tr 254,4,35840
s_waitcnt lgkmcnt(6)
mv 192,228,140
v_exp_f32 v51, v51
mv 192,248,148
v_exp_f32 v52, v52
mv 184,248,144
tr 248,4,18432
tr 250,4,24576
mv 184,228,136
tr 228,4,30720
tr 230,4,36864
mv 184,232,152
v_exp_f32 v53, v53
mv 184,236,160
v_exp_f32 v54, v54
mv 192,236,164
v_exp_f32 v55, v55
mv 192,232,156
cv 48,48,49
cv 49,50,51
mvz 196,240,212
s_waitcnt vmcnt(6) lgkmcnt(0)
s_barrier
mvz 188,240,208
tr 236,1,40960
tr 238,1,47104
mv 188,220,120
tr 232,2,40960
tr 234,2,47104
mv 188,224,128
cv 50,52,53
cv 51,54,55
mv 196,224,132
tr 224,3,40960
tr 226,3,47104
mv 196,220,124
tr 220,4,40960
tr 222,4,47104
mv 196,216,140
v_exp_f32 v56, v56
mv 196,252,148
v_exp_f32 v57, v57
mv 188,252,144
tr 252,1,41984
tr 254,1,48128
mv 188,216,136
s_add_u32 m0, s16, 24576
s_add_u32 s25, s20, 492032
dl 15,72,25,0
mv 188,248,152
tr 216,2,41984
tr 218,2,48128
mv 188,228,160
v_exp_f32 v58, v58
mv 196,228,164
tr 228,3,41984
tr 230,3,48128
mv 196,248,156
dl 15,76,25,1024
v_mfma_f32_16x16x32_bf16 a[244:247], a[240:243], v[40:43], a[244:247]
tr 248,4,41984
tr 250,4,48128
v_mfma_f32_16x16x32_bf16 a[248:251], a[240:243], v[48:51], a[248:251]
v_exp_f32 v59, v59
s_waitcnt lgkmcnt(6)
ma 0,236,40
v_exp_f32 v60, v60
ma 8,236,48
dl 15,80,25,2048
ma 4,232,40
tr 236,1,43008
tr 238,1,49152
ma 12,232,48
tr 232,2,43008
tr 234,2,49152
ma 16,224,40
v_exp_f32 v61, v61
ma 24,224,48
tr 224,3,43008
tr 226,3,49152
ma 20,220,40
v_exp_f32 v62, v62
ma 28,220,48
tr 220,4,43008
tr 222,4,49152
ma 32,252,40
v_exp_f32 v63, v63
ma 40,252,48
tr 252,1,53248
tr 254,1,59392
s_waitcnt lgkmcnt(6)
ma 36,216,40
cv 56,56,57
cv 57,58,59
ma 44,216,48
tr 216,2,53248
tr 218,2,59392
ma 48,228,40
cv 58,60,61
cv 59,62,63
ma 56,228,48
v_exp_f32 v64, v64
ma 52,248,40
v_exp_f32 v65, v65
ma 60,248,48
v_exp_f32 v66, v66
ma 64,236,40
v_exp_f32 v67, v67
ma 72,236,48
v_exp_f32 v68, v68
ma 68,232,40
v_exp_f32 v69, v69
ma 76,232,48
s_add_u32 m0, s16, 27648
v_exp_f32 v70, v70
dl 16,72,25,0
s_waitcnt lgkmcnt(0)
ma 80,224,40
v_exp_f32 v71, v71
ma 88,224,48
cv 64,64,65
tr 228,3,53248
ma 84,220,40
tr 230,3,59392
cv 65,66,67
ma 92,220,48
dl 16,76,25,1024
v_mfma_f32_16x16x32_bf16 a[244:247], a[240:243], v[56:59], a[244:247]
cv 66,68,69
cv 67,70,71
tr 248,4,53248
tr 250,4,59392
v_mfma_f32_16x16x32_bf16 a[248:251], a[240:243], v[64:67], a[248:251]
tr 236,1,54272
tr 238,1,60416
ma 0,252,56
tr 232,2,54272
tr 234,2,60416
dl 16,80,25,2048
ma 8,252,64
tr 224,3,54272
tr 226,3,60416
ma 4,216,56
tr 220,4,54272
tr 222,4,60416
ma 12,216,64
tr 252,1,55296
tr 254,1,61440
s_waitcnt lgkmcnt(12)
ma 16,228,56
tr 216,2,55296
tr 218,2,61440
ma 24,228,64
tr 228,3,55296
tr 230,3,61440
s_waitcnt lgkmcnt(11)
ma 20,248,56
cv 176,176,177
cv 177,178,179
ma 28,248,64
tr 248,4,55296
tr 250,4,61440
ma 32,236,56
cv 178,180,181
cv 179,182,183
ma 40,236,64
wr 176,53248
cv 168,168,169
s_waitcnt lgkmcnt(7)
ma 36,232,56
cv 169,170,171
cv 170,172,173
ma 44,232,64
cv 171,174,175
wr 168,49152
ma 48,224,56
s_waitcnt vmcnt(6) lgkmcnt(0)
s_barrier
ma 56,224,64
rd 236,5,0
rd 232,6,0
ma 52,220,56
rd 224,5,1024
cv 192,192,193
ma 60,220,64
rd 220,6,1024
cv 193,194,195
ma 64,252,56
cv 194,196,197
cv 195,198,199
ma 72,252,64
rd 252,5,2048
wr 192,61440
ma 68,216,56
cv 184,184,185
cv 185,186,187
ma 76,216,64
s_add_u32 m0, s16, 49152
s_add_u32 s22, s18, 688128
dn 11,48,22,0
ma 80,228,56
rd 216,6,2048
cv 186,188,189
ma 88,228,64
rd 228,5,6144
cv 187,190,191
ma 84,248,56
wr 184,57344
rd 212,19,61440
ma 92,248,64
dn 11,52,22,1024
s_waitcnt lgkmcnt(4)
mvc 40,236,72,32
rd 248,6,6144
mv 40,232,80
mvc 48,232,84,36
rd 232,5,7168
mv 48,236,76
dn 11,56,22,2048
mv 48,224,92
rd 236,6,7168
mv 48,220,100
mv 40,220,96
rd 220,5,8192
mv 40,224,88
rd 224,6,8192
mv 40,252,104
s_waitcnt lgkmcnt(3)
mv 40,216,112
mv 48,216,116
rd 216,5,12288
mv 48,252,108
rd 252,6,12288
mvc 44,228,72,32
s_nop 2
v_max3_f32 v20, v40, v41, v42
mv 44,248,80
mvc 52,248,84,36
rd 248,5,13312
v_max3_f32 v20, v20, v43, v48
mv 52,228,76
rd 228,6,13312
v_max3_f32 v20, v20, v49, v50
mv 52,232,92
s_waitcnt lgkmcnt(3)
mv 52,236,100
mv 44,236,96
rd 236,5,14336
mv 44,232,88
s_add_u32 m0, s16, 52224
rd 232,6,14336
dn 12,48,22,0
mv 44,220,104
mv 44,224,112
mv 52,224,116
rd 224,5,18432
mv 52,220,108
dn 12,52,22,1024
mvc 56,216,72,32
rd 220,6,18432
s_nop 1
v_max3_f32 v20, v20, v51, v44
s_waitcnt lgkmcnt(3)
mv 56,252,80
v_max3_f32 v20, v20, v45, v46
v_max3_f32 v20, v20, v47, v52
mvc 64,252,84,36
rd 252,5,19456
v_max3_f32 v20, v20, v53, v54
mv 64,216,76
dn 12,56,22,2048
mv 64,248,92
rd 216,6,19456
mv 64,228,100
mv 56,228,96
rd 228,5,20480
mv 56,248,88
rd 248,6,20480
mv 56,236,104
s_waitcnt lgkmcnt(3)
mv 56,232,112
mv 64,232,116
mv 64,236,108
s_waitcnt vmcnt(6) lgkmcnt(0)
s_barrier
mvc 60,224,72,32
tr 232,1,16384
tr 234,1,22528
mv 60,220,80
tr 236,1,28672
tr 238,1,34816
mvc 68,220,84,36
tr 220,1,17408
tr 222,1,23552
mv 68,224,76
tr 224,1,29696
tr 226,1,35840
mv 68,252,92
v_max3_f32 v20, v20, v55, v56
v_max3_f32 v20, v20, v57, v58
mv 68,216,100
v_max3_f32 v20, v20, v59, v64
v_max3_f32 v20, v20, v65, v66
mv 60,216,96
tr 216,1,18432
tr 218,1,24576
mv 60,252,88
s_add_u32 m0, s16, 0
s_add_u32 s23, s18, 712704
dn 9,36,23,0
mv 60,228,104
tr 252,1,30720
tr 254,1,36864
mv 60,248,112
mv 68,248,116
tr 248,2,16384
tr 250,2,22528
mv 68,228,108
dn 9,40,23,1024
v_mfma_f32_16x16x32_bf16 v[176:179], v[242:245], a[100:103], 0
tr 228,2,28672
tr 230,2,34816
v_mfma_f32_16x16x32_bf16 v[168:171], v[242:245], a[96:99], 0
v_max3_f32 v20, v20, v67, v60
v_max3_f32 v20, v20, v61, v62
s_waitcnt lgkmcnt(6)
mv 168,232,120
v_max3_f32 v20, v20, v63, v68
v_max3_f32 v20, v20, v69, v70
mv 168,236,128
dn 9,44,23,2048
mv 176,236,132
tr 236,2,17408
tr 238,2,23552
mv 176,232,124
tr 232,2,29696
tr 234,2,35840
mv 176,220,140
v_max_f32 v20, v20, v71
v_cmp_lt_f32 vcc, s27, v20
s_cbranch_vccz Lc24
s_swappc_b64 s[30:31], s[28:29]
Lc24:
mv 176,224,148
v_exp_f32 v40, v40
mv 168,224,144
tr 224,2,18432
tr 226,2,24576
mv 168,220,136
tr 220,2,30720
tr 222,2,36864
mv 168,216,152
v_exp_f32 v41, v41
s_waitcnt lgkmcnt(6)
mv 168,252,160
v_exp_f32 v42, v42
mv 176,252,164
tr 252,3,16384
tr 254,3,22528
mv 176,216,156
tr 216,3,28672
tr 218,3,34816
v_mfma_f32_16x16x32_bf16 v[180:183], v[240:243], a[100:103], 0
v_exp_f32 v43, v43
v_mfma_f32_16x16x32_bf16 v[172:175], v[240:243], a[96:99], 0
v_exp_f32 v44, v44
mv 172,248,120
v_exp_f32 v45, v45
mv 172,228,128
v_exp_f32 v46, v46
mv 180,228,132
tr 228,3,17408
tr 230,3,23552
mv 180,248,124
tr 248,3,29696
tr 250,3,35840
mv 180,236,140
v_exp_f32 v47, v47
s_waitcnt lgkmcnt(4)
mv 180,232,148
cv 40,40,41
cv 41,42,43
mv 172,232,144
tr 232,3,18432
tr 234,3,24576
mv 172,236,136
s_add_u32 m0, s16, 3072
tr 236,3,30720
dn 10,36,23,0
mv 172,224,152
tr 238,3,36864
cv 42,44,45
mv 172,220,160
cv 43,46,47
v_exp_f32 v48, v48
mv 180,220,164
tr 220,4,16384
tr 222,4,22528
mv 180,224,156
dn 10,40,23,1024
v_mfma_f32_16x16x32_bf16 v[192:195], v[242:245], a[108:111], 0
tr 224,4,28672
tr 226,4,34816
v_mfma_f32_16x16x32_bf16 v[184:187], v[242:245], a[104:107], 0
v_exp_f32 v49, v49
mv 184,252,120
v_exp_f32 v50, v50
mv 184,216,128
dn 10,44,23,2048
mv 192,216,132
tr 216,4,17408
tr 218,4,23552
mv 192,252,124
tr 252,4,29696
tr 254,4,35840
s_waitcnt lgkmcnt(6)
mv 192,228,140
v_exp_f32 v51, v51
mv 192,248,148
v_exp_f32 v52, v52
mv 184,248,144
tr 248,4,18432
tr 250,4,24576
mv 184,228,136
tr 228,4,30720
tr 230,4,36864
mv 184,232,152
v_exp_f32 v53, v53
mv 184,236,160
v_exp_f32 v54, v54
mv 192,236,164
v_exp_f32 v55, v55
mv 192,232,156
cv 48,48,49
cv 49,50,51
v_mfma_f32_16x16x32_bf16 v[196:199], v[240:243], a[108:111], 0
s_waitcnt vmcnt(6) lgkmcnt(0)
s_barrier
v_mfma_f32_16x16x32_bf16 v[188:191], v[240:243], a[104:107], 0
tr 236,1,40960
tr 238,1,47104
mv 188,220,120
tr 232,2,40960
tr 234,2,47104
mv 188,224,128
cv 50,52,53
cv 51,54,55
mv 196,224,132
tr 224,3,40960
tr 226,3,47104
mv 196,220,124
tr 220,4,40960
tr 222,4,47104
mv 196,216,140
v_exp_f32 v56, v56
mv 196,252,148
v_exp_f32 v57, v57
mv 188,252,144
tr 252,1,41984
tr 254,1,48128
mv 188,216,136
s_add_u32 m0, s16, 24576
s_add_u32 s24, s20, 492160
dl 15,72,24,0
mv 188,248,152
tr 216,2,41984
tr 218,2,48128
mv 188,228,160
v_exp_f32 v58, v58
mv 196,228,164
tr 228,3,41984
tr 230,3,48128
mv 196,248,156
dl 15,76,24,1024
v_mfma_f32_16x16x32_bf16 a[244:247], a[240:243], v[40:43], a[244:247]
tr 248,4,41984
tr 250,4,48128
v_mfma_f32_16x16x32_bf16 a[248:251], a[240:243], v[48:51], a[248:251]
v_exp_f32 v59, v59
s_waitcnt lgkmcnt(6)
ma 0,236,40
v_exp_f32 v60, v60
ma 8,236,48
dl 15,80,24,2048
ma 4,232,40
tr 236,1,43008
tr 238,1,49152
ma 12,232,48
tr 232,2,43008
tr 234,2,49152
ma 16,224,40
v_exp_f32 v61, v61
ma 24,224,48
tr 224,3,43008
tr 226,3,49152
ma 20,220,40
v_exp_f32 v62, v62
ma 28,220,48
tr 220,4,43008
tr 222,4,49152
ma 32,252,40
v_exp_f32 v63, v63
ma 40,252,48
tr 252,1,53248
tr 254,1,59392
s_waitcnt lgkmcnt(6)
ma 36,216,40
cv 56,56,57
cv 57,58,59
ma 44,216,48
tr 216,2,53248
tr 218,2,59392
ma 48,228,40
cv 58,60,61
cv 59,62,63
ma 56,228,48
v_exp_f32 v64, v64
ma 52,248,40
v_exp_f32 v65, v65
ma 60,248,48
v_exp_f32 v66, v66
ma 64,236,40
v_exp_f32 v67, v67
ma 72,236,48
v_exp_f32 v68, v68
ma 68,232,40
v_exp_f32 v69, v69
ma 76,232,48
s_add_u32 m0, s16, 27648
v_exp_f32 v70, v70
dl 16,72,24,0
s_waitcnt lgkmcnt(0)
ma 80,224,40
v_exp_f32 v71, v71
ma 88,224,48
cv 64,64,65
tr 228,3,53248
ma 84,220,40
tr 230,3,59392
cv 65,66,67
ma 92,220,48
dl 16,76,24,1024
v_mfma_f32_16x16x32_bf16 a[244:247], a[240:243], v[56:59], a[244:247]
cv 66,68,69
cv 67,70,71
tr 248,4,53248
tr 250,4,59392
v_mfma_f32_16x16x32_bf16 a[248:251], a[240:243], v[64:67], a[248:251]
tr 236,1,54272
tr 238,1,60416
ma 0,252,56
tr 232,2,54272
tr 234,2,60416
dl 16,80,24,2048
ma 8,252,64
tr 224,3,54272
tr 226,3,60416
ma 4,216,56
tr 220,4,54272
tr 222,4,60416
ma 12,216,64
tr 252,1,55296
tr 254,1,61440
s_waitcnt lgkmcnt(12)
ma 16,228,56
tr 216,2,55296
tr 218,2,61440
ma 24,228,64
tr 228,3,55296
tr 230,3,61440
s_waitcnt lgkmcnt(11)
ma 20,248,56
cv 176,176,177
cv 177,178,179
ma 28,248,64
tr 248,4,55296
tr 250,4,61440
ma 32,236,56
cv 178,180,181
cv 179,182,183
ma 40,236,64
aw 100,176
aw 101,177
s_waitcnt lgkmcnt(6)
ma 36,232,56
aw 102,178
aw 103,179
ma 44,232,64
cv 168,168,169
cv 169,170,171
ma 48,224,56
s_waitcnt vmcnt(6) lgkmcnt(0)
s_barrier
ma 56,224,64
rd 236,5,0
rd 232,6,0
ma 52,220,56
rd 224,5,1024
cv 170,172,173
ma 60,220,64
rd 220,6,1024
cv 171,174,175
ma 64,252,56
aw 96,168
aw 97,169
ma 72,252,64
rd 252,5,2048
aw 98,170
ma 68,216,56
aw 99,171
cv 192,192,193
ma 76,216,64
s_add_u32 m0, s16, 49152
s_add_u32 s25, s18, 712704
dn 11,48,25,0
ma 80,228,56
rd 216,6,2048
cv 193,194,195
ma 88,228,64
rd 228,5,6144
cv 194,196,197
ma 84,248,56
cv 195,198,199
aw 108,192
ma 92,248,64
dn 11,52,25,1024
s_waitcnt lgkmcnt(2)
mvc 40,236,72,32
rd 248,6,6144
aw 109,193
mv 40,232,80
aw 110,194
aw 111,195
mvc 48,232,84,36
rd 232,5,7168
cv 184,184,185
mv 48,236,76
dn 11,56,25,2048
mv 48,224,92
rd 236,6,7168
cv 185,186,187
mv 48,220,100
cv 186,188,189
cv 187,190,191
mv 40,220,96
rd 220,5,8192
aw 104,184
mv 40,224,88
rd 224,6,8192
aw 105,185
mv 40,252,104
aw 106,186
aw 107,187
s_waitcnt lgkmcnt(3)
mv 40,216,112
mv 48,216,116
rd 216,5,12288
mv 48,252,108
rd 252,6,12288
mvc 44,228,72,32
s_nop 2
v_max3_f32 v20, v40, v41, v42
mv 44,248,80
mvc 52,248,84,36
rd 248,5,13312
v_max3_f32 v20, v20, v43, v48
mv 52,228,76
rd 228,6,13312
v_max3_f32 v20, v20, v49, v50
mv 52,232,92
s_waitcnt lgkmcnt(3)
mv 52,236,100
mv 44,236,96
rd 236,5,14336
mv 44,232,88
s_add_u32 m0, s16, 52224
rd 232,6,14336
dn 12,48,25,0
mv 44,220,104
mv 44,224,112
mv 52,224,116
rd 224,5,18432
mv 52,220,108
dn 12,52,25,1024
mvc 56,216,72,32
rd 220,6,18432
s_nop 1
v_max3_f32 v20, v20, v51, v44
s_waitcnt lgkmcnt(3)
mv 56,252,80
v_max3_f32 v20, v20, v45, v46
v_max3_f32 v20, v20, v47, v52
mvc 64,252,84,36
rd 252,5,19456
v_max3_f32 v20, v20, v53, v54
mv 64,216,76
dn 12,56,25,2048
mv 64,248,92
rd 216,6,19456
mv 64,228,100
mv 56,228,96
rd 228,5,20480
mv 56,248,88
rd 248,6,20480
mv 56,236,104
s_waitcnt lgkmcnt(3)
mv 56,232,112
mv 64,232,116
mv 64,236,108
s_waitcnt vmcnt(6) lgkmcnt(0)
s_barrier
mvc 60,224,72,32
tr 232,1,16384
tr 234,1,22528
mv 60,220,80
tr 236,1,28672
tr 238,1,34816
mvc 68,220,84,36
tr 220,1,17408
tr 222,1,23552
mv 68,224,76
tr 224,1,29696
tr 226,1,35840
mv 68,252,92
v_max3_f32 v20, v20, v55, v56
v_max3_f32 v20, v20, v57, v58
mv 68,216,100
v_max3_f32 v20, v20, v59, v64
v_max3_f32 v20, v20, v65, v66
mv 60,216,96
tr 216,1,18432
tr 218,1,24576
mv 60,252,88
s_add_u32 m0, s16, 0
s_add_u32 s22, s18, 737280
dn 9,36,22,0
mv 60,228,104
tr 252,1,30720
tr 254,1,36864
mv 60,248,112
mv 68,248,116
tr 248,2,16384
tr 250,2,22528
mv 68,228,108
dn 9,40,22,1024
v_mfma_f32_16x16x32_bf16 v[176:179], v[242:245], a[116:119], 0
tr 228,2,28672
tr 230,2,34816
v_mfma_f32_16x16x32_bf16 v[168:171], v[242:245], a[112:115], 0
v_max3_f32 v20, v20, v67, v60
v_max3_f32 v20, v20, v61, v62
s_waitcnt lgkmcnt(6)
mv 168,232,120
v_max3_f32 v20, v20, v63, v68
v_max3_f32 v20, v20, v69, v70
mv 168,236,128
dn 9,44,22,2048
mv 176,236,132
tr 236,2,17408
tr 238,2,23552
mv 176,232,124
)"
R"(tr 232,2,29696
tr 234,2,35840
mv 176,220,140
v_max_f32 v20, v20, v71
v_cmp_lt_f32 vcc, s27, v20
s_cbranch_vccz Lc25
s_swappc_b64 s[30:31], s[28:29]
Lc25:
mv 176,224,148
v_exp_f32 v40, v40
mv 168,224,144
tr 224,2,18432
tr 226,2,24576
mv 168,220,136
tr 220,2,30720
tr 222,2,36864
mv 168,216,152
v_exp_f32 v41, v41
s_waitcnt lgkmcnt(6)
mv 168,252,160
v_exp_f32 v42, v42
mv 176,252,164
tr 252,3,16384
tr 254,3,22528
mv 176,216,156
tr 216,3,28672
tr 218,3,34816
v_mfma_f32_16x16x32_bf16 v[180:183], v[240:243], a[116:119], 0
v_exp_f32 v43, v43
v_mfma_f32_16x16x32_bf16 v[172:175], v[240:243], a[112:115], 0
v_exp_f32 v44, v44
mv 172,248,120
v_exp_f32 v45, v45
mv 172,228,128
v_exp_f32 v46, v46
mv 180,228,132
tr 228,3,17408
tr 230,3,23552
mv 180,248,124
tr 248,3,29696
tr 250,3,35840
mv 180,236,140
v_exp_f32 v47, v47
s_waitcnt lgkmcnt(4)
mv 180,232,148
cv 40,40,41
cv 41,42,43
mv 172,232,144
tr 232,3,18432
tr 234,3,24576
mv 172,236,136
s_add_u32 m0, s16, 3072
tr 236,3,30720
dn 10,36,22,0
mv 172,224,152
tr 238,3,36864
cv 42,44,45
mv 172,220,160
cv 43,46,47
v_exp_f32 v48, v48
mv 180,220,164
tr 220,4,16384
tr 222,4,22528
mv 180,224,156
dn 10,40,22,1024
v_mfma_f32_16x16x32_bf16 v[192:195], v[242:245], a[124:127], 0
tr 224,4,28672
tr 226,4,34816
v_mfma_f32_16x16x32_bf16 v[184:187], v[242:245], a[120:123], 0
v_exp_f32 v49, v49
mv 184,252,120
v_exp_f32 v50, v50
mv 184,216,128
dn 10,44,22,2048
mv 192,216,132
tr 216,4,17408
tr 218,4,23552
mv 192,252,124
tr 252,4,29696
tr 254,4,35840
s_waitcnt lgkmcnt(6)
mv 192,228,140
v_exp_f32 v51, v51
mv 192,248,148
v_exp_f32 v52, v52
mv 184,248,144
tr 248,4,18432
tr 250,4,24576
mv 184,228,136
tr 228,4,30720
tr 230,4,36864
mv 184,232,152
v_exp_f32 v53, v53
mv 184,236,160
v_exp_f32 v54, v54
mv 192,236,164
v_exp_f32 v55, v55
mv 192,232,156
cv 48,48,49
cv 49,50,51
v_mfma_f32_16x16x32_bf16 v[196:199], v[240:243], a[124:127], 0
s_waitcnt vmcnt(6) lgkmcnt(0)
s_barrier
v_mfma_f32_16x16x32_bf16 v[188:191], v[240:243], a[120:123], 0
tr 236,1,40960
tr 238,1,47104
mv 188,220,120
tr 232,2,40960
tr 234,2,47104
mv 188,224,128
cv 50,52,53
cv 51,54,55
mv 196,224,132
tr 224,3,40960
tr 226,3,47104
mv 196,220,124
tr 220,4,40960
tr 222,4,47104
mv 196,216,140
v_exp_f32 v56, v56
mv 196,252,148
v_exp_f32 v57, v57
mv 188,252,144
tr 252,1,41984
tr 254,1,48128
mv 188,216,136
s_add_u32 m0, s16, 24576
s_add_u32 s23, s20, 492288
dl 15,72,23,0
mv 188,248,152
tr 216,2,41984
tr 218,2,48128
mv 188,228,160
v_exp_f32 v58, v58
mv 196,228,164
tr 228,3,41984
tr 230,3,48128
mv 196,248,156
dl 15,76,23,1024
v_mfma_f32_16x16x32_bf16 a[244:247], a[240:243], v[40:43], a[244:247]
tr 248,4,41984
tr 250,4,48128
v_mfma_f32_16x16x32_bf16 a[248:251], a[240:243], v[48:51], a[248:251]
v_exp_f32 v59, v59
s_waitcnt lgkmcnt(6)
ma 0,236,40
v_exp_f32 v60, v60
ma 8,236,48
dl 15,80,23,2048
ma 4,232,40
tr 236,1,43008
tr 238,1,49152
ma 12,232,48
tr 232,2,43008
tr 234,2,49152
ma 16,224,40
v_exp_f32 v61, v61
ma 24,224,48
tr 224,3,43008
tr 226,3,49152
ma 20,220,40
v_exp_f32 v62, v62
ma 28,220,48
tr 220,4,43008
tr 222,4,49152
ma 32,252,40
v_exp_f32 v63, v63
ma 40,252,48
tr 252,1,53248
tr 254,1,59392
s_waitcnt lgkmcnt(6)
ma 36,216,40
cv 56,56,57
cv 57,58,59
ma 44,216,48
tr 216,2,53248
tr 218,2,59392
ma 48,228,40
cv 58,60,61
cv 59,62,63
ma 56,228,48
v_exp_f32 v64, v64
ma 52,248,40
v_exp_f32 v65, v65
ma 60,248,48
v_exp_f32 v66, v66
ma 64,236,40
v_exp_f32 v67, v67
ma 72,236,48
v_exp_f32 v68, v68
ma 68,232,40
v_exp_f32 v69, v69
ma 76,232,48
s_add_u32 m0, s16, 27648
v_exp_f32 v70, v70
dl 16,72,23,0
s_waitcnt lgkmcnt(0)
ma 80,224,40
v_exp_f32 v71, v71
ma 88,224,48
cv 64,64,65
tr 228,3,53248
ma 84,220,40
tr 230,3,59392
cv 65,66,67
ma 92,220,48
dl 16,76,23,1024
v_mfma_f32_16x16x32_bf16 a[244:247], a[240:243], v[56:59], a[244:247]
cv 66,68,69
cv 67,70,71
tr 248,4,53248
tr 250,4,59392
v_mfma_f32_16x16x32_bf16 a[248:251], a[240:243], v[64:67], a[248:251]
tr 236,1,54272
tr 238,1,60416
ma 0,252,56
tr 232,2,54272
tr 234,2,60416
dl 16,80,23,2048
ma 8,252,64
tr 224,3,54272
tr 226,3,60416
ma 4,216,56
tr 220,4,54272
tr 222,4,60416
ma 12,216,64
tr 252,1,55296
tr 254,1,61440
s_waitcnt lgkmcnt(12)
ma 16,228,56
tr 216,2,55296
tr 218,2,61440
ma 24,228,64
tr 228,3,55296
tr 230,3,61440
s_waitcnt lgkmcnt(11)
ma 20,248,56
cv 176,176,177
cv 177,178,179
ma 28,248,64
tr 248,4,55296
tr 250,4,61440
ma 32,236,56
cv 178,180,181
cv 179,182,183
ma 40,236,64
aw 116,176
aw 117,177
s_waitcnt lgkmcnt(6)
ma 36,232,56
aw 118,178
aw 119,179
ma 44,232,64
cv 168,168,169
cv 169,170,171
ma 48,224,56
s_waitcnt vmcnt(6) lgkmcnt(0)
s_barrier
ma 56,224,64
rd 236,5,0
rd 232,6,0
ma 52,220,56
rd 224,5,1024
cv 170,172,173
ma 60,220,64
rd 220,6,1024
cv 171,174,175
ma 64,252,56
aw 112,168
aw 113,169
ma 72,252,64
rd 252,5,2048
aw 114,170
ma 68,216,56
aw 115,171
cv 192,192,193
ma 76,216,64
s_add_u32 m0, s16, 49152
s_add_u32 s24, s18, 737280
dn 11,48,24,0
ma 80,228,56
rd 216,6,2048
cv 193,194,195
ma 88,228,64
rd 228,5,6144
cv 194,196,197
ma 84,248,56
cv 195,198,199
aw 124,192
ma 92,248,64
dn 11,52,24,1024
s_waitcnt lgkmcnt(2)
mvc 40,236,72,32
rd 248,6,6144
aw 125,193
mv 40,232,80
aw 126,194
aw 127,195
mvc 48,232,84,36
rd 232,5,7168
cv 184,184,185
mv 48,236,76
dn 11,56,24,2048
mv 48,224,92
rd 236,6,7168
cv 185,186,187
mv 48,220,100
cv 186,188,189
cv 187,190,191
mv 40,220,96
rd 220,5,8192
aw 120,184
mv 40,224,88
rd 224,6,8192
aw 121,185
mv 40,252,104
aw 122,186
aw 123,187
s_waitcnt lgkmcnt(3)
mv 40,216,112
mv 48,216,116
rd 216,5,12288
mv 48,252,108
rd 252,6,12288
mvc 44,228,72,32
s_nop 2
v_max3_f32 v20, v40, v41, v42
mv 44,248,80
mvc 52,248,84,36
rd 248,5,13312
v_max3_f32 v20, v20, v43, v48
mv 52,228,76
rd 228,6,13312
v_max3_f32 v20, v20, v49, v50
mv 52,232,92
s_waitcnt lgkmcnt(3)
mv 52,236,100
mv 44,236,96
rd 236,5,14336
mv 44,232,88
s_add_u32 m0, s16, 52224
rd 232,6,14336
dn 12,48,24,0
mv 44,220,104
mv 44,224,112
mv 52,224,116
rd 224,5,18432
mv 52,220,108
dn 12,52,24,1024
rd 220,6,18432
mvc 56,216,72,32
s_nop 1
v_max3_f32 v20, v20, v51, v44
v_max3_f32 v20, v20, v45, v46
s_waitcnt lgkmcnt(3)
mv 56,252,80
v_max3_f32 v20, v20, v47, v52
v_max3_f32 v20, v20, v53, v54
mvc 64,252,84,36
rd 252,5,19456
dn 12,56,24,2048
mv 64,216,76
rd 216,6,19456
mv 64,248,92
mv 64,228,100
mv 56,228,96
rd 228,5,20480
mv 56,248,88
rd 248,6,20480
mv 56,236,104
s_waitcnt lgkmcnt(3)
mv 56,232,112
mv 64,232,116
mv 64,236,108
s_waitcnt vmcnt(6) lgkmcnt(0)
s_barrier
mvc 60,224,72,32
tr 232,1,16384
tr 234,1,22528
mv 60,220,80
tr 236,1,28672
tr 238,1,34816
mvc 68,220,84,36
tr 220,1,17408
tr 222,1,23552
mv 68,224,76
tr 224,1,29696
tr 226,1,35840
mv 68,252,92
v_max3_f32 v20, v20, v55, v56
v_max3_f32 v20, v20, v57, v58
mv 68,216,100
v_max3_f32 v20, v20, v59, v64
v_max3_f32 v20, v20, v65, v66
mv 60,216,96
tr 216,1,18432
tr 218,1,24576
mv 60,252,88
s_add_u32 m0, s16, 0
s_add_u32 s25, s18, 761856
dn 9,36,25,0
tr 252,1,30720
tr 254,1,36864
mv 60,228,104
mv 60,248,112
mv 68,248,116
tr 248,2,16384
tr 250,2,22528
dn 9,40,25,1024
mv 68,228,108
tr 228,2,28672
tr 230,2,34816
v_mfma_f32_16x16x32_bf16 v[176:179], v[242:245], a[132:135], 0
v_max3_f32 v20, v20, v67, v60
v_max3_f32 v20, v20, v61, v62
v_mfma_f32_16x16x32_bf16 v[168:171], v[242:245], a[128:131], 0
s_waitcnt lgkmcnt(4)
mv 168,232,120
dn 9,44,25,2048
mv 168,236,128
v_max3_f32 v20, v20, v63, v68
v_max3_f32 v20, v20, v69, v70
mv 176,236,132
tr 236,2,17408
tr 238,2,23552
mv 176,232,124
tr 232,2,29696
tr 234,2,35840
mv 176,220,140
v_max_f32 v20, v20, v71
v_cmp_lt_f32 vcc, s27, v20
s_cbranch_vccz Lc26
s_swappc_b64 s[30:31], s[28:29]
Lc26:
mv 176,224,148
v_exp_f32 v40, v40
mv 168,224,144
tr 224,2,18432
tr 226,2,24576
mv 168,220,136
tr 220,2,30720
tr 222,2,36864
mv 168,216,152
v_exp_f32 v41, v41
mv 168,252,160
v_exp_f32 v42, v42
mv 176,252,164
tr 252,3,16384
tr 254,3,22528
mv 176,216,156
tr 216,3,28672
tr 218,3,34816
v_mfma_f32_16x16x32_bf16 v[180:183], v[240:243], a[132:135], 0
v_exp_f32 v43, v43
v_mfma_f32_16x16x32_bf16 v[172:175], v[240:243], a[128:131], 0
v_exp_f32 v44, v44
s_waitcnt lgkmcnt(4)
mv 172,248,120
v_exp_f32 v45, v45
mv 172,228,128
v_exp_f32 v46, v46
mv 180,228,132
tr 228,3,17408
tr 230,3,23552
mv 180,248,124
tr 248,3,29696
tr 250,3,35840
mv 180,236,140
v_exp_f32 v47, v47
mv 180,232,148
cv 40,40,41
cv 41,42,43
mv 172,232,144
tr 232,3,18432
tr 234,3,24576
mv 172,236,136
s_add_u32 m0, s16, 3072
tr 236,3,30720
dn 10,36,25,0
tr 238,3,36864
mv 172,224,152
cv 42,44,45
cv 43,46,47
mv 172,220,160
v_exp_f32 v48, v48
mv 180,220,164
tr 220,4,16384
tr 222,4,22528
mv 180,224,156
dn 10,40,25,1024
tr 224,4,28672
tr 226,4,34816
v_mfma_f32_16x16x32_bf16 v[192:195], v[242:245], a[140:143], 0
v_exp_f32 v49, v49
v_mfma_f32_16x16x32_bf16 v[184:187], v[242:245], a[136:139], 0
v_exp_f32 v50, v50
s_waitcnt lgkmcnt(4)
mv 184,252,120
v_exp_f32 v51, v51
dn 10,44,25,2048
mv 184,216,128
v_exp_f32 v52, v52
mv 192,216,132
tr 216,4,17408
tr 218,4,23552
mv 192,252,124
tr 252,4,29696
tr 254,4,35840
mv 192,228,140
v_exp_f32 v53, v53
mv 192,248,148
v_exp_f32 v54, v54
mv 184,248,144
tr 248,4,18432
tr 250,4,24576
mv 184,228,136
tr 228,4,30720
tr 230,4,36864
mv 184,232,152
v_exp_f32 v55, v55
mv 184,236,160
cv 48,48,49
cv 49,50,51
mv 192,236,164
cv 50,52,53
cv 51,54,55
mv 192,232,156
v_exp_f32 v56, v56
v_mfma_f32_16x16x32_bf16 v[196:199], v[240:243], a[140:143], 0
s_waitcnt vmcnt(6) lgkmcnt(0)
s_barrier
tr 236,1,40960
tr 238,1,47104
tr 232,2,40960
tr 234,2,47104
v_mfma_f32_16x16x32_bf16 v[188:191], v[240:243], a[136:139], 0
v_exp_f32 v57, v57
mv 188,220,120
v_exp_f32 v58, v58
mv 188,224,128
v_exp_f32 v59, v59
mv 196,224,132
tr 224,3,40960
tr 226,3,47104
mv 196,220,124
tr 220,4,40960
tr 222,4,47104
mv 196,216,140
v_exp_f32 v60, v60
mv 196,252,148
v_exp_f32 v61, v61
mv 188,252,144
tr 252,1,41984
tr 254,1,48128
mv 188,216,136
s_add_u32 m0, s16, 24576
s_add_u32 s22, s20, 492416
dl 15,72,22,0
tr 216,2,41984
tr 218,2,48128
mv 188,248,152
v_exp_f32 v62, v62
mv 188,228,160
v_exp_f32 v63, v63
mv 196,228,164
tr 228,3,41984
tr 230,3,48128
dl 15,76,22,1024
mv 196,248,156
tr 248,4,41984
tr 250,4,48128
v_mfma_f32_16x16x32_bf16 a[244:247], a[240:243], v[40:43], a[244:247]
cv 56,56,57
cv 57,58,59
v_mfma_f32_16x16x32_bf16 a[248:251], a[240:243], v[48:51], a[248:251]
cv 58,60,61
cv 59,62,63
s_waitcnt lgkmcnt(4)
ma 0,236,40
dl 15,80,22,2048
ma 8,236,48
tr 236,1,43008
tr 238,1,49152
ma 4,232,40
v_exp_f32 v64, v64
ma 12,232,48
tr 232,2,43008
tr 234,2,49152
ma 16,224,40
v_exp_f32 v65, v65
ma 24,224,48
tr 224,3,43008
tr 226,3,49152
ma 20,220,40
v_exp_f32 v66, v66
ma 28,220,48
tr 220,4,43008
tr 222,4,49152
ma 32,252,40
v_exp_f32 v67, v67
ma 40,252,48
tr 252,1,53248
tr 254,1,59392
ma 36,216,40
v_exp_f32 v68, v68
ma 44,216,48
tr 216,2,53248
tr 218,2,59392
s_waitcnt lgkmcnt(6)
ma 48,228,40
v_exp_f32 v69, v69
ma 56,228,48
tr 228,3,53248
tr 230,3,59392
ma 52,248,40
v_exp_f32 v70, v70
ma 60,248,48
tr 248,4,53248
tr 250,4,59392
ma 64,236,40
v_exp_f32 v71, v71
ma 72,236,48
tr 236,1,54272
tr 238,1,60416
ma 68,232,40
cv 64,64,65
cv 65,66,67
ma 76,232,48
s_add_u32 m0, s16, 27648
tr 232,2,54272
dl 16,72,22,0
tr 234,2,60416
ma 80,224,40
cv 66,68,69
cv 67,70,71
ma 88,224,48
tr 224,3,54272
tr 226,3,60416
s_waitcnt lgkmcnt(6)
ma 84,220,40
cv 176,176,177
cv 177,178,179
dl 16,76,22,1024
ma 92,220,48
tr 220,4,54272
tr 222,4,60416
v_mfma_f32_16x16x32_bf16 a[244:247], a[240:243], v[56:59], a[244:247]
cv 178,180,181
cv 179,182,183
v_mfma_f32_16x16x32_bf16 a[248:251], a[240:243], v[64:67], a[248:251]
aw 132,176
aw 133,177
ma 0,252,56
dl 16,80,22,2048
ma 8,252,64
tr 252,1,55296
tr 254,1,61440
ma 4,216,56
aw 134,178
aw 135,179
ma 12,216,64
tr 216,2,55296
tr 218,2,61440
ma 16,228,56
cv 168,168,169
cv 169,170,171
ma 24,228,64
tr 228,3,55296
tr 230,3,61440
ma 20,248,56
cv 170,172,173
cv 171,174,175
ma 28,248,64
tr 248,4,55296
tr 250,4,61440
s_waitcnt lgkmcnt(6)
ma 32,236,56
aw 128,168
aw 129,169
ma 40,236,64
aw 130,170
aw 131,171
ma 36,232,56
cv 192,192,193
cv 193,194,195
ma 44,232,64
cv 194,196,197
cv 195,198,199
s_waitcnt vmcnt(6) lgkmcnt(0)
s_barrier
ma 48,224,56
rd 236,5,0
rd 232,6,0
ma 56,224,64
rd 224,5,1024
aw 140,192
ma 52,220,56
aw 141,193
aw 142,194
ma 60,220,64
rd 220,6,1024
aw 143,195
ma 64,252,56
cv 184,184,185
cv 185,186,187
ma 72,252,64
rd 252,5,2048
cv 186,188,189
ma 68,216,56
cv 187,190,191
aw 136,184
ma 76,216,64
s_add_u32 m0, s16, 49152
s_add_u32 s23, s18, 761856
dn 11,48,23,0
ma 80,228,56
rd 216,6,2048
aw 137,185
ma 88,228,64
rd 228,5,6144
aw 138,186
ma 84,248,56
aw 139,187
ma 92,248,64
dn 11,52,23,1024
s_waitcnt lgkmcnt(2)
mvc 40,236,72,32
rd 248,6,6144
mv 40,232,80
mvc 48,232,84,36
rd 232,5,7168
mv 48,236,76
dn 11,56,23,2048
mv 48,224,92
rd 236,6,7168
mv 48,220,100
mv 40,220,96
rd 220,5,8192
mv 40,224,88
s_add_u32 s24, s20, 492544
rd 224,6,8192
mv 40,252,104
s_waitcnt lgkmcnt(3)
mv 40,216,112
mv 48,216,116
rd 216,5,12288
mv 48,252,108
rd 252,6,12288
s_add_u32 m0, s16, 52224
mvc 44,228,72,32
dn 12,48,23,0
mv 44,248,80
v_max3_f32 v20, v40, v41, v42
s_nop 1
v_max3_f32 v20, v20, v43, v48
mvc 52,248,84,36
rd 248,5,13312
v_max3_f32 v20, v20, v49, v50
mv 52,228,76
rd 228,6,13312
mv 52,232,92
dn 12,52,23,1024
s_waitcnt lgkmcnt(3)
mv 52,236,100
mv 44,236,96
rd 236,5,14336
mv 44,232,88
s_add_u32 s25, s20, 492672
rd 232,6,14336
mv 44,220,104
dn 12,56,23,2048
mv 44,224,112
mv 52,224,116
rd 224,5,18432
mv 52,220,108
rd 220,6,18432
mvc 56,216,72,32
s_nop 2
v_max3_f32 v20, v20, v51, v44
v_max3_f32 v20, v20, v45, v46
s_waitcnt lgkmcnt(3)
mv 56,252,80
v_max3_f32 v20, v20, v47, v52
mvc 64,252,84,36
rd 252,5,19456
v_max3_f32 v20, v20, v53, v54
mv 64,216,76
rd 216,6,19456
mv 64,248,92
s_add_u32 m0, s16, 73728
mv 64,228,100
dl 15,72,24,0
mv 56,228,96
rd 228,5,20480
mv 56,248,88
s_add_u32 s22, s20, 737664
rd 248,6,20480
mv 56,236,104
s_waitcnt lgkmcnt(3)
mv 56,232,112
dl 15,76,24,1024
mv 64,232,116
mv 64,236,108
mvc 60,224,72,32
s_waitcnt vmcnt(8) lgkmcnt(0)
s_barrier
mv 60,220,80
dl 15,80,24,2048
mvc 68,220,84,36
tr 232,1,16384
tr 234,1,22528
mv 68,224,76
tr 236,1,28672
tr 238,1,34816
mv 68,252,92
tr 220,1,17408
tr 222,1,23552
mv 68,216,100
tr 224,1,29696
tr 226,1,35840
mv 60,216,96
tr 216,1,18432
tr 218,1,24576
mv 60,252,88
s_add_u32 s23, s20, 737280
tr 252,1,30720
mv 60,228,104
tr 254,1,36864
v_max3_f32 v20, v20, v55, v56
mv 60,248,112
v_max3_f32 v20, v20, v57, v58
v_max3_f32 v20, v20, v59, v64
mv 68,248,116
s_add_u32 m0, s16, 76800
tr 248,2,16384
dl 16,72,24,0
mv 68,228,108
tr 250,2,22528
tr 228,2,28672
v_mfma_f32_16x16x32_bf16 v[176:179], v[242:245], a[148:151], 0
tr 230,2,34816
v_max3_f32 v20, v20, v65, v66
v_mfma_f32_16x16x32_bf16 v[168:171], v[242:245], a[144:147], 0
v_max3_f32 v20, v20, v67, v60
v_max3_f32 v20, v20, v61, v62
s_waitcnt lgkmcnt(5)
mv 168,232,120
dl 16,76,24,1024
mv 168,236,128
v_max3_f32 v20, v20, v63, v68
v_max3_f32 v20, v20, v69, v70
mv 176,236,132
tr 236,2,17408
tr 238,2,23552
mv 176,232,124
tr 232,2,29696
tr 234,2,35840
mv 176,220,140
dl 16,80,24,2048
mv 176,224,148
v_max_f32 v20, v20, v71
v_cmp_lt_f32 vcc, s27, v20
s_cbranch_vccz Lc27
s_swappc_b64 s[30:31], s[28:29]
Lc27:
mv 168,224,144
tr 224,2,18432
tr 226,2,24576
mv 168,220,136
tr 220,2,30720
tr 222,2,36864
mv 168,216,152
v_exp_f32 v40, v40
s_waitcnt lgkmcnt(6)
mv 168,252,160
v_exp_f32 v41, v41
mv 176,252,164
tr 252,3,16384
tr 254,3,22528
mv 176,216,156
tr 216,3,28672
tr 218,3,34816
v_mfma_f32_16x16x32_bf16 v[180:183], v[240:243], a[148:151], 0
v_exp_f32 v42, v42
s_add_u32 m0, s16, 98304
v_mfma_f32_16x16x32_bf16 v[172:175], v[240:243], a[144:147], 0
dl 15,72,25,0
mv 172,248,120
v_exp_f32 v43, v43
mv 172,228,128
v_exp_f32 v44, v44
mv 180,228,132
tr 228,3,17408
tr 230,3,23552
mv 180,248,124
dl 15,76,25,1024
mv 180,236,140
tr 248,3,29696
tr 250,3,35840
s_waitcnt lgkmcnt(4)
mv 180,232,148
v_exp_f32 v45, v45
mv 172,232,144
tr 232,3,18432
tr 234,3,24576
mv 172,236,136
dl 15,80,25,2048
mv 172,224,152
tr 236,3,30720
tr 238,3,36864
mv 172,220,160
v_exp_f32 v46, v46
mv 180,220,164
tr 220,4,16384
tr 222,4,22528
mv 180,224,156
tr 224,4,28672
tr 226,4,34816
v_mfma_f32_16x16x32_bf16 v[192:195], v[242:245], a[156:159], 0
v_exp_f32 v47, v47
v_mfma_f32_16x16x32_bf16 v[184:187], v[242:245], a[152:155], 0
cv 40,40,41
cv 41,42,43
mv 184,252,120
cv 42,44,45
cv 43,46,47
mv 184,216,128
v_exp_f32 v48, v48
mv 192,216,132
s_add_u32 m0, s16, 101376
tr 216,4,17408
dl 16,72,25,0
mv 192,252,124
tr 218,4,23552
tr 252,4,29696
s_waitcnt lgkmcnt(3)
mv 192,228,140
tr 254,4,35840
v_exp_f32 v49, v49
mv 192,248,148
v_exp_f32 v50, v50
dl 16,76,25,1024
mv 184,248,144
tr 248,4,18432
tr 250,4,24576
mv 184,228,136
tr 228,4,30720
tr 230,4,36864
mv 184,232,152
v_exp_f32 v51, v51
mv 184,236,160
dl 16,80,25,2048
mv 192,236,164
v_exp_f32 v52, v52
mv 192,232,156
v_exp_f32 v53, v53
v_mfma_f32_16x16x32_bf16 v[196:199], v[240:243], a[156:159], 0
s_waitcnt vmcnt(12) lgkmcnt(0)
s_barrier
v_mfma_f32_16x16x32_bf16 v[188:191], v[240:243], a[152:155], 0
tr 236,1,40960
tr 238,1,47104
mv 188,220,120
tr 232,2,40960
tr 234,2,47104
mv 188,224,128
v_exp_f32 v54, v54
mv 196,224,132
v_exp_f32 v55, v55
mv 196,220,124
cv 48,48,49
tr 224,3,40960
s_add_u32 m0, s16, 122880
mv 196,216,140
dl 15,72,22,0
mv 196,252,148
tr 226,3,47104
cv 49,50,51
mv 188,252,144
cv 50,52,53
cv 51,54,55
mv 188,216,136
s_add_u32 s24, s20, 737408
tr 220,4,40960
mv 188,248,152
dl 15,76,22,1024
mv 188,228,160
tr 222,4,47104
tr 252,1,41984
mv 196,228,164
tr 254,1,48128
tr 216,2,41984
mv 196,248,156
tr 218,2,48128
tr 228,3,41984
v_mfma_f32_16x16x32_bf16 a[244:247], a[240:243], v[40:43], a[244:247]
dl 15,80,22,2048
v_mfma_f32_16x16x32_bf16 a[248:251], a[240:243], v[48:51], a[248:251]
tr 230,3,48128
tr 248,4,41984
s_waitcnt lgkmcnt(8)
ma 0,236,40
tr 250,4,48128
v_exp_f32 v56, v56
ma 8,236,48
tr 236,1,43008
tr 238,1,49152
ma 4,232,40
v_exp_f32 v57, v57
ma 12,232,48
tr 232,2,43008
tr 234,2,49152
ma 16,224,40
v_exp_f32 v58, v58
ma 24,224,48
tr 224,3,43008
tr 226,3,49152
s_waitcnt lgkmcnt(7)
ma 20,220,40
s_add_u32 m0, s16, 125952
v_exp_f32 v59, v59
dl 16,72,22,0
ma 28,220,48
tr 220,4,43008
tr 222,4,49152
ma 32,252,40
v_exp_f32 v60, v60
ma 40,252,48
tr 252,1,53248
tr 254,1,59392
ma 36,216,40
dl 16,76,22,1024
ma 44,216,48
tr 216,2,53248
tr 218,2,59392
ma 48,228,40
v_exp_f32 v61, v61
ma 56,228,48
v_exp_f32 v62, v62
s_waitcnt lgkmcnt(4)
ma 52,248,40
dl 16,80,22,2048
ma 60,248,48
v_exp_f32 v63, v63
ma 64,236,40
cv 56,56,57
cv 57,58,59
ma 72,236,48
cv 58,60,61
cv 59,62,63
ma 68,232,40
v_exp_f32 v64, v64
ma 76,232,48
v_exp_f32 v65, v65
ma 80,224,40
v_exp_f32 v66, v66
ma 88,224,48
v_exp_f32 v67, v67
ma 84,220,40
v_exp_f32 v68, v68
ma 92,220,48
s_add_u32 m0, s16, 0
v_exp_f32 v69, v69
dl 15,72,23,0
v_exp_f32 v70, v70
v_exp_f32 v71, v71
cv 64,64,65
tr 228,3,53248
tr 230,3,59392
v_mfma_f32_16x16x32_bf16 a[244:247], a[240:243], v[56:59], a[244:247]
cv 65,66,67
cv 66,68,69
cv 67,70,71
tr 248,4,53248
tr 250,4,59392
dl 15,76,23,1024
v_mfma_f32_16x16x32_bf16 a[248:251], a[240:243], v[64:67], a[248:251]
tr 236,1,54272
tr 238,1,60416
s_waitcnt lgkmcnt(6)
ma 0,252,56
tr 232,2,54272
tr 234,2,60416
ma 8,252,64
tr 224,3,54272
tr 226,3,60416
ma 4,216,56
dl 15,80,23,2048
ma 12,216,64
tr 220,4,54272
tr 222,4,60416
s_waitcnt lgkmcnt(9)
ma 16,228,56
tr 252,1,55296
tr 254,1,61440
ma 24,228,64
tr 216,2,55296
tr 218,2,61440
s_waitcnt lgkmcnt(10)
ma 20,248,56
tr 228,3,55296
tr 230,3,61440
ma 28,248,64
tr 248,4,55296
tr 250,4,61440
ma 32,236,56
v_add_u32 v1, 0xc000, v1
cv 176,176,177
ma 40,236,64
cv 177,178,179
cv 178,180,181
s_waitcnt lgkmcnt(8)
ma 36,232,56
s_add_u32 m0, s16, 3072
cv 179,182,183
dl 16,72,23,0
ma 44,232,64
s_waitcnt vmcnt(16) lgkmcnt(0)
s_barrier
ma 48,224,56
tr 236,1,16384
tr 238,1,22528
ma 56,224,64
tr 232,1,28672
tr 234,1,34816
ma 52,220,56
dl 16,76,23,1024
ma 60,220,64
aw 148,176
aw 149,177
ma 64,252,56
aw 150,178
aw 151,179
ma 72,252,64
tr 224,1,17408
tr 226,1,23552
ma 68,216,56
dl 16,80,23,2048
ma 76,216,64
s_add_u32 s25, s20, 737536
cv 168,168,169
ma 80,228,56
cv 169,170,171
cv 170,172,173
ma 88,228,64
cv 171,174,175
aw 144,168
ma 84,248,56
aw 145,169
aw 146,170
ma 92,248,64
aw 147,171
tr 220,1,29696
v_mfma_f32_16x16x32_bf16 v[176:179], v[242:245], a[164:167], 0
tr 222,1,35840
tr 252,1,18432
v_mfma_f32_16x16x32_bf16 v[168:171], v[242:245], a[160:163], 0
tr 254,1,24576
tr 216,1,30720
s_waitcnt lgkmcnt(5)
mv 168,236,120
tr 218,1,36864
v_add_u32 v2, 0xc000, v2
s_add_u32 m0, s16, 24576
mv 168,232,128
dl 15,72,24,0
mv 176,232,132
tr 228,2,16384
tr 230,2,22528
mv 176,236,124
tr 248,2,28672
tr 250,2,34816
mv 176,224,140
tr 232,2,17408
tr 234,2,23552
tr 236,2,29696
tr 238,2,35840
dl 15,76,24,1024
s_waitcnt lgkmcnt(9)
mv 176,220,148
v_add_u32 v3, 0xc000, v3
cv 192,192,193
mv 168,220,144
tr 220,2,18432
tr 222,2,24576
mv 168,224,136
tr 224,2,30720
tr 226,2,36864
mv 168,252,152
dl 15,80,24,2048
s_waitcnt lgkmcnt(8)
mv 168,216,160
cv 193,194,195
cv 194,196,197
mv 176,216,164
tr 216,3,16384
tr 218,3,22528
mv 176,252,156
tr 252,3,28672
tr 254,3,34816
v_mfma_f32_16x16x32_bf16 v[180:183], v[240:243], a[164:167], 0
cv 195,198,199
aw 156,192
v_mfma_f32_16x16x32_bf16 v[172:175], v[240:243], a[160:163], 0
aw 157,193
aw 158,194
mv 172,228,120
aw 159,195
cv 184,184,185
mv 172,248,128
cv 185,186,187
cv 186,188,189
mv 180,248,132
tr 248,3,17408
tr 250,3,23552
s_add_u32 m0, s16, 27648
mv 180,228,124
dl 16,72,24,0
s_waitcnt lgkmcnt(2)
mv 180,232,140
cv 187,190,191
aw 152,184
mv 180,236,148
aw 153,185
aw 154,186
mv 172,236,144
aw 155,187
tr 228,3,29696
mv 172,232,136
dl 16,76,24,1024
mv 172,220,152
tr 230,3,35840
tr 236,3,18432
mv 172,224,160
tr 238,3,24576
tr 232,3,30720
mv 180,224,164
tr 234,3,36864
v_add_u32 v4, 0xc000, v4
mv 180,220,156
dl 16,80,24,2048
v_mfma_f32_16x16x32_bf16 v[192:195], v[242:245], a[172:175], 0
tr 224,4,16384
tr 226,4,22528
v_mfma_f32_16x16x32_bf16 v[184:187], v[242:245], a[168:171], 0
tr 220,4,28672
tr 222,4,34816
mv 184,216,120
cv 176,176,177
cv 177,178,179
mv 184,252,128
cv 178,180,181
cv 179,182,183
mv 192,252,132
tr 252,4,17408
tr 254,4,23552
mv 192,216,124
tr 216,4,29696
tr 218,4,35840
s_waitcnt lgkmcnt(8)
mv 192,248,140
aw 164,176
aw 165,177
mv 192,228,148
aw 166,178
aw 167,179
s_add_u32 m0, s16, 49152
mv 184,228,144
dl 15,72,25,0
mv 184,248,136
tr 228,4,18432
tr 230,4,24576
mv 184,236,152
tr 248,4,30720
tr 250,4,36864
mv 184,232,160
cv 168,168,169
cv 169,170,171
mv 192,232,164
dl 15,76,25,1024
mv 192,236,156
cv 170,172,173
cv 171,174,175
v_mfma_f32_16x16x32_bf16 v[196:199], v[240:243], a[172:175], 0
aw 160,168
aw 161,169
v_mfma_f32_16x16x32_bf16 v[188:191], v[240:243], a[168:171], 0
s_waitcnt vmcnt(20) lgkmcnt(0)
s_barrier
mv 188,224,120
dl 15,80,25,2048
mv 188,220,128
tr 232,1,40960
tr 234,1,47104
mv 196,220,132
tr 236,1,53248
tr 238,1,59392
mv 196,224,124
tr 220,1,41984
tr 222,1,48128
mv 196,252,140
aw 162,170
aw 163,171
mv 196,216,148
tr 224,1,54272
tr 226,1,60416
mv 188,216,144
tr 216,1,43008
tr 218,1,49152
mv 188,252,136
s_add_u32 s22, s20, 737792
tr 252,1,55296
mv 188,228,152
tr 254,1,61440
cv 192,192,193
mv 188,248,160
s_add_u32 m0, s16, 52224
cv 193,194,195
dl 16,72,25,0
mv 196,248,164
tr 248,2,40960
tr 250,2,47104
mv 196,228,156
tr 228,2,53248
tr 230,2,59392
v_mfma_f32_16x16x32_bf16 v[176:179], v[242:245], a[180:183], 0
aw 172,192
aw 173,193
v_mfma_f32_16x16x32_bf16 v[168:171], v[242:245], a[176:179], 0
dl 16,76,25,1024
s_waitcnt lgkmcnt(5)
mv 168,232,120
cv 194,196,197
cv 195,198,199
mv 168,236,128
aw 174,194
aw 175,195
mv 176,236,132
tr 236,2,41984
tr 238,2,48128
mv 176,232,124
dl 16,80,25,2048
mv 176,220,140
tr 232,2,54272
tr 234,2,60416
mv 176,224,148
cv 184,184,185
cv 185,186,187
mv 168,224,144
tr 224,2,43008
tr 226,2,49152
mv 168,220,136
tr 220,2,55296
tr 222,2,61440
mv 168,216,152
cv 186,188,189
cv 187,190,191
s_waitcnt lgkmcnt(6)
mv 168,252,160
aw 168,184
aw 169,185
mv 176,252,164
tr 252,3,40960
tr 254,3,47104
mv 176,216,156
tr 216,3,53248
tr 218,3,59392
s_add_u32 m0, s16, 73728
v_mfma_f32_16x16x32_bf16 v[180:183], v[240:243], a[180:183], 0
dl 15,72,22,0
v_mfma_f32_16x16x32_bf16 v[172:175], v[240:243], a[176:179], 0
aw 170,186
aw 171,187
mv 172,248,120
ar 208,244
v_rcp_f32 v208, v208
mv 172,228,128
ar 204,4
ar 205,5
mv 180,228,132
dl 15,76,22,1024
mv 180,248,124
tr 228,3,41984
tr 230,3,48128
mv 180,236,140
tr 248,3,54272
tr 250,3,60416
s_waitcnt lgkmcnt(4)
mv 180,232,148
ar 206,6
ar 207,7
mv 172,232,144
dl 15,80,22,2048
mv 172,236,136
tr 232,3,43008
tr 234,3,49152
mv 172,224,152
tr 236,3,55296
tr 238,3,61440
mv 172,220,160
v_mul_f32 v204, v208, v204
v_mul_f32 v205, v208, v205
mv 180,220,164
tr 220,4,40960
tr 222,4,47104
mv 180,224,156
tr 224,4,53248
tr 226,4,59392
v_mfma_f32_16x16x32_bf16 v[192:195], v[242:245], a[188:191], 0
v_mul_f32 v206, v208, v206
v_mul_f32 v207, v208, v207
v_mfma_f32_16x16x32_bf16 v[184:187], v[242:245], a[184:187], 0
cv 74,204,205
cv 75,206,207
mv 184,252,120
ar 204,20
ar 205,21
mv 184,216,128
s_add_u32 m0, s16, 76800
ar 206,22
dl 16,72,22,0
mv 192,216,132
tr 216,4,41984
tr 218,4,48128
mv 192,252,124
tr 252,4,54272
tr 254,4,60416
s_waitcnt lgkmcnt(4)
mv 192,228,140
ar 207,23
v_mul_f32 v204, v208, v204
mv 192,248,148
dl 16,76,22,1024
mv 184,248,144
tr 248,4,43008
tr 250,4,49152
mv 184,228,136
tr 228,4,55296
tr 230,4,61440
mv 184,232,152
v_mul_f32 v205, v208, v205
v_mul_f32 v206, v208, v206
mv 184,236,160
dl 16,80,22,2048
mv 192,236,164
v_mul_f32 v207, v208, v207
ar 200,0
mv 192,232,156
ar 201,1
ar 202,2
v_mfma_f32_16x16x32_bf16 v[196:199], v[240:243], a[188:191], 0
s_waitcnt vmcnt(24) lgkmcnt(0)
s_barrier
v_mfma_f32_16x16x32_bf16 v[188:191], v[240:243], a[184:187], 0
ar 203,3
cv 82,204,205
mv 188,220,120
cv 83,206,207
v_mul_f32 v200, v208, v200
mv 188,224,128
v_mul_f32 v201, v208, v201
v_mul_f32 v202, v208, v202
mv 196,224,132
v_mul_f32 v203, v208, v203
ar 204,36
mv 196,220,124
ar 205,37
ar 206,38
ar 207,39
cv 72,200,201
cv 73,202,203
v_mul_f32 v204, v208, v204
v_mul_f32 v205, v208, v205
v_mul_f32 v206, v208, v206
v_mul_f32 v207, v208, v207
ar 200,16
ar 201,17
ar 202,18
ar 203,19
cv 90,204,205
cv 91,206,207
mv 196,216,140
v_mul_f32 v200, v208, v200
v_mul_f32 v201, v208, v201
v_mul_f32 v202, v208, v202
v_mul_f32 v203, v208, v203
ar 204,52
ar 205,53
ar 206,54
ar 207,55
cv 80,200,201
cv 81,202,203
v_mul_f32 v204, v208, v204
v_mul_f32 v205, v208, v205
v_mul_f32 v206, v208, v206
v_mul_f32 v207, v208, v207
ar 200,32
ar 201,33
ar 202,34
ar 203,35
)"
R"(cv 98,204,205
cv 99,206,207
v_mul_f32 v200, v208, v200
v_mul_f32 v201, v208, v201
v_mul_f32 v202, v208, v202
v_mul_f32 v203, v208, v203
ar 204,68
ar 205,69
ar 206,70
ar 207,71
cv 88,200,201
cv 89,202,203
v_mul_f32 v204, v208, v204
v_mul_f32 v205, v208, v205
v_mul_f32 v206, v208, v206
v_mul_f32 v207, v208, v207
mv 196,252,148
ar 200,48
ar 201,49
ar 202,50
ar 203,51
cv 106,204,205
cv 107,206,207
v_mul_f32 v200, v208, v200
v_mul_f32 v201, v208, v201
v_mul_f32 v202, v208, v202
v_mul_f32 v203, v208, v203
ar 204,84
ar 205,85
ar 206,86
ar 207,87
cv 96,200,201
cv 97,202,203
v_mul_f32 v204, v208, v204
v_mul_f32 v205, v208, v205
v_mul_f32 v206, v208, v206
v_mul_f32 v207, v208, v207
ar 200,64
ar 201,65
ar 202,66
ar 203,67
cv 114,204,205
cv 115,206,207
v_mul_f32 v200, v208, v200
v_mul_f32 v201, v208, v201
v_mul_f32 v202, v208, v202
v_mul_f32 v203, v208, v203
rd 204,19,53248
mv 188,252,144
cv 104,200,201
cv 105,202,203
ar 200,80
ar 201,81
ar 202,82
ar 203,83
v_mul_f32 v200, v208, v200
v_mul_f32 v201, v208, v201
v_mul_f32 v202, v208, v202
v_mul_f32 v203, v208, v203
cv 112,200,201
cv 113,202,203
rd 200,19,49152
mv 188,216,136
s_add_u32 m0, s16, 98304
s_add_u32 s23, s20, 737920
dl 15,72,23,0
v_add_u32 v1, 0x6000, v1
tr 236,1,40960
tr 238,1,47104
mv 188,248,152
tr 232,1,53248
tr 234,1,59392
mv 188,228,160
cv 176,176,177
cv 177,178,179
mv 196,228,164
cv 178,180,181
dl 15,76,23,1024
mv 196,248,156
cv 179,182,183
aw 180,176
aw 181,177
aw 182,178
aw 183,179
tr 224,1,41984
tr 226,1,48128
s_waitcnt lgkmcnt(6)
mvz 176,242,204
cv 168,168,169
cv 169,170,171
dl 15,80,23,2048
cv 170,172,173
cv 171,174,175
aw 176,168
aw 177,169
aw 178,170
aw 179,171
tr 220,1,54272
tr 222,1,60416
mvz 168,242,200
ar 209,248
v_rcp_f32 v209, v209
s_waitcnt lgkmcnt(4)
mv 168,236,72
ar 24,8
ar 25,9
ar 26,10
ar 27,11
ar 28,12
ar 29,13
ar 30,14
ar 31,15
v_mul_f32 v24, v209, v24
v_mul_f32 v25, v209, v25
v_mul_f32 v26, v209, v26
v_mul_f32 v27, v209, v27
v_mul_f32 v28, v209, v28
v_mul_f32 v29, v209, v29
v_mul_f32 v30, v209, v30
v_mul_f32 v31, v209, v31
cv 76,24,25
cv 77,26,27
cv 78,28,29
cv 79,30,31
ar 24,24
ar 25,25
ar 26,26
ar 27,27
ar 28,28
ar 29,29
ar 30,30
ar 31,31
mv 168,232,80
v_mul_f32 v24, v209, v24
v_mul_f32 v25, v209, v25
v_mul_f32 v26, v209, v26
v_mul_f32 v27, v209, v27
v_mul_f32 v28, v209, v28
v_mul_f32 v29, v209, v29
v_mul_f32 v30, v209, v30
v_mul_f32 v31, v209, v31
cv 84,24,25
cv 85,26,27
cv 86,28,29
cv 87,30,31
tr 252,1,43008
tr 254,1,49152
mv 176,232,84
ar 24,40
ar 25,41
ar 26,42
ar 27,43
ar 28,44
ar 29,45
ar 30,46
ar 31,47
tr 216,1,55296
tr 218,1,61440
mv 176,236,76
v_mul_f32 v24, v209, v24
v_mul_f32 v25, v209, v25
v_mul_f32 v26, v209, v26
v_mul_f32 v27, v209, v27
v_mul_f32 v28, v209, v28
v_mul_f32 v29, v209, v29
v_mul_f32 v30, v209, v30
v_mul_f32 v31, v209, v31
cv 92,24,25
cv 93,26,27
cv 94,28,29
cv 95,30,31
ar 24,56
ar 25,57
ar 26,58
ar 27,59
ar 28,60
ar 29,61
ar 30,62
ar 31,63
s_waitcnt lgkmcnt(2)
mv 176,224,92
v_mul_f32 v24, v209, v24
v_mul_f32 v25, v209, v25
v_mul_f32 v26, v209, v26
v_mul_f32 v27, v209, v27
v_mul_f32 v28, v209, v28
v_mul_f32 v29, v209, v29
v_mul_f32 v30, v209, v30
v_mul_f32 v31, v209, v31
cv 100,24,25
cv 101,26,27
cv 102,28,29
cv 103,30,31
v_add_u32 v2, 0x6000, v2
tr 228,2,40960
mv 176,220,100
tr 230,2,47104
ar 24,72
mv 168,220,96
ar 25,73
ar 26,74
mv 168,224,88
ar 27,75
ar 28,76
mv 168,252,104
ar 29,77
ar 30,78
ar 31,79
v_mul_f32 v24, v209, v24
v_mul_f32 v25, v209, v25
v_mul_f32 v26, v209, v26
v_mul_f32 v27, v209, v27
v_mul_f32 v28, v209, v28
v_mul_f32 v29, v209, v29
v_mul_f32 v30, v209, v30
v_mul_f32 v31, v209, v31
cv 108,24,25
cv 109,26,27
cv 110,28,29
cv 111,30,31
ar 24,88
ar 25,89
ar 26,90
ar 27,91
ar 28,92
ar 29,93
ar 30,94
ar 31,95
tr 248,2,53248
tr 250,2,59392
s_waitcnt lgkmcnt(2)
mv 168,216,112
v_mul_f32 v24, v209, v24
v_mul_f32 v25, v209, v25
v_mul_f32 v26, v209, v26
v_mul_f32 v27, v209, v27
v_mul_f32 v28, v209, v28
v_mul_f32 v29, v209, v29
v_mul_f32 v30, v209, v30
v_mul_f32 v31, v209, v31
cv 116,24,25
cv 117,26,27
cv 118,28,29
cv 119,30,31
tr 232,2,41984
tr 234,2,48128
mv 176,216,116
tr 236,2,54272
tr 238,2,60416
mv 176,252,108
tr 220,2,43008
tr 222,2,49152
mvz 180,240,204
tr 224,2,55296
tr 226,2,61440
mvz 172,240,200
rd 208,19,57344
v_add_u32 v3, 0x6000, v3
mv 172,228,72
tr 216,3,40960
tr 218,3,47104
s_waitcnt lgkmcnt(11)
mv 172,248,80
tr 252,3,53248
tr 254,3,59392
mv 180,248,84
cv 192,192,193
cv 193,194,195
mv 180,228,76
cv 194,196,197
cv 195,198,199
s_waitcnt lgkmcnt(7)
mv 180,232,92
aw 188,192
aw 189,193
mv 180,236,100
aw 190,194
aw 191,195
mv 172,236,96
tr 248,3,41984
tr 250,3,48128
mv 172,232,88
s_add_u32 m0, s16, 101376
cv 184,184,185
dl 16,72,23,0
mv 172,220,104
cv 185,186,187
cv 186,188,189
s_waitcnt lgkmcnt(2)
mv 172,224,112
cv 187,190,191
aw 184,184
mv 180,224,116
aw 185,185
aw 186,186
mv 180,220,108
dl 16,76,23,1024
mvz 192,242,212
aw 187,187
tr 228,3,54272
tr 230,3,60416
mvz 184,242,208
tr 236,3,43008
tr 238,3,49152
mv 184,216,72
tr 232,3,55296
tr 234,3,61440
mv 184,252,80
dl 16,80,23,2048
mv 192,252,84
v_add_u32 v4, 0x6000, v4
tr 224,4,40960
mv 192,216,76
tr 226,4,47104
tr 220,4,53248
s_waitcnt lgkmcnt(9)
mv 192,248,92
tr 222,4,59392
tr 252,4,41984
tr 254,4,48128
tr 216,4,54272
tr 218,4,60416
v_add_u32 v1, 0xfffec000, v1
s_waitcnt lgkmcnt(10)
mv 192,228,100
cv 176,176,177
cv 177,178,179
mv 184,228,96
tr 228,4,43008
tr 230,4,49152
mv 184,248,88
tr 248,4,55296
tr 250,4,61440
mv 184,236,104
cv 178,180,181
cv 179,182,183
s_waitcnt lgkmcnt(9)
mv 184,232,112
aw 4,176
aw 5,177
mv 192,232,116
aw 6,178
aw 7,179
mv 192,236,108
cv 168,168,169
cv 169,170,171
mvz 196,240,212
s_waitcnt vmcnt(24) lgkmcnt(0)
s_barrier
mvz 188,240,208
tr 232,1,0
tr 234,1,6144
mv 188,224,72
tr 236,1,12288
tr 238,1,18432
mv 188,220,80
cv 170,172,173
cv 171,174,175
mv 196,220,84
tr 220,1,1024
tr 222,1,7168
mv 196,224,76
aw 0,168
aw 1,169
mv 196,252,92
aw 2,170
aw 3,171
mv 196,216,100
tr 224,1,13312
tr 226,1,19456
mv 188,216,96
tr 216,1,2048
tr 218,1,8192
mv 188,252,88
s_add_u32 m0, s16, 122880
s_add_u32 s25, s20, 738048
dl 15,72,25,0
mv 188,228,104
tr 252,1,14336
tr 254,1,20480
mv 188,248,112
v_add_u32 v2, 0xfffec000, v2
v_add_u32 v3, 0xfffec000, v3
mv 196,248,116
tr 248,2,0
tr 250,2,6144
mv 196,228,108
dl 15,76,25,1024
v_mfma_f32_16x16x32_bf16 v[176:179], v[242:245], a[196:199], 0
tr 228,2,12288
tr 230,2,18432
v_mfma_f32_16x16x32_bf16 v[168:171], v[242:245], a[192:195], 0
cv 192,192,193
cv 193,194,195
s_waitcnt lgkmcnt(6)
mv 168,232,72
cv 194,196,197
cv 195,198,199
mv 168,236,80
dl 15,80,25,2048
mv 176,236,84
tr 236,2,1024
tr 238,2,7168
mv 176,232,76
tr 232,2,13312
tr 234,2,19456
mv 176,220,92
aw 12,192
aw 13,193
mv 176,224,100
aw 14,194
aw 15,195
mv 168,224,96
tr 224,2,2048
tr 226,2,8192
mv 168,220,88
tr 220,2,14336
tr 222,2,20480
mv 168,216,104
cv 184,184,185
cv 185,186,187
s_waitcnt lgkmcnt(6)
mv 168,252,112
cv 186,188,189
cv 187,190,191
mv 176,252,116
tr 252,3,0
tr 254,3,6144
mv 176,216,108
tr 216,3,12288
tr 218,3,18432
v_mfma_f32_16x16x32_bf16 v[180:183], v[240:243], a[196:199], 0
aw 8,184
aw 9,185
v_mfma_f32_16x16x32_bf16 v[172:175], v[240:243], a[192:195], 0
aw 10,186
aw 11,187
mv 172,248,72
v_add_u32 v4, 0xfffec000, v4
cv 176,176,177
mv 172,228,80
cv 177,178,179
aw 196,176
mv 180,228,84
tr 228,3,1024
tr 230,3,7168
mv 180,248,76
tr 248,3,13312
tr 250,3,19456
mv 180,236,92
aw 197,177
cv 168,168,169
s_waitcnt lgkmcnt(4)
mv 180,232,100
cv 169,170,171
aw 192,168
mv 172,232,96
tr 232,3,2048
tr 234,3,8192
mv 172,236,88
s_add_u32 m0, s16, 125952
tr 236,3,14336
dl 16,72,25,0
mv 172,224,104
tr 238,3,20480
aw 193,169
mv 172,220,112
s_mov_b32 s24, 12288
s_mov_b32 s24, 4096
mv 180,220,116
tr 220,4,0
tr 222,4,6144
mv 180,224,108
dl 16,76,25,1024
v_mfma_f32_16x16x32_bf16 v[192:195], v[242:245], a[204:207], 0
tr 224,4,12288
tr 226,4,18432
v_mfma_f32_16x16x32_bf16 v[184:187], v[242:245], a[200:203], 0
cv 170,172,173
cv 171,174,175
mv 184,252,72
cv 178,180,181
cv 179,182,183
mv 184,216,80
dl 16,80,25,2048
mv 192,216,84
tr 216,4,1024
tr 218,4,7168
mv 192,252,76
tr 252,4,13312
tr 254,4,19456
s_waitcnt lgkmcnt(6)
mv 192,228,92
aw 198,178
aw 199,179
mv 192,248,100
aw 194,170
aw 195,171
mv 184,248,96
tr 248,4,2048
tr 250,4,8192
mv 184,228,88
tr 228,4,14336
tr 230,4,20480
mv 184,232,104
s_mov_b32 s24, 12288
s_mov_b32 s22, 0
mv 184,236,112
s_mov_b32 s22, 8192
s_mov_b32 s22, 4096
mv 192,236,116
mv 192,232,108
v_mfma_f32_16x16x32_bf16 v[196:199], v[240:243], a[204:207], 0
s_waitcnt vmcnt(24) lgkmcnt(0)
s_barrier
v_mfma_f32_16x16x32_bf16 v[188:191], v[240:243], a[200:203], 0
tr 236,1,24576
tr 238,1,30720
mv 188,220,72
tr 232,1,36864
tr 234,1,43008
mv 188,224,80
cv 192,192,193
cv 193,194,195
mv 196,224,84
tr 224,1,25600
tr 226,1,31744
mv 196,220,76
tr 220,1,37888
tr 222,1,44032
mv 196,216,92
aw 204,192
aw 205,193
mv 196,252,100
cv 184,184,185
cv 185,186,187
mv 188,252,96
tr 252,1,26624
tr 254,1,32768
mv 188,216,88
s_add_u32 m0, s16, 0
s_add_u32 s23, s20, 738176
dl 15,72,23,0
mv 188,248,104
tr 216,1,38912
tr 218,1,45056
mv 188,228,112
aw 200,184
aw 201,185
mv 196,228,116
tr 228,2,24576
tr 230,2,30720
mv 196,248,108
dl 15,76,23,1024
v_mfma_f32_16x16x32_bf16 v[176:179], v[242:245], a[212:215], 0
tr 248,2,36864
tr 250,2,43008
v_mfma_f32_16x16x32_bf16 v[168:171], v[242:245], a[208:211], 0
cv 186,188,189
cv 187,190,191
s_waitcnt lgkmcnt(6)
mv 168,236,72
cv 194,196,197
cv 195,198,199
mv 168,232,80
dl 15,80,23,2048
mv 176,232,84
tr 232,2,25600
tr 234,2,31744
mv 176,236,76
tr 236,2,37888
tr 238,2,44032
mv 176,224,92
aw 206,194
aw 207,195
mv 176,220,100
aw 202,186
aw 203,187
mv 168,220,96
tr 220,2,26624
tr 222,2,32768
mv 168,224,88
tr 224,2,38912
tr 226,2,45056
mv 168,252,104
v_add_u32 v1, 0x8000, v1
v_add_u32 v2, 0x8000, v2
s_waitcnt lgkmcnt(6)
mv 168,216,112
mv 176,216,116
tr 216,3,24576
tr 218,3,30720
mv 176,252,108
tr 252,3,36864
tr 254,3,43008
v_mfma_f32_16x16x32_bf16 v[180:183], v[240:243], a[212:215], 0
s_nop 0
cv 168,168,169
cv 169,170,171
v_mfma_f32_16x16x32_bf16 v[172:175], v[240:243], a[208:211], 0
aw 208,168
aw 209,169
mv 172,228,72
cv 176,176,177
cv 177,178,179
mv 172,248,80
aw 212,176
aw 213,177
mv 180,248,84
tr 248,3,25600
tr 250,3,31744
mv 180,228,76
tr 228,3,37888
tr 230,3,44032
mv 180,232,92
s_waitcnt lgkmcnt(4)
mv 180,236,100
mv 172,236,96
tr 236,3,26624
tr 238,3,32768
mv 172,232,88
s_add_u32 m0, s16, 3072
tr 232,3,38912
dl 16,72,23,0
mv 172,220,104
tr 234,3,45056
v_add_u32 v3, 0x8000, v3
mv 172,224,112
mv 180,224,116
tr 224,4,24576
tr 226,4,30720
mv 180,220,108
dl 16,76,23,1024
v_mfma_f32_16x16x32_bf16 v[192:195], v[242:245], a[220:223], 0
tr 220,4,36864
tr 222,4,43008
v_mfma_f32_16x16x32_bf16 v[184:187], v[242:245], a[216:219], 0
cv 170,172,173
cv 171,174,175
mv 184,216,72
cv 178,180,181
cv 179,182,183
mv 184,252,80
dl 16,80,23,2048
mv 192,252,84
tr 252,4,25600
tr 254,4,31744
mv 192,216,76
tr 216,4,37888
tr 218,4,44032
s_waitcnt lgkmcnt(6)
mv 192,248,92
aw 214,178
aw 215,179
mv 192,228,100
aw 210,170
aw 211,171
mv 184,228,96
tr 228,4,26624
tr 230,4,32768
mv 184,248,88
tr 248,4,38912
tr 250,4,45056
mv 184,236,104
v_add_u32 v4, 0x8000, v4
mv 184,232,112
mv 192,232,116
mv 192,236,108
v_mfma_f32_16x16x32_bf16 v[196:199], v[240:243], a[220:223], 0
s_waitcnt vmcnt(24) lgkmcnt(0)
s_barrier
v_mfma_f32_16x16x32_bf16 v[188:191], v[240:243], a[216:219], 0
tr 232,1,16384
tr 234,1,22528
mv 188,224,72
tr 236,1,28672
tr 238,1,34816
mv 188,220,80
cv 192,192,193
cv 193,194,195
mv 196,220,84
tr 220,1,17408
tr 222,1,23552
mv 196,224,76
tr 224,1,29696
tr 226,1,35840
mv 196,252,92
aw 220,192
aw 221,193
mv 196,216,100
cv 184,184,185
cv 185,186,187
mv 188,216,96
tr 216,1,18432
tr 218,1,24576
mv 188,252,88
s_add_u32 m0, s16, 24576
s_add_u32 s25, s20, 738304
dl 15,72,25,0
mv 188,228,104
tr 252,1,30720
tr 254,1,36864
mv 188,248,112
aw 216,184
aw 217,185
mv 196,248,116
tr 248,2,16384
tr 250,2,22528
mv 196,228,108
dl 15,76,25,1024
v_mfma_f32_16x16x32_bf16 v[176:179], v[242:245], a[228:231], 0
tr 228,2,28672
tr 230,2,34816
v_mfma_f32_16x16x32_bf16 v[168:171], v[242:245], a[224:227], 0
cv 186,188,189
cv 187,190,191
s_waitcnt lgkmcnt(6)
mv 168,232,72
cv 194,196,197
cv 195,198,199
mv 168,236,80
dl 15,80,25,2048
mv 176,236,84
tr 236,2,17408
tr 238,2,23552
mv 176,232,76
tr 232,2,29696
tr 234,2,35840
mv 176,220,92
aw 222,194
aw 223,195
mv 176,224,100
aw 218,186
aw 219,187
mv 168,224,96
tr 224,2,18432
tr 226,2,24576
mv 168,220,88
tr 220,2,30720
tr 222,2,36864
mv 168,216,104
s_waitcnt lgkmcnt(6)
mv 168,252,112
mv 176,252,116
tr 252,3,16384
tr 254,3,22528
mv 176,216,108
tr 216,3,28672
tr 218,3,34816
v_mfma_f32_16x16x32_bf16 v[180:183], v[240:243], a[228:231], 0
s_nop 0
cv 168,168,169
cv 169,170,171
v_mfma_f32_16x16x32_bf16 v[172:175], v[240:243], a[224:227], 0
aw 224,168
aw 225,169
mv 172,248,72
cv 176,176,177
cv 177,178,179
mv 172,228,80
aw 228,176
aw 229,177
mv 180,228,84
tr 228,3,17408
tr 230,3,23552
mv 180,248,76
tr 248,3,29696
tr 250,3,35840
mv 180,236,92
s_waitcnt lgkmcnt(4)
mv 180,232,100
mv 172,232,96
tr 232,3,18432
tr 234,3,24576
mv 172,236,88
s_add_u32 m0, s16, 27648
tr 236,3,30720
dl 16,72,25,0
mv 172,224,104
tr 238,3,36864
mv 172,220,112
mv 180,220,116
tr 220,4,16384
tr 222,4,22528
mv 180,224,108
dl 16,76,25,1024
v_mfma_f32_16x16x32_bf16 v[192:195], v[242:245], a[236:239], 0
tr 224,4,28672
tr 226,4,34816
v_mfma_f32_16x16x32_bf16 v[184:187], v[242:245], a[232:235], 0
cv 170,172,173
cv 171,174,175
mv 184,252,72
cv 178,180,181
cv 179,182,183
mv 184,216,80
dl 16,80,25,2048
mv 192,216,84
tr 216,4,17408
tr 218,4,23552
mv 192,252,76
tr 252,4,29696
tr 254,4,35840
s_waitcnt lgkmcnt(6)
mv 192,228,92
aw 230,178
aw 231,179
mv 192,248,100
aw 226,170
aw 227,171
mv 184,248,96
tr 248,4,18432
tr 250,4,24576
mv 184,228,88
tr 228,4,30720
tr 230,4,36864
mv 184,232,104
s_mov_b32 s25, 0
mv 184,236,112
mv 192,236,116
mv 192,232,108
v_mfma_f32_16x16x32_bf16 v[196:199], v[240:243], a[236:239], 0
s_waitcnt vmcnt(24) lgkmcnt(0)
s_barrier
v_mfma_f32_16x16x32_bf16 v[188:191], v[240:243], a[232:235], 0
tr 236,1,40960
tr 238,1,47104
mv 188,220,72
tr 232,1,53248
tr 234,1,59392
mv 188,224,80
cv 192,192,193
cv 193,194,195
mv 196,224,84
tr 224,1,41984
tr 226,1,48128
mv 196,220,76
tr 220,1,54272
tr 222,1,60416
mv 196,216,92
aw 236,192
aw 237,193
mv 196,252,100
cv 184,184,185
cv 185,186,187
mv 188,252,96
tr 252,1,43008
tr 254,1,49152
mv 188,216,88
s_add_u32 m0, s16, 49152
s_add_u32 s23, s20, 738432
dl 15,72,23,0
mv 188,248,104
tr 216,1,55296
tr 218,1,61440
mv 188,228,112
aw 232,184
aw 233,185
mv 196,228,116
tr 228,2,40960
tr 230,2,47104
mv 196,248,108
dl 15,76,23,1024
v_mfma_f32_16x16x32_bf16 v[176:179], v[242:245], a[100:103], 0
tr 248,2,53248
tr 250,2,59392
v_mfma_f32_16x16x32_bf16 v[168:171], v[242:245], a[96:99], 0
cv 186,188,189
cv 187,190,191
s_waitcnt lgkmcnt(6)
mv 168,236,72
cv 194,196,197
cv 195,198,199
mv 168,232,80
dl 15,80,23,2048
mv 176,232,84
tr 232,2,41984
tr 234,2,48128
mv 176,236,76
tr 236,2,54272
tr 238,2,60416
mv 176,224,92
aw 238,194
aw 239,195
mv 176,220,100
aw 234,186
aw 235,187
mv 168,220,96
tr 220,2,43008
tr 222,2,49152
mv 168,224,88
tr 224,2,55296
tr 226,2,61440
mv 168,252,104
v_add_u32 v1, 0xc000, v1
v_add_u32 v2, 0xc000, v2
s_waitcnt lgkmcnt(6)
mv 168,216,112
mv 176,216,116
tr 216,3,40960
tr 218,3,47104
mv 176,252,108
tr 252,3,53248
tr 254,3,59392
v_mfma_f32_16x16x32_bf16 v[180:183], v[240:243], a[100:103], 0
s_nop 0
cv 168,168,169
cv 169,170,171
v_mfma_f32_16x16x32_bf16 v[172:175], v[240:243], a[96:99], 0
mv 172,228,72
cv 176,176,177
cv 177,178,179
mv 172,248,80
mv 180,248,84
tr 248,3,41984
tr 250,3,48128
mv 180,228,76
tr 228,3,54272
tr 230,3,60416
mv 180,232,92
s_waitcnt lgkmcnt(4)
mv 180,236,100
mv 172,236,96
tr 236,3,43008
tr 238,3,49152
mv 172,232,88
s_add_u32 m0, s16, 52224
tr 232,3,55296
dl 16,72,23,0
mv 172,220,104
tr 234,3,61440
v_add_u32 v3, 0xc000, v3
mv 172,224,112
mv 180,224,116
tr 224,4,40960
tr 226,4,47104
mv 180,220,108
dl 16,76,23,1024
v_mfma_f32_16x16x32_bf16 v[192:195], v[242:245], a[108:111], 0
tr 220,4,53248
tr 222,4,59392
v_mfma_f32_16x16x32_bf16 v[184:187], v[242:245], a[104:107], 0
cv 170,172,173
cv 171,174,175
mv 184,216,72
cv 178,180,181
cv 179,182,183
mv 184,252,80
dl 16,80,23,2048
mv 192,252,84
s_mov_b32 s23, 8192
tr 252,4,41984
mv 192,216,76
tr 254,4,48128
tr 216,4,54272
s_waitcnt lgkmcnt(5)
mv 192,248,92
tr 218,4,60416
mv 192,228,100
sta 0,24,0
mv 184,228,96
tr 228,4,43008
tr 230,4,49152
mv 184,248,88
tr 248,4,55296
tr 250,4,61440
mv 184,236,104
v_add_u32 v4, 0xc000, v4
mv 184,232,112
sta 4,24,1024
mv 192,232,116
mv 192,236,108
v_mfma_f32_16x16x32_bf16 v[196:199], v[240:243], a[108:111], 0
s_waitcnt vmcnt(26) lgkmcnt(0)
s_barrier
v_mfma_f32_16x16x32_bf16 v[188:191], v[240:243], a[104:107], 0
sta 8,24,2048
mv 188,224,72
tr 232,1,16384
tr 234,1,22528
mv 188,220,80
tr 236,1,28672
tr 238,1,34816
mv 196,220,84
tr 220,1,17408
tr 222,1,23552
mv 196,224,76
sta 12,24,3072
mv 196,252,92
tr 224,1,29696
s_mov_b32 s24, 16384
mv 196,216,100
tr 226,1,35840
cv 184,184,185
mv 188,216,96
tr 216,1,18432
tr 218,1,24576
mv 188,252,88
sta 192,25,0
mv 188,228,104
tr 252,1,30720
tr 254,1,36864
mv 188,248,112
cv 185,186,187
cv 192,192,193
mv 196,248,116
tr 248,2,16384
tr 250,2,22528
mv 196,228,108
sta 196,25,1024
tr 228,2,28672
tr 230,2,34816
cv 186,188,189
cv 187,190,191
cv 193,194,195
s_nop 1
cv 194,196,197
cv 195,198,199
sta 200,25,2048
sta 204,25,3072
s_mov_b32 s25, 20480
sta 208,22,0
sta 212,22,1024
sta 216,22,2048
sta 220,22,3072
s_mov_b32 s22, 24576
sta 224,23,0
sta 228,23,1024
sta 232,23,2048
sta 236,23,3072
s_mov_b32 s23, 28672
st 168,24,0
st 176,24,1024
v_mfma_f32_16x16x32_bf16 v[176:179], v[242:245], a[116:119], 0
v_mfma_f32_16x16x32_bf16 v[168:171], v[242:245], a[112:115], 0
s_waitcnt lgkmcnt(0)
mv 168,232,72
st 184,24,2048
mv 168,236,80
mv 176,236,84
tr 236,2,17408
tr 238,2,23552
mv 176,232,76
tr 232,2,29696
tr 234,2,35840
mv 176,220,92
st 192,24,3072
mv 176,224,100
s_mov_b32 s24, 32768
mv 168,224,96
tr 224,2,18432
tr 226,2,24576
mv 168,220,88
tr 220,2,30720
tr 222,2,36864
mv 168,216,104
mv 168,252,112
mv 176,252,116
tr 252,3,16384
tr 254,3,22528
mv 176,216,108
tr 216,3,28672
tr 218,3,34816
v_mfma_f32_16x16x32_bf16 v[180:183], v[240:243], a[116:119], 0
s_nop 0
cv 168,168,169
cv 169,170,171
v_mfma_f32_16x16x32_bf16 v[172:175], v[240:243], a[112:115], 0
mv 172,248,72
cv 176,176,177
cv 177,178,179
mv 172,228,80
mv 180,228,84
tr 228,3,17408
tr 230,3,23552
mv 180,248,76
tr 248,3,29696
tr 250,3,35840
s_waitcnt lgkmcnt(4)
mv 180,236,92
mv 180,232,100
mv 172,232,96
tr 232,3,18432
tr 234,3,24576
mv 172,236,88
tr 236,3,30720
tr 238,3,36864
mv 172,224,104
mv 172,220,112
mv 180,220,116
tr 220,4,16384
tr 222,4,22528
mv 180,224,108
tr 224,4,28672
tr 226,4,34816
v_mfma_f32_16x16x32_bf16 v[192:195], v[242:245], a[124:127], 0
s_nop 0
cv 170,172,173
cv 171,174,175
v_mfma_f32_16x16x32_bf16 v[184:187], v[242:245], a[120:123], 0
st 168,25,0
mv 184,252,72
cv 178,180,181
cv 179,182,183
mv 184,216,80
mv 192,216,84
tr 216,4,17408
tr 218,4,23552
mv 192,252,76
tr 252,4,29696
tr 254,4,35840
s_waitcnt lgkmcnt(4)
mv 192,228,92
st 176,25,1024
mv 192,248,100
mv 184,248,96
tr 248,4,18432
tr 250,4,24576
mv 184,228,88
tr 228,4,30720
tr 230,4,36864
mv 184,232,104
mv 184,236,112
mv 192,236,116
mv 192,232,108
v_mfma_f32_16x16x32_bf16 v[196:199], v[240:243], a[124:127], 0
s_waitcnt vmcnt(40) lgkmcnt(0)
s_barrier
v_mfma_f32_16x16x32_bf16 v[188:191], v[240:243], a[120:123], 0
tr 236,1,40960
tr 238,1,47104
mv 188,220,72
tr 232,1,53248
tr 234,1,59392
mv 188,224,80
cv 184,184,185
cv 185,186,187
mv 196,224,84
tr 224,1,41984
tr 226,1,48128
mv 196,220,76
tr 220,1,54272
tr 222,1,60416
mv 196,216,92
cv 192,192,193
cv 193,194,195
mv 196,252,100
mv 188,252,96
tr 252,1,43008
tr 254,1,49152
mv 188,216,88
tr 216,1,55296
tr 218,1,61440
mv 188,248,104
v_add_u32 v1, 0xfffec000, v1
mv 188,228,112
mv 196,228,116
tr 228,2,40960
tr 230,2,47104
mv 196,248,108
tr 248,2,53248
tr 250,2,59392
v_mfma_f32_16x16x32_bf16 v[176:179], v[242:245], a[132:135], 0
s_nop 0
cv 186,188,189
cv 187,190,191
v_mfma_f32_16x16x32_bf16 v[168:171], v[242:245], a[128:131], 0
st 184,25,2048
s_waitcnt lgkmcnt(4)
mv 168,236,72
cv 194,196,197
cv 195,198,199
mv 168,232,80
mv 176,232,84
tr 232,2,41984
tr 234,2,48128
mv 176,236,76
tr 236,2,54272
tr 238,2,60416
mv 176,224,92
st 192,25,3072
mv 176,220,100
s_mov_b32 s25, 36864
mv 168,220,96
tr 220,2,43008
tr 222,2,49152
mv 168,224,88
tr 224,2,55296
tr 226,2,61440
mv 168,252,104
v_add_u32 v2, 0xfffec000, v2
mv 168,216,112
mv 176,216,116
tr 216,3,40960
tr 218,3,47104
mv 176,252,108
tr 252,3,53248
tr 254,3,59392
v_mfma_f32_16x16x32_bf16 v[180:183], v[240:243], a[132:135], 0
s_nop 0
cv 168,168,169
cv 169,170,171
v_mfma_f32_16x16x32_bf16 v[172:175], v[240:243], a[128:131], 0
s_waitcnt lgkmcnt(4)
mv 172,228,72
cv 176,176,177
cv 177,178,179
mv 172,248,80
mv 180,248,84
tr 248,3,41984
tr 250,3,48128
mv 180,228,76
tr 228,3,54272
tr 230,3,60416
mv 180,232,92
mv 180,236,100
mv 172,236,96
tr 236,3,43008
tr 238,3,49152
mv 172,232,88
tr 232,3,55296
tr 234,3,61440
mv 172,220,104
v_add_u32 v3, 0xfffec000, v3
mv 172,224,112
mv 180,224,116
tr 224,4,40960
tr 226,4,47104
mv 180,220,108
tr 220,4,53248
tr 222,4,59392
v_mfma_f32_16x16x32_bf16 v[192:195], v[242:245], a[140:143], 0
s_nop 0
cv 170,172,173
cv 171,174,175
v_mfma_f32_16x16x32_bf16 v[184:187], v[242:245], a[136:139], 0
st 168,22,0
s_waitcnt lgkmcnt(4)
mv 184,216,72
cv 178,180,181
cv 179,182,183
mv 184,252,80
mv 192,252,84
tr 252,4,41984
tr 254,4,48128
mv 192,216,76
tr 216,4,54272
tr 218,4,60416
mv 192,248,92
st 176,22,1024
mv 192,228,100
mv 184,228,96
tr 228,4,43008
tr 230,4,49152
mv 184,248,88
tr 248,4,55296
tr 250,4,61440
mv 184,236,104
v_add_u32 v4, 0xfffec000, v4
mv 184,232,112
mv 192,232,116
mv 192,236,108
v_mfma_f32_16x16x32_bf16 v[196:199], v[240:243], a[140:143], 0
s_waitcnt vmcnt(38) lgkmcnt(0)
s_barrier
v_mfma_f32_16x16x32_bf16 v[188:191], v[240:243], a[136:139], 0
tr 232,1,0
tr 234,1,6144
mv 188,224,72
tr 236,1,12288
tr 238,1,18432
mv 188,220,80
cv 184,184,185
cv 185,186,187
mv 196,220,84
tr 220,1,1024
tr 222,1,7168
mv 196,224,76
tr 224,1,13312
tr 226,1,19456
mv 196,252,92
cv 192,192,193
cv 193,194,195
mv 196,216,100
mv 188,216,96
tr 216,1,2048
tr 218,1,8192
mv 188,252,88
tr 252,1,14336
tr 254,1,20480
mv 188,228,104
mv 188,248,112
mv 196,248,116
tr 248,2,0
tr 250,2,6144
mv 196,228,108
tr 228,2,12288
tr 230,2,18432
v_mfma_f32_16x16x32_bf16 v[176:179], v[242:245], a[148:151], 0
s_nop 0
cv 186,188,189
cv 187,190,191
v_mfma_f32_16x16x32_bf16 v[168:171], v[242:245], a[144:147], 0
st 184,22,2048
s_waitcnt lgkmcnt(4)
mv 168,232,72
cv 194,196,197
cv 195,198,199
mv 168,236,80
mv 176,236,84
tr 236,2,1024
tr 238,2,7168
mv 176,232,76
tr 232,2,13312
tr 234,2,19456
mv 176,220,92
st 192,22,3072
mv 176,224,100
mv 168,224,96
tr 224,2,2048
tr 226,2,8192
mv 168,220,88
tr 220,2,14336
tr 222,2,20480
mv 168,216,104
mv 168,252,112
mv 176,252,116
tr 252,3,0
tr 254,3,6144
mv 176,216,108
tr 216,3,12288
tr 218,3,18432
v_mfma_f32_16x16x32_bf16 v[180:183], v[240:243], a[148:151], 0
s_nop 0
cv 168,168,169
cv 169,170,171
v_mfma_f32_16x16x32_bf16 v[172:175], v[240:243], a[144:147], 0
s_waitcnt lgkmcnt(4)
mv 172,248,72
cv 176,176,177
cv 177,178,179
mv 172,228,80
mv 180,228,84
tr 228,3,1024
tr 230,3,7168
mv 180,248,76
tr 248,3,13312
tr 250,3,19456
mv 180,236,92
mv 180,232,100
mv 172,232,96
tr 232,3,2048
tr 234,3,8192
mv 172,236,88
tr 236,3,14336
tr 238,3,20480
mv 172,224,104
mv 172,220,112
mv 180,220,116
tr 220,4,0
tr 222,4,6144
mv 180,224,108
tr 224,4,12288
tr 226,4,18432
v_mfma_f32_16x16x32_bf16 v[192:195], v[242:245], a[156:159], 0
s_nop 0
cv 170,172,173
cv 171,174,175
v_mfma_f32_16x16x32_bf16 v[184:187], v[242:245], a[152:155], 0
st 168,23,0
s_waitcnt lgkmcnt(4)
mv 184,252,72
cv 178,180,181
cv 179,182,183
mv 184,216,80
mv 192,216,84
tr 216,4,1024
tr 218,4,7168
mv 192,252,76
tr 252,4,13312
tr 254,4,19456
mv 192,228,92
st 176,23,1024
mv 192,248,100
mv 184,248,96
tr 248,4,2048
tr 250,4,8192
mv 184,228,88
tr 228,4,14336
tr 230,4,20480
mv 184,232,104
mv 184,236,112
mv 192,236,116
mv 192,232,108
v_mfma_f32_16x16x32_bf16 v[196:199], v[240:243], a[156:159], 0
s_waitcnt vmcnt(36) lgkmcnt(0)
s_barrier
v_mfma_f32_16x16x32_bf16 v[188:191], v[240:243], a[152:155], 0
tr 236,1,24576
tr 238,1,30720
mv 188,220,72
tr 232,1,36864
tr 234,1,43008
mv 188,224,80
cv 184,184,185
cv 185,186,187
mv 196,224,84
tr 224,1,25600
tr 226,1,31744
mv 196,220,76
tr 220,1,37888
tr 222,1,44032
mv 196,216,92
cv 192,192,193
cv 193,194,195
mv 196,252,100
mv 188,252,96
tr 252,1,26624
tr 254,1,32768
mv 188,216,88
tr 216,1,38912
tr 218,1,45056
mv 188,248,104
v_add_u32 v1, 0x2000, v1
mv 188,228,112
mv 196,228,116
tr 228,2,24576
tr 230,2,30720
mv 196,248,108
tr 248,2,36864
tr 250,2,43008
v_mfma_f32_16x16x32_bf16 v[176:179], v[242:245], a[164:167], 0
s_nop 0
cv 186,188,189
cv 187,190,191
v_mfma_f32_16x16x32_bf16 v[168:171], v[242:245], a[160:163], 0
st 184,23,2048
s_waitcnt lgkmcnt(4)
mv 168,236,72
cv 194,196,197
cv 195,198,199
mv 168,232,80
mv 176,232,84
tr 232,2,25600
tr 234,2,31744
mv 176,236,76
tr 236,2,37888
tr 238,2,44032
mv 176,224,92
st 192,23,3072
mv 176,220,100
mv 168,220,96
tr 220,2,26624
tr 222,2,32768
mv 168,224,88
tr 224,2,38912
tr 226,2,45056
mv 168,252,104
v_add_u32 v2, 0x2000, v2
mv 168,216,112
mv 176,216,116
tr 216,3,24576
tr 218,3,30720
mv 176,252,108
tr 252,3,36864
tr 254,3,43008
v_mfma_f32_16x16x32_bf16 v[180:183], v[240:243], a[164:167], 0
s_nop 0
cv 168,168,169
cv 169,170,171
v_mfma_f32_16x16x32_bf16 v[172:175], v[240:243], a[160:163], 0
s_waitcnt lgkmcnt(4)
mv 172,228,72
cv 176,176,177
cv 177,178,179
mv 172,248,80
mv 180,248,84
tr 248,3,25600
tr 250,3,31744
mv 180,228,76
tr 228,3,37888
tr 230,3,44032
mv 180,232,92
mv 180,236,100
mv 172,236,96
tr 236,3,26624
tr 238,3,32768
mv 172,232,88
tr 232,3,38912
tr 234,3,45056
mv 172,220,104
v_add_u32 v3, 0x2000, v3
mv 172,224,112
mv 180,224,116
tr 224,4,24576
tr 226,4,30720
mv 180,220,108
tr 220,4,36864
tr 222,4,43008
v_mfma_f32_16x16x32_bf16 v[192:195], v[242:245], a[172:175], 0
)"
R"(s_nop 0
cv 170,172,173
cv 171,174,175
v_mfma_f32_16x16x32_bf16 v[184:187], v[242:245], a[168:171], 0
st 168,24,0
s_waitcnt lgkmcnt(4)
mv 184,216,72
cv 178,180,181
cv 179,182,183
mv 184,252,80
mv 192,252,84
tr 252,4,25600
tr 254,4,31744
mv 192,216,76
tr 216,4,37888
tr 218,4,44032
mv 192,248,92
st 176,24,1024
mv 192,228,100
mv 184,228,96
tr 228,4,26624
tr 230,4,32768
mv 184,248,88
tr 248,4,38912
tr 250,4,45056
mv 184,236,104
v_add_u32 v4, 0x2000, v4
mv 184,232,112
mv 192,232,116
mv 192,236,108
v_mfma_f32_16x16x32_bf16 v[196:199], v[240:243], a[172:175], 0
s_waitcnt vmcnt(34) lgkmcnt(0)
s_barrier
v_mfma_f32_16x16x32_bf16 v[188:191], v[240:243], a[168:171], 0
tr 232,1,40960
tr 234,1,47104
mv 188,224,72
tr 236,1,53248
tr 238,1,59392
mv 188,220,80
cv 184,184,185
cv 185,186,187
mv 196,220,84
tr 220,1,41984
tr 222,1,48128
mv 196,224,76
tr 224,1,54272
tr 226,1,60416
mv 196,252,92
cv 192,192,193
cv 193,194,195
mv 196,216,100
mv 188,216,96
tr 216,1,43008
tr 218,1,49152
mv 188,252,88
tr 252,1,55296
tr 254,1,61440
mv 188,228,104
mv 188,248,112
mv 196,248,116
tr 248,2,40960
tr 250,2,47104
mv 196,228,108
tr 228,2,53248
tr 230,2,59392
v_mfma_f32_16x16x32_bf16 v[176:179], v[242:245], a[180:183], 0
s_nop 0
cv 186,188,189
cv 187,190,191
v_mfma_f32_16x16x32_bf16 v[168:171], v[242:245], a[176:179], 0
st 184,24,2048
s_waitcnt lgkmcnt(4)
mv 168,232,72
cv 194,196,197
cv 195,198,199
mv 168,236,80
mv 176,236,84
tr 236,2,41984
tr 238,2,48128
mv 176,232,76
tr 232,2,54272
tr 234,2,60416
mv 176,220,92
st 192,24,3072
mv 176,224,100
mv 168,224,96
tr 224,2,43008
tr 226,2,49152
mv 168,220,88
tr 220,2,55296
tr 222,2,61440
mv 168,216,104
mv 168,252,112
mv 176,252,116
tr 252,3,40960
tr 254,3,47104
mv 176,216,108
tr 216,3,53248
tr 218,3,59392
v_mfma_f32_16x16x32_bf16 v[180:183], v[240:243], a[180:183], 0
s_nop 0
cv 168,168,169
cv 169,170,171
v_mfma_f32_16x16x32_bf16 v[172:175], v[240:243], a[176:179], 0
s_waitcnt lgkmcnt(4)
mv 172,248,72
cv 176,176,177
cv 177,178,179
mv 172,228,80
mv 180,228,84
tr 228,3,41984
tr 230,3,48128
mv 180,248,76
tr 248,3,54272
tr 250,3,60416
mv 180,236,92
mv 180,232,100
mv 172,232,96
tr 232,3,43008
tr 234,3,49152
mv 172,236,88
tr 236,3,55296
tr 238,3,61440
mv 172,224,104
mv 172,220,112
mv 180,220,116
tr 220,4,40960
tr 222,4,47104
mv 180,224,108
tr 224,4,53248
tr 226,4,59392
v_mfma_f32_16x16x32_bf16 v[192:195], v[242:245], a[188:191], 0
s_nop 0
cv 170,172,173
cv 171,174,175
v_mfma_f32_16x16x32_bf16 v[184:187], v[242:245], a[184:187], 0
st 168,25,0
s_waitcnt lgkmcnt(4)
mv 184,252,72
cv 178,180,181
cv 179,182,183
mv 184,216,80
mv 192,216,84
tr 216,4,41984
tr 218,4,48128
mv 192,252,76
tr 252,4,54272
tr 254,4,60416
mv 192,228,92
st 176,25,1024
mv 192,248,100
mv 184,248,96
tr 248,4,43008
tr 250,4,49152
mv 184,228,88
tr 228,4,55296
tr 230,4,61440
mv 184,232,104
mv 184,236,112
mv 192,236,116
mv 192,232,108
v_mfma_f32_16x16x32_bf16 v[196:199], v[240:243], a[188:191], 0
s_nop 4
cv 184,184,185
cv 185,186,187
v_mfma_f32_16x16x32_bf16 v[188:191], v[240:243], a[184:187], 0
s_waitcnt lgkmcnt(0)
mv 188,220,72
cv 192,192,193
cv 193,194,195
mv 188,224,80
mv 196,224,84
mv 196,220,76
mv 196,216,92
mv 196,252,100
mv 188,252,96
mv 188,216,88
mv 188,248,104
mv 188,228,112
mv 196,228,116
mv 196,248,108
s_nop 5
cv 186,188,189
cv 187,190,191
st 184,25,2048
cv 194,196,197
cv 195,198,199
st 192,25,3072
s_waitcnt vmcnt(0)
s_branch Lend
Lsub:
s_nop 7
s_nop 7
v_max3_f32 v20, v40, v41, 0
v_max3_f32 v20, v20, v42, v43
v_max3_f32 v20, v20, v44, v45
v_max3_f32 v20, v20, v46, v47
v_max3_f32 v20, v20, v56, v57
v_max3_f32 v20, v20, v58, v59
v_max3_f32 v20, v20, v60, v61
v_max3_f32 v20, v20, v62, v63
v_mov_b32 v21, v20
s_nop 1
v_permlane32_swap_b32 v20, v21
s_nop 1
v_max_f32 v20, v20, v21
v_mov_b32 v21, v20
s_nop 1
v_permlane16_swap_b32 v20, v21
s_nop 1
v_max_f32 v20, v20, v21
v_sub_f32 v32, v32, v20
v_sub_f32 v33, v33, v20
v_sub_f32 v34, v34, v20
v_sub_f32 v35, v35, v20
v_sub_f32 v40, v40, v20
v_sub_f32 v41, v41, v20
v_sub_f32 v42, v42, v20
v_sub_f32 v43, v43, v20
v_sub_f32 v44, v44, v20
v_sub_f32 v45, v45, v20
v_sub_f32 v46, v46, v20
v_sub_f32 v47, v47, v20
v_sub_f32 v56, v56, v20
v_sub_f32 v57, v57, v20
v_sub_f32 v58, v58, v20
v_sub_f32 v59, v59, v20
v_sub_f32 v60, v60, v20
v_sub_f32 v61, v61, v20
v_sub_f32 v62, v62, v20
v_sub_f32 v63, v63, v20
v_sub_f32 v21, 0, v20
v_exp_f32 v21, v21
s_nop 1
ar 0,0
v_mul_f32 v0, v0, v21
aw 0,0
ar 0,1
v_mul_f32 v0, v0, v21
aw 1,0
ar 0,2
v_mul_f32 v0, v0, v21
aw 2,0
ar 0,3
v_mul_f32 v0, v0, v21
aw 3,0
ar 0,4
v_mul_f32 v0, v0, v21
aw 4,0
ar 0,5
v_mul_f32 v0, v0, v21
aw 5,0
ar 0,6
v_mul_f32 v0, v0, v21
aw 6,0
ar 0,7
v_mul_f32 v0, v0, v21
aw 7,0
ar 0,16
v_mul_f32 v0, v0, v21
aw 16,0
ar 0,17
v_mul_f32 v0, v0, v21
aw 17,0
ar 0,18
v_mul_f32 v0, v0, v21
aw 18,0
ar 0,19
v_mul_f32 v0, v0, v21
aw 19,0
ar 0,20
v_mul_f32 v0, v0, v21
aw 20,0
ar 0,21
v_mul_f32 v0, v0, v21
aw 21,0
ar 0,22
v_mul_f32 v0, v0, v21
aw 22,0
ar 0,23
v_mul_f32 v0, v0, v21
aw 23,0
ar 0,32
v_mul_f32 v0, v0, v21
aw 32,0
ar 0,33
v_mul_f32 v0, v0, v21
aw 33,0
ar 0,34
v_mul_f32 v0, v0, v21
aw 34,0
ar 0,35
v_mul_f32 v0, v0, v21
aw 35,0
ar 0,36
v_mul_f32 v0, v0, v21
aw 36,0
ar 0,37
v_mul_f32 v0, v0, v21
aw 37,0
ar 0,38
v_mul_f32 v0, v0, v21
aw 38,0
ar 0,39
v_mul_f32 v0, v0, v21
aw 39,0
ar 0,48
v_mul_f32 v0, v0, v21
aw 48,0
ar 0,49
v_mul_f32 v0, v0, v21
aw 49,0
ar 0,50
v_mul_f32 v0, v0, v21
aw 50,0
ar 0,51
v_mul_f32 v0, v0, v21
aw 51,0
ar 0,52
v_mul_f32 v0, v0, v21
aw 52,0
ar 0,53
v_mul_f32 v0, v0, v21
aw 53,0
ar 0,54
v_mul_f32 v0, v0, v21
aw 54,0
ar 0,55
v_mul_f32 v0, v0, v21
aw 55,0
ar 0,64
v_mul_f32 v0, v0, v21
aw 64,0
ar 0,65
v_mul_f32 v0, v0, v21
aw 65,0
ar 0,66
v_mul_f32 v0, v0, v21
aw 66,0
ar 0,67
v_mul_f32 v0, v0, v21
aw 67,0
ar 0,68
v_mul_f32 v0, v0, v21
aw 68,0
ar 0,69
v_mul_f32 v0, v0, v21
aw 69,0
ar 0,70
v_mul_f32 v0, v0, v21
aw 70,0
ar 0,71
v_mul_f32 v0, v0, v21
aw 71,0
ar 0,80
v_mul_f32 v0, v0, v21
aw 80,0
ar 0,81
v_mul_f32 v0, v0, v21
aw 81,0
ar 0,82
v_mul_f32 v0, v0, v21
aw 82,0
ar 0,83
v_mul_f32 v0, v0, v21
aw 83,0
ar 0,84
v_mul_f32 v0, v0, v21
aw 84,0
ar 0,85
v_mul_f32 v0, v0, v21
aw 85,0
ar 0,86
v_mul_f32 v0, v0, v21
aw 86,0
ar 0,87
v_mul_f32 v0, v0, v21
aw 87,0
ar 0,244
v_mul_f32 v0, v0, v21
aw 244,0
ar 0,245
v_mul_f32 v0, v0, v21
aw 245,0
ar 0,246
v_mul_f32 v0, v0, v21
aw 246,0
ar 0,247
v_mul_f32 v0, v0, v21
aw 247,0
v_max3_f32 v20, v48, v49, 0
v_max3_f32 v20, v20, v50, v51
v_max3_f32 v20, v20, v52, v53
v_max3_f32 v20, v20, v54, v55
v_max3_f32 v20, v20, v64, v65
v_max3_f32 v20, v20, v66, v67
v_max3_f32 v20, v20, v68, v69
v_max3_f32 v20, v20, v70, v71
v_mov_b32 v21, v20
s_nop 1
v_permlane32_swap_b32 v20, v21
s_nop 1
v_max_f32 v20, v20, v21
v_mov_b32 v21, v20
s_nop 1
v_permlane16_swap_b32 v20, v21
s_nop 1
v_max_f32 v20, v20, v21
v_sub_f32 v36, v36, v20
v_sub_f32 v37, v37, v20
v_sub_f32 v38, v38, v20
v_sub_f32 v39, v39, v20
v_sub_f32 v48, v48, v20
v_sub_f32 v49, v49, v20
v_sub_f32 v50, v50, v20
v_sub_f32 v51, v51, v20
v_sub_f32 v52, v52, v20
v_sub_f32 v53, v53, v20
v_sub_f32 v54, v54, v20
v_sub_f32 v55, v55, v20
v_sub_f32 v64, v64, v20
v_sub_f32 v65, v65, v20
v_sub_f32 v66, v66, v20
v_sub_f32 v67, v67, v20
v_sub_f32 v68, v68, v20
v_sub_f32 v69, v69, v20
v_sub_f32 v70, v70, v20
v_sub_f32 v71, v71, v20
v_sub_f32 v21, 0, v20
v_exp_f32 v21, v21
s_nop 1
ar 0,8
v_mul_f32 v0, v0, v21
aw 8,0
ar 0,9
v_mul_f32 v0, v0, v21
aw 9,0
ar 0,10
v_mul_f32 v0, v0, v21
aw 10,0
ar 0,11
v_mul_f32 v0, v0, v21
aw 11,0
ar 0,12
v_mul_f32 v0, v0, v21
aw 12,0
ar 0,13
v_mul_f32 v0, v0, v21
aw 13,0
ar 0,14
v_mul_f32 v0, v0, v21
aw 14,0
ar 0,15
v_mul_f32 v0, v0, v21
aw 15,0
ar 0,24
v_mul_f32 v0, v0, v21
aw 24,0
ar 0,25
v_mul_f32 v0, v0, v21
aw 25,0
ar 0,26
v_mul_f32 v0, v0, v21
aw 26,0
ar 0,27
v_mul_f32 v0, v0, v21
aw 27,0
ar 0,28
v_mul_f32 v0, v0, v21
aw 28,0
ar 0,29
v_mul_f32 v0, v0, v21
aw 29,0
ar 0,30
v_mul_f32 v0, v0, v21
aw 30,0
ar 0,31
v_mul_f32 v0, v0, v21
aw 31,0
ar 0,40
v_mul_f32 v0, v0, v21
aw 40,0
ar 0,41
v_mul_f32 v0, v0, v21
aw 41,0
ar 0,42
v_mul_f32 v0, v0, v21
aw 42,0
ar 0,43
v_mul_f32 v0, v0, v21
aw 43,0
ar 0,44
v_mul_f32 v0, v0, v21
aw 44,0
ar 0,45
v_mul_f32 v0, v0, v21
aw 45,0
ar 0,46
v_mul_f32 v0, v0, v21
aw 46,0
ar 0,47
v_mul_f32 v0, v0, v21
aw 47,0
ar 0,56
v_mul_f32 v0, v0, v21
aw 56,0
ar 0,57
v_mul_f32 v0, v0, v21
aw 57,0
ar 0,58
v_mul_f32 v0, v0, v21
aw 58,0
ar 0,59
v_mul_f32 v0, v0, v21
aw 59,0
ar 0,60
v_mul_f32 v0, v0, v21
aw 60,0
ar 0,61
v_mul_f32 v0, v0, v21
aw 61,0
ar 0,62
v_mul_f32 v0, v0, v21
aw 62,0
ar 0,63
v_mul_f32 v0, v0, v21
aw 63,0
ar 0,72
v_mul_f32 v0, v0, v21
aw 72,0
ar 0,73
v_mul_f32 v0, v0, v21
aw 73,0
ar 0,74
v_mul_f32 v0, v0, v21
aw 74,0
ar 0,75
v_mul_f32 v0, v0, v21
aw 75,0
ar 0,76
v_mul_f32 v0, v0, v21
aw 76,0
ar 0,77
v_mul_f32 v0, v0, v21
aw 77,0
ar 0,78
v_mul_f32 v0, v0, v21
aw 78,0
ar 0,79
v_mul_f32 v0, v0, v21
aw 79,0
ar 0,88
v_mul_f32 v0, v0, v21
aw 88,0
ar 0,89
v_mul_f32 v0, v0, v21
aw 89,0
ar 0,90
v_mul_f32 v0, v0, v21
aw 90,0
ar 0,91
v_mul_f32 v0, v0, v21
aw 91,0
ar 0,92
v_mul_f32 v0, v0, v21
aw 92,0
ar 0,93
v_mul_f32 v0, v0, v21
aw 93,0
ar 0,94
v_mul_f32 v0, v0, v21
aw 94,0
ar 0,95
v_mul_f32 v0, v0, v21
aw 95,0
ar 0,248
v_mul_f32 v0, v0, v21
aw 248,0
ar 0,249
v_mul_f32 v0, v0, v21
aw 249,0
ar 0,250
v_mul_f32 v0, v0, v21
aw 250,0
ar 0,251
v_mul_f32 v0, v0, v21
aw 251,0
s_nop 7
s_setpc_b64 s[30:31]
Lend:
.purgem mv
.purgem mvz
.purgem mvc
.purgem ma
.purgem maz
.purgem tr
.purgem rd
.purgem wr
.purgem dl
.purgem dn
.purgem st
.purgem sta
.purgem cv
.purgem ar
.purgem aw

)"
    :: "s"(x), "s"(k), "s"(v), "s"(wq), "s"(wo), "s"(p), "s"(b), "s"(hg), "s"(w), "s"(ldsb), "s"(chunk)
    : "v0", "v1", "v2", "v3", "v4", "v5", "v6", "v7", "v8", "v9", "v10", "v11", "v12", "v13", "v14", "v15", "v16", "v17", "v18", "v19", "v20", "v21", "v22", "v23", "v24", "v25", "v26", "v27", "v28", "v29", "v30", "v31", "v32", "v33", "v34", "v35", "v36", "v37", "v38", "v39", "v40", "v41", "v42", "v43", "v44", "v45", "v46", "v47", "v48", "v49", "v50", "v51", "v52", "v53", "v54", "v55", "v56", "v57", "v58", "v59", "v60", "v61", "v62", "v63", "v64", "v65", "v66", "v67", "v68", "v69", "v70", "v71", "v72", "v73", "v74", "v75", "v76", "v77", "v78", "v79", "v80", "v81", "v82", "v83", "v84", "v85", "v86", "v87", "v88", "v89", "v90", "v91", "v92", "v93", "v94", "v95", "v96", "v97", "v98", "v99", "v100", "v101", "v102", "v103", "v104", "v105", "v106", "v107", "v108", "v109", "v110", "v111", "v112", "v113", "v114", "v115", "v116", "v117", "v118", "v119", "v120", "v121", "v122", "v123", "v124", "v125", "v126", "v127", "v128", "v129", "v130", "v131", "v132", "v133", "v134", "v135", "v136", "v137", "v138", "v139", "v140", "v141", "v142", "v143", "v144", "v145", "v146", "v147", "v148", "v149", "v150", "v151", "v152", "v153", "v154", "v155", "v156", "v157", "v158", "v159", "v160", "v161", "v162", "v163", "v164", "v165", "v166", "v167", "v168", "v169", "v170", "v171", "v172", "v173", "v174", "v175", "v176", "v177", "v178", "v179", "v180", "v181", "v182", "v183", "v184", "v185", "v186", "v187", "v188", "v189", "v190", "v191", "v192", "v193", "v194", "v195", "v196", "v197", "v198", "v199", "v200", "v201", "v202", "v203", "v204", "v205", "v206", "v207", "v208", "v209", "v210", "v211", "v212", "v213", "v214", "v215", "v216", "v217", "v218", "v219", "v220", "v221", "v222", "v223", "v224", "v225", "v226", "v227", "v228", "v229", "v230", "v231", "v232", "v233", "v234", "v235", "v236", "v237", "v238", "v239", "v240", "v241", "v242", "v243", "v244", "v245", "v246", "v247", "v248", "v249", "v250", "v251", "v252", "v253", "v254", "v255", "a0", "a1", "a2", "a3", "a4", "a5", "a6", "a7", "a8", "a9", "a10", "a11", "a12", "a13", "a14", "a15", "a16", "a17", "a18", "a19", "a20", "a21", "a22", "a23", "a24", "a25", "a26", "a27", "a28", "a29", "a30", "a31", "a32", "a33", "a34", "a35", "a36", "a37", "a38", "a39", "a40", "a41", "a42", "a43", "a44", "a45", "a46", "a47", "a48", "a49", "a50", "a51", "a52", "a53", "a54", "a55", "a56", "a57", "a58", "a59", "a60", "a61", "a62", "a63", "a64", "a65", "a66", "a67", "a68", "a69", "a70", "a71", "a72", "a73", "a74", "a75", "a76", "a77", "a78", "a79", "a80", "a81", "a82", "a83", "a84", "a85", "a86", "a87", "a88", "a89", "a90", "a91", "a92", "a93", "a94", "a95", "a96", "a97", "a98", "a99", "a100", "a101", "a102", "a103", "a104", "a105", "a106", "a107", "a108", "a109", "a110", "a111", "a112", "a113", "a114", "a115", "a116", "a117", "a118", "a119", "a120", "a121", "a122", "a123", "a124", "a125", "a126", "a127", "a128", "a129", "a130", "a131", "a132", "a133", "a134", "a135", "a136", "a137", "a138", "a139", "a140", "a141", "a142", "a143", "a144", "a145", "a146", "a147", "a148", "a149", "a150", "a151", "a152", "a153", "a154", "a155", "a156", "a157", "a158", "a159", "a160", "a161", "a162", "a163", "a164", "a165", "a166", "a167", "a168", "a169", "a170", "a171", "a172", "a173", "a174", "a175", "a176", "a177", "a178", "a179", "a180", "a181", "a182", "a183", "a184", "a185", "a186", "a187", "a188", "a189", "a190", "a191", "a192", "a193", "a194", "a195", "a196", "a197", "a198", "a199", "a200", "a201", "a202", "a203", "a204", "a205", "a206", "a207", "a208", "a209", "a210", "a211", "a212", "a213", "a214", "a215", "a216", "a217", "a218", "a219", "a220", "a221", "a222", "a223", "a224", "a225", "a226", "a227", "a228", "a229", "a230", "a231", "a232", "a233", "a234", "a235", "a236", "a237", "a238", "a239", "a240", "a241", "a242", "a243", "a244", "a245", "a246", "a247", "a248", "a249", "a250", "a251", "a252", "a253", "a254", "a255", "s35", "s36", "s37", "s38", "s39", "s40", "s41", "s42", "s43", "s44", "s45", "s46", "s47", "s48", "s49", "s50", "s51", "s52", "s53", "s54", "s55", "s56", "s57", "s58", "s59", "s60", "s61", "s62", "s63", "s64", "s65", "s66", "s67", "s68", "s69", "s70", "s71", "s72", "s73", "s74", "s75", "s76", "s77", "s78", "s79", "s80", "s81", "s82", "s83", "s84", "s85", "s86", "s87", "s88", "s89", "s90", "s91", "s92", "s93", "s94", "s95", "s96", "s97", "s98", "s99", "vcc", "scc", "memory");
}

__device__ __forceinline__ unsigned cvtpk(float lo, float hi) { f32x2 v = {lo, hi}; bf16x2 b = __builtin_convertvector(v, bf16x2); return __builtin_bit_cast(unsigned, b); }

constexpr int RIT = 1;
__global__ __launch_bounds__(256)
void reduce_kernel(const u32x4* __restrict__ part, unsigned short* __restrict__ out) {
  constexpr unsigned BPG = 10240u / (256u * RIT);
  const unsigned bb = blockIdx.x / BPG, base = (blockIdx.x % BPG) * (256u * RIT) + threadIdx.x;
  u32x4 v[RIT][8];
  #pragma unroll
  for (int it = 0; it < RIT; ++it) {
    const unsigned rem = base + 256u * it;
    #pragma unroll
    for (int g = 0; g < 8; ++g) v[it][g] = part[(size_t)(bb * 8u + g) * 10240u + rem];
  }
  #pragma unroll
  for (int it = 0; it < RIT; ++it) {
    const unsigned rem = base + 256u * it;
    const unsigned w = rem / 2560u, u = (rem % 2560u) >> 6, l = rem & 63u;
    const unsigned J = u >> 2, np = (u >> 1) & 1u, qb = u & 1u;
    float s[8] = {0, 0, 0, 0, 0, 0, 0, 0};
    #pragma unroll
    for (int g = 0; g < 8; ++g) {
      #pragma unroll
      for (int e = 0; e < 4; ++e) { s[2 * e] += __uint_as_float(v[it][g][e] << 16); s[2 * e + 1] += __uint_as_float(v[it][g][e] & 0xffff0000u); }
    }
    const unsigned br = bb >> 4, b = bb & 15u, q = 32u * w + 16u * qb + (l & 15u), g4 = l >> 4;
    unsigned short* o = out + ((size_t)(b * 128u + q) * 1280u + br * 640u + 64u * J + 32u * np + 4u * g4);
    *(u32x2*)o = u32x2{cvtpk(s[0], s[1]), cvtpk(s[2], s[3])};
    *(u32x2*)(o + 16) = u32x2{cvtpk(s[4], s[5]), cvtpk(s[6], s[7])};
  }
}

extern "C" void kernel_launch(void* const* d_in, const int* in_sizes, int n_in, void* d_out, int out_size, void* d_ws, size_t ws_size, hipStream_t stream) {
  (void)hipFuncSetAttribute((const void*)fused_kernel, hipFuncAttributeMaxDynamicSharedMemorySize, LDS_BYTES);
  fused_kernel<<<256, 256, LDS_BYTES, stream>>>(d_in[0], d_in[1], d_in[2], d_in[3], d_in[4], d_in[5], d_in[6], d_in[7], d_in[8], d_in[9], (char*)d_ws);
  reduce_kernel<<<1280 / RIT, 256, 0, stream>>>((const u32x4*)d_ws, (unsigned short*)d_out);
}
```
